```python
import torch

def _softmax(x, axis=-1):
    return torch.softmax(x, dim=axis)
def _gelu(x):
    return torch.nn.functional.gelu(x, approximate="tanh")
def _silu(x):
    return torch.nn.functional.silu(x)
def _sigmoid(x):
    return torch.sigmoid(x)
def _layernorm(x, axis=-1, eps=1e-5):
    m = x.mean(dim=axis, keepdim=True)
    v = x.var(dim=axis, keepdim=True, unbiased=False)
    return (x - m) / torch.sqrt(v + eps)
def _scatter_add(tgt, idx, src, axis=0):
    return tgt.clone().index_add_(axis, idx, src)
def _segment_sum(src, seg, n):
    out = torch.zeros((n,) + src.shape[1:], dtype=src.dtype)
    return out.index_add_(0, seg, src)
def _pad(x, pw):
    flat = tuple(v for pair in reversed(pw) for v in pair)
    return torch.nn.functional.pad(x, flat)

kernel_name = 'sequential_preLN__sliding_block__mixer_s_mlp__prompt'

def setup_inputs():

    _w = (lambda a: (lambda t: t.to(torch.bfloat16) if t.is_floating_point() else t)(torch.as_tensor(a)))
    def _A_setup():
        B, S, D = 1, 2048, 448
        return {
            "x": _w((lambda *s: torch.randn(*s, dtype=torch.bfloat16) * 0.05)(B, S, D)),
            "wqkv": _w((lambda *s: torch.randn(*s, dtype=torch.bfloat16) * 0.05)(D, 3 * D)),
            "wo": _w((lambda *s: torch.randn(*s, dtype=torch.bfloat16) * 0.05)(D, D)),
        }
    _A_in = _A_setup()

    def _B_setup():
        B, S, D, F = 1, 2048, 448, 1792
        return {
            "x":  _w((lambda *s: torch.randn(*s, dtype=torch.bfloat16) * 0.05)(B, S, D)),
            "w1": _w((lambda *s: torch.randn(*s, dtype=torch.bfloat16) * 3.2)(D, D) * (D ** -0.5)),
            "w2": _w((lambda *s: torch.randn(*s, dtype=torch.bfloat16) * 3.2)(D, D) * (D ** -0.5)),
            "w3": _w((lambda *s: torch.randn(*s, dtype=torch.bfloat16) * 3.2)(D, F) * (D ** -0.5)),
            "w4": _w((lambda *s: torch.randn(*s, dtype=torch.bfloat16) * 3.2)(F, D) * (F ** -0.5)),
        }
    _B_in = _B_setup()

    return {
        "A_x": _A_in["x"],
        "A_wqkv": _A_in["wqkv"],
        "A_wo": _A_in["wo"],
        "B_x": _B_in["x"],
        "B_w1": _B_in["w1"],
        "B_w2": _B_in["w2"],
        "B_w3": _B_in["w3"],
        "B_w4": _B_in["w4"],
    }

def reference(A_x, A_wqkv, A_wo, B_x, B_w1, B_w2, B_w3, B_w4):
    def _A_ref(x, wqkv, wo):
        B, S, D = x.shape
        H = 16; Dh = D // H; W = 256
        qkv = (lambda x, *d: x.permute(*d))((x @ wqkv).reshape(B, S, 3, H, Dh), 2, 0, 3, 1, 4)
        q, k, v = qkv[0], qkv[1], qkv[2]
        scores = torch.einsum("bhqd,bhkd->bhqk", q, k) * (Dh ** -0.5)
        idx = torch.arange(S)
        mask = (idx[:, None] - idx[None, :] >= 0) & (idx[:, None] - idx[None, :] < W)
        scores = torch.where(mask[None, None], scores, -1e9)
        p = _softmax(scores, axis=-1)
        o = torch.einsum("bhqk,bhkd->bhqd", p, v)
        return (lambda x, *d: x.permute(*d))(o, 0, 2, 1, 3).reshape(B, S, D) @ wo

    def _B_ref(x, w1, w2, w3, w4):
        B, S, D = x.shape
        h = _gelu(_layernorm(x, axis=-1) @ w1)
        c = D // 4
        g0 = _pad(h[:, :, 0*c:1*c], ((0, 0), (1, 0), (0, 0)))[:, :S, :]
        g1 = _pad(h[:, :, 1*c:2*c], ((0, 0), (0, 1), (0, 0)))[:, 1:, :]
        g2 = _pad(h[:, :, 2*c:3*c], ((0, 0), (2, 0), (0, 0)))[:, :S, :]
        g3 = _pad(h[:, :, 3*c:   ], ((0, 0), (0, 2), (0, 0)))[:, 2:, :]
        hs = torch.concatenate([g0, g1, g2, g3], axis=-1) @ w2
        x1 = x + hs
        h2 = _gelu(_layernorm(x1, axis=-1) @ w3) @ w4
        return x1 + h2

    x = A_x
    h = x + _A_ref(_layernorm(x, axis=-1), A_wqkv, A_wo)
    h2 = h + _B_ref(_layernorm(h, axis=-1), B_w1, B_w2, B_w3, B_w4)
    return h2
```

```cpp
#include <hip/hip_runtime.h>
#include <hip/hip_bf16.h>
#include <stdint.h>

typedef unsigned short u16;
__device__ inline float bf2f(u16 v){ return __uint_as_float(((unsigned)v)<<16); }
__device__ inline u16 f2bf(float f){ unsigned u=__float_as_uint(f); u += 0x7fffu + ((u>>16)&1u); return (u16)(u>>16); }

constexpr int S=2048, D=448, H=16, DH=28, W=256, F=1792;

__global__ void ln_kernel(const u16* in, u16* out, int twice){
  int r = blockIdx.x; int t = threadIdx.x;
  float v[7];
  for(int i=0;i<7;i++) v[i]=bf2f(in[r*D+t+64*i]);
  for(int pass=0; pass<=twice; pass++){
    float s=0; for(int i=0;i<7;i++) s+=v[i];
    for(int m=32;m>=1;m>>=1) s+=__shfl_xor(s,m);
    float mean=s/D; float q=0; for(int i=0;i<7;i++){ float d=v[i]-mean; q+=d*d; }
    for(int m=32;m>=1;m>>=1) q+=__shfl_xor(q,m);
    float rs=1.0f/sqrtf(q/D+1e-5f);
    for(int i=0;i<7;i++) v[i]=bf2f(f2bf((v[i]-mean)*rs));
  }
  for(int i=0;i<7;i++) out[r*D+t+64*i]=f2bf(v[i]);
}
__global__ void gemm_kernel(const u16* A, const u16* B, u16* C, int M,int N,int K,int act,const u16* res1,const u16* res2,int shift){
  int n = blockIdx.x*64+threadIdx.x; int m = blockIdx.y;
  if(n>=N) return;
  float acc=0;
  for(int k=0;k<K;k++){
    int mm=m;
    if(shift){ int g=k/112; mm = m + (g==0?-1: g==1?1: g==2?-2:2); }
    float a = (mm>=0 && mm<M) ? bf2f(A[(size_t)mm*K+k]) : 0.f;
    acc += a*bf2f(B[(size_t)k*N+n]);
  }
  float v = bf2f(f2bf(acc));
  if(act){ float u=0.7978845608028654f*(v+0.044715f*v*v*v); v = 0.5f*v*(1.f+tanhf(u)); v=bf2f(f2bf(v)); }
  if(res1){ v = bf2f(f2bf(v+bf2f(res1[(size_t)m*N+n]))); }
  if(res2){ v = bf2f(f2bf(v+bf2f(res2[(size_t)m*N+n]))); }
  C[(size_t)m*N+n]=f2bf(v);
}
__global__ void attn_kernel(const u16* qkv, u16* o){
  int t=blockIdx.x, h=blockIdx.y, l=threadIdx.x;
  __shared__ float p[256];
  float q[DH]; for(int d=0;d<DH;d++) q[d]=bf2f(qkv[(size_t)t*3*D + h*DH + d]);
  float mx=-1e30f;
  for(int j=l;j<W;j+=64){ int kt=t-j; float s=-1e30f; if(kt>=0){ s=0; for(int d=0;d<DH;d++) s+=q[d]*bf2f(qkv[(size_t)kt*3*D + D + h*DH + d]); s*=0.18898223650461363f; } p[j]=s; mx=fmaxf(mx,s);}
  for(int m=32;m>=1;m>>=1) mx=fmaxf(mx,__shfl_xor(mx,m));
  float sum=0; for(int j=l;j<W;j+=64){ float e = (t-j>=0)? __expf(p[j]-mx):0.f; p[j]=e; sum+=e; }
  for(int m=32;m>=1;m>>=1) sum+=__shfl_xor(sum,m);
  __syncthreads();
  if(l<DH){ float acc=0; for(int j=0;j<W;j++){ int kt=t-j; if(kt>=0) acc+=p[j]*bf2f(qkv[(size_t)kt*3*D + 2*D + h*DH + l]); } o[(size_t)t*D + h*DH + l]=f2bf(acc/sum); }
}

extern "C" void kernel_launch(void* const* d_in, const int* in_sizes, int n_in, void* d_out, int out_size, void* d_ws, size_t ws_size, hipStream_t stream){
  const u16* x=(const u16*)d_in[0]; const u16* wqkv=(const u16*)d_in[1]; const u16* wo=(const u16*)d_in[2];
  const u16* w1=(const u16*)d_in[4]; const u16* w2=(const u16*)d_in[5]; const u16* w3=(const u16*)d_in[6]; const u16* w4=(const u16*)d_in[7];
  u16* ws=(u16*)d_ws; size_t n=(size_t)S*D;
  u16* xn=ws; u16* qkv=ws+n; u16* o=ws+4*n; u16* h=ws+5*n; u16* y=ws+6*n; u16* yy=ws+7*n; u16* z=ws+8*n; u16* x1=ws+9*n; u16* x1n=ws+10*n; u16* hid=ws+11*n; u16* t2=ws+16*n;
  ln_kernel<<<S,64,0,stream>>>(x,xn,0);
  gemm_kernel<<<dim3(21,S),64,0,stream>>>(xn,wqkv,qkv,S,3*D,D,0,nullptr,nullptr,0);
  attn_kernel<<<dim3(S,H),64,0,stream>>>(qkv,o);
  gemm_kernel<<<dim3(7,S),64,0,stream>>>(o,wo,h,S,D,D,0,x,nullptr,0);
  ln_kernel<<<S,64,0,stream>>>(h,y,0);
  ln_kernel<<<S,64,0,stream>>>(y,yy,0);
  gemm_kernel<<<dim3(7,S),64,0,stream>>>(yy,w1,z,S,D,D,1,nullptr,nullptr,0);
  gemm_kernel<<<dim3(7,S),64,0,stream>>>(z,w2,x1,S,D,D,0,y,nullptr,1);
  ln_kernel<<<S,64,0,stream>>>(x1,x1n,0);
  gemm_kernel<<<dim3(28,S),64,0,stream>>>(x1n,w3,hid,S,F,D,1,nullptr,nullptr,0);
  gemm_kernel<<<dim3(7,S),64,0,stream>>>(hid,w4,(u16*)d_out,S,D,F,0,x1,h,0);
}
```

```cpp
#include <hip/hip_runtime.h>
#include <hip/hip_bf16.h>
#include <stdint.h>

typedef unsigned short u16;
typedef __attribute__((ext_vector_type(8))) __bf16 bf16x8;
typedef __attribute__((ext_vector_type(2))) __bf16 bf16x2;
typedef __attribute__((ext_vector_type(4))) short s16x4;
typedef __attribute__((ext_vector_type(4))) float f32x4;
typedef __attribute__((ext_vector_type(16))) float f32x16;
typedef __attribute__((ext_vector_type(4))) unsigned u32x4;
typedef __attribute__((ext_vector_type(2))) unsigned u32x2;
#define LAS __attribute__((address_space(3)))
#define DI __device__ __forceinline__

constexpr int S = 2048, D = 448, NH = 16, DH = 28, F = 1792;
constexpr unsigned SENT = 0xFFFFFFFFu;

constexpr unsigned KB = 1024;
constexpr unsigned WS_Q   = 0;
constexpr unsigned WS_K   = WS_Q + 2048 * KB;
constexpr unsigned WS_V   = WS_K + 2048 * KB;
constexpr unsigned WS_WO  = WS_V + 2048 * KB;
constexpr unsigned WS_W1  = WS_WO + 448 * KB;
constexpr unsigned WS_W2  = WS_W1 + 392 * KB;
constexpr unsigned WS_W3  = WS_W2 + 392 * KB;
constexpr unsigned WS_W4  = WS_W3 + 1568 * KB;
constexpr unsigned WS_O   = WS_W4 + 1568 * KB;
constexpr unsigned WS_H   = WS_O + 2048 * KB;
constexpr unsigned WS_X1  = WS_H + 1792 * KB;
constexpr unsigned WS_Z   = WS_X1 + 1792 * KB;
constexpr unsigned WS_P   = WS_Z + 1820 * KB;
constexpr unsigned WS_END = WS_P + 32 * 49 * 8 * KB;

struct Params {
  const u16* x; const u16* wqkv; const u16* wo; const u16* w1; const u16* w2; const u16* w3; const u16* w4;
  u16* out; char* ws;
};

DI __amdgpu_buffer_rsrc_t mk_rsrc(const void* p, unsigned bytes) { return __builtin_amdgcn_make_buffer_rsrc((void*)p, (short)0, (int)bytes, 0x00020000); }
DI void dma16(__amdgpu_buffer_rsrc_t rs, unsigned voff, unsigned soff, unsigned ldsaddr) {
  asm volatile("s_mov_b32 m0, %0\n\ts_nop 0\n\tbuffer_load_dwordx4 %1, %2, %3 offen lds" :: "s"(ldsaddr), "v"(voff), "s"(rs), "s"(soff) : "memory");
}
DI void dma16_sc1(__amdgpu_buffer_rsrc_t rs, unsigned voff, unsigned soff, unsigned ldsaddr) {
  asm volatile("s_mov_b32 m0, %0\n\ts_nop 0\n\tbuffer_load_dwordx4 %1, %2, %3 offen sc1 lds" :: "s"(ldsaddr), "v"(voff), "s"(rs), "s"(soff) : "memory");
}
#define WAIT_VM(n) asm volatile("s_waitcnt vmcnt(" #n ")" ::: "memory")
#define BAR() asm volatile("s_waitcnt lgkmcnt(0)\n\ts_barrier" ::: "memory")
#define BARS() do { __builtin_amdgcn_sched_barrier(0); BAR(); __builtin_amdgcn_sched_barrier(0); } while (0)
DI unsigned rfl(unsigned v) { return __builtin_amdgcn_readfirstlane(v); }
DI float bflo(unsigned u) { return __uint_as_float(u << 16); }
DI float bfhi(unsigned u) { return __uint_as_float(u & 0xffff0000u); }
DI unsigned pk2(float a, float b) { bf16x2 v; v[0] = (__bf16)a; v[1] = (__bf16)b; return __builtin_bit_cast(unsigned, v); }
DI float dot2(unsigned a, unsigned b, float c) { return __builtin_amdgcn_fdot2_f32_bf16(__builtin_bit_cast(bf16x2, a), __builtin_bit_cast(bf16x2, b), c, false); }
DI f32x16 mfma32(u32x4 a, u32x4 b, f32x16 c) { return __builtin_amdgcn_mfma_f32_32x32x16_bf16(__builtin_bit_cast(bf16x8, a), __builtin_bit_cast(bf16x8, b), c, 0, 0, 0); }
DI f32x4 mfma16(u32x4 a, u32x4 b, f32x4 c) { return __builtin_amdgcn_mfma_f32_16x16x32_bf16(__builtin_bit_cast(bf16x8, a), __builtin_bit_cast(bf16x8, b), c, 0, 0, 0); }
DI u32x2 tr_read(const LAS char* p) { return __builtin_bit_cast(u32x2, __builtin_amdgcn_ds_read_tr16_b64_v4i16((LAS s16x4*)p)); }
DI u32x4 lds128(const LAS char* p) { return *(const LAS u32x4*)p; }
#define XJ j
constexpr unsigned ONES = 0x3F803F80u;
#define ST_DECL
#define CSTAMP(n)
#define STAMP(n)
#define ST_FLUSH(kern, ws, b, w)

DI void map_block(int b, int& i, int& j) { const int s = b >> 3; i = (b & 7) * 4 + (s >> 3); j = s & 7; }

constexpr unsigned K1_XS = 0;
constexpr unsigned K1_W0 = 7 * 8192;
constexpr unsigned K1_SLOT = 24576;
constexpr unsigned K1_ST = K1_W0 + 4 * K1_SLOT;
constexpr unsigned K1_CS = K1_ST + 512;
constexpr unsigned K1_LDS = K1_CS + 2 * 2 * 3 * 32 * 4;

struct Retile { const u16* src; unsigned ldw, dst; int kind; };
DI void retile_load(const Params& p, int n, int lane, u32x4 (&v)[4], unsigned& dst, int& kind) {
  const u16* W; unsigned ldw, krow0, cb;
  if (n < 112)      { W = p.wo; ldw = D; const int ks = n / 7; cb = n % 7; krow0 = 28 * ks; kind = 1; dst = WS_WO + (4 * cb * 16 + ks) * KB; }
  else if (n < 308) { const int m = n < 210 ? n - 112 : n - 210; W = n < 210 ? p.w1 : p.w2; ldw = D; const int ks = m / 7; cb = m % 7; krow0 = 32 * ks; kind = 0;
                      dst = (n < 210 ? WS_W1 : WS_W2) + (4 * cb * 14 + ks) * KB; }
  else if (n < 700) { const int m = n - 308; W = p.w3; ldw = F; const int ks = m / 28; cb = m % 28; krow0 = 32 * ks; kind = 0; dst = WS_W3 + (4 * cb * 14 + ks) * KB; }
  else              { const int m = n - 700; W = p.w4; ldw = D; const int ks = m / 7; cb = m % 7; krow0 = 32 * ks; kind = 2;
                      dst = WS_W4 + ((((ks >> 3) * 7 + cb) * 4) * 8 + (ks & 7)) * KB; }
  const unsigned nrows = (kind == 2) ? F : D;
#pragma unroll
  for (int q = 0; q < 4; ++q) {
    unsigned row = krow0 + (lane >> 3) + 8 * q; row = row < nrows ? row : nrows - 1;
    v[q] = *(const u32x4*)(W + (size_t)row * ldw + cb * 64 + (lane & 7) * 8);
  }
}
DI void retile_store(const Params& p, LAS char* scr, int lane, const u32x4 (&v)[4], unsigned dst, int kind) {
#pragma unroll
  for (int q = 0; q < 4; ++q) *(LAS u32x4*)(scr + q * 1024 + lane * 16) = v[q];
  const unsigned fstride = (kind == 1 ? 16u : kind == 2 ? 8u : 14u) * KB;
  const int g = lane >> 4, q4 = (lane & 15) >> 2, pp = lane & 3;
  const int r0 = (kind == 2 ? 4 * g : 8 * g) + q4, r1 = (kind == 2 ? 16 + 4 * g : 8 * g + 4) + q4;
  char* out = p.ws + dst + lane * 16;
#pragma unroll
  for (int ft = 0; ft < 4; ++ft) {
    u32x2 lo = tr_read(scr + r0 * 128 + ft * 32 + pp * 8), hi = tr_read(scr + r1 * 128 + ft * 32 + pp * 8);
    if (kind == 1 && g == 3) { hi[0] = 0u; hi[1] = 0u; }
    u32x4 f = {lo[0], lo[1], hi[0], hi[1]};
    *(u32x4*)(out + ft * fstride) = f;
  }
}

__global__ void __launch_bounds__(512, 2) k1_qkv(Params p) {
  extern __shared__ __attribute__((aligned(16))) char smem[];
  LAS char* lds = (LAS char*)smem;
  const unsigned lds0 = (unsigned)(size_t)lds;
  const int b = blockIdx.x, tid = threadIdx.x, lane = tid & 63;
  const int w = rfl(tid >> 6);
  int i, j; map_block(b, i, j);
  ST_DECL; STAMP(0);

  if (j == 7) {
    const int gw = i * 8 + w;
    u32x4 v[5][4]; unsigned dst[5]; int kind[5];
#pragma unroll
    for (int q = 0; q < 5; ++q) if (gw + 256 * q < 1092) retile_load(p, gw + 256 * q, lane, v[q], dst[q], kind[q]);
    const u32x4 sv = {SENT, SENT, SENT, SENT}, zv = {0u, 0u, 0u, 0u};
    {
      const int pr = gw * 4 + (lane >> 4), gq = (lane >> 2) & 3, row = lane & 3;
      const unsigned o = row == 0 ? ONES : 0u;
      *(u32x4*)(p.ws + WS_V + ((size_t)(pr * 2 + 1) * 64 + gq * 16 + 12 + row) * 16) = u32x4{o, o, o, o};
    }
#pragma unroll
    for (int q = 0; q < 4; ++q) { const int pc = (gw * 4 + q) * 64 + lane;
      *(u32x2*)(p.ws + WS_Q + ((size_t)(pc >> 4) * 64 + 48 + (pc & 15)) * 16 + 8) = u32x2{0u, 0u}; }
    if (gw < 28) *(u32x4*)(p.ws + WS_Z + (size_t)(gw < 14 ? gw : 129 * 14 + gw - 14) * KB + lane * 16) = zv;
    {
      if (gw < 224) { const int pc = gw * 64 + lane, tok = pc & 1, gq = (pc >> 1) & 3, rest = pc >> 3, ks = rest % 14, ce = rest / 14, edge = ce & 1, cl = ce >> 1;
        const int tt = 2 * cl + edge + 1;
        *(u32x4*)(p.ws + WS_Z + ((size_t)(tt * 14 + ks) * 64 + gq * 16 + (edge ? 14 : 0) + tok) * 16) = sv; }
    }
#pragma unroll
    for (int q = 0; q < 5; ++q) if (gw + 256 * q < 1092) retile_store(p, lds + w * 4096, lane, v[q], dst[q], kind[q]);
    STAMP(7); ST_FLUSH(0, p.ws, b, w);
    return;
  }

  if (w >= 6) {
    {
      const u32x4 sv = {SENT, SENT, SENT, SENT};
      for (int kb = (i * 7 + j) * 2 + (w - 6); kb < 2048; kb += 448) *(u32x4*)(p.ws + WS_O + (size_t)kb * KB + lane * 16) = sv; }
    const int tok = 32 * (w - 6) + (lane & 31), part = lane >> 5;
    const LAS char* xb[4];
#pragma unroll
    for (int q = 0; q < 4; ++q) xb[q] = lds + K1_XS + tok * 128 + ((4 * part + q) ^ ((tok >> 1) & 7)) * 16;
    float s1a = 0.f, s1b = 0.f, s2a = 0.f, s2b = 0.f;
    BARS();
#pragma unroll
    for (int c = 0; c < 7; ++c) {
      u32x4 v[4];
#pragma unroll
      for (int q = 0; q < 4; ++q) v[q] = lds128(xb[q] + c * 8192);
      __builtin_amdgcn_sched_barrier(0);
#pragma unroll
      for (int q = 0; q < 4; ++q)
#pragma unroll
        for (int u = 0; u < 4; ++u) { float lo = bflo(v[q][u]), hi = bfhi(v[q][u]); asm volatile("" : "+v"(lo), "+v"(hi));
          s1a += lo; s1b += hi; s2a = fmaf(lo, lo, s2a); s2b = fmaf(hi, hi, s2b); }
      asm volatile("" : "+v"(s1a), "+v"(s1b), "+v"(s2a), "+v"(s2b));
      if (c == 6) {
        float a1 = s1a + s1b, a2 = s2a + s2b;
        a1 += __shfl_xor(a1, 32); a2 += __shfl_xor(a2, 32);
        const float mean = a1 * (1.f / D), var = fmaxf(a2 * (1.f / D) - mean * mean, 0.f);
        if (lane < 32) { *(LAS float*)(lds + K1_ST + tok * 4) = mean; *(LAS float*)(lds + K1_ST + 256 + tok * 4) = __builtin_amdgcn_rsqf(var + 1e-5f); }
      }
      BARS();
    }
    STAMP(7); ST_FLUSH(0, p.ws, b, w);
    return;
  }
  if (w >= 4) {
    const int lw = w - 4;
    const __amdgpu_buffer_rsrc_t rx = mk_rsrc(p.x, S * D * 2), rw = mk_rsrc(p.wqkv, D * 3 * D * 2);
    const unsigned xv0 = (lane >> 3) * 896 + ((lane & 7) ^ (lane >> 4)) * 16, xv1 = (lane >> 3) * 896 + ((lane & 7) ^ (lane >> 4) ^ 4) * 16;
    const unsigned wv = (lane >> 3) * 2688 + (((lane & 7) ^ (((lane >> 4) & 1) * 4)) * 16);
    auto issue_chunk = [&](int c) {
#pragma unroll
      for (int q = 0; q < 4; ++q) { const unsigned n = lw * 4 + q; dma16(rx, (q & 1) ? xv1 : xv0, (64 * i + 8 * n) * 896 + c * 128, lds0 + K1_XS + c * 8192 + n * 1024); }
#pragma unroll
      for (int q = 0; q < 12; ++q) { const unsigned n = lw * 12 + q, type = n >> 3, rg = n & 7;
        dma16(rw, wv, (64 * c + 8 * rg) * 2688 + type * 896 + j * 128, lds0 + K1_W0 + (c & 3) * K1_SLOT + type * 8192 + rg * 1024); }
    };
    issue_chunk(0); issue_chunk(1);
    STAMP(1);
    WAIT_VM(16); STAMP(2); BARS();
    issue_chunk(2); issue_chunk(3);
    WAIT_VM(32); STAMP(3); BARS();
    issue_chunk(4);
    WAIT_VM(32); STAMP(8); BARS();
    issue_chunk(5);
    WAIT_VM(32); STAMP(9); BARS();
    issue_chunk(6);
    WAIT_VM(32); STAMP(10); BARS();
    WAIT_VM(16); STAMP(11); BARS();
    WAIT_VM(0); STAMP(5); BARS();
    BAR();
    STAMP(7); ST_FLUSH(0, p.ws, b, w);
    return;
  }
  const int tb = w & 1, e = w >> 1, r = lane & 31, h = lane >> 5;
  f32x16 aq = {}, ak = {}, av = {};
  f32x4 cq = {}, ck = {}, cv = {};
  const unsigned cs1 = ((lane & 15) == ((lane >> 4) & 1)) ? ONES : 0u;
  const u32x4 csel = {cs1, cs1, cs1, cs1};
  const int q4 = (lane & 15) >> 2, pp = lane & 3, blk = (lane >> 4) & 1;
  const LAS char* xb[4];
#pragma unroll
  for (int kl = 0; kl < 4; ++kl) xb[kl] = lds + K1_XS + (32 * tb + r) * 128 + ((2 * (kl ^ tb) + h) ^ ((r >> 1) & 7)) * 16;
  const unsigned wb = K1_W0 + (8 * h + q4) * 128 + (((8 * e + 4 * blk + pp) ^ (8 * ((q4 >> 1) & 1))) * 8);
  const LAS char* we = lds + wb + tb * 2048;
  const LAS char* wo = lds + wb + (1 - tb) * 2048;
  u32x4 xf[3], f[3][3];
  auto load_step = [&](int n) {
    xf[n % 3] = lds128(xb[n & 3] + (n >> 2) * 8192);
    const LAS char* wp = ((n & 1) ? wo : we) + ((n >> 2) & 3) * K1_SLOT + (n & 2) * 2048;
#pragma unroll
    for (int t = 0; t < 3; ++t) { const u32x2 lo = tr_read(wp + t * 8192), hi = tr_read(wp + t * 8192 + 512); f[n % 3][t] = u32x4{lo[0], lo[1], hi[0], hi[1]}; }
  };
  const __amdgpu_buffer_rsrc_t rws = mk_rsrc(p.ws, WS_END);
  unsigned qoff[4], voff;
  { const unsigned tt = 4 * i + 2 * tb + (r >> 4);
#pragma unroll
    for (int g4 = 0; g4 < 4; ++g4) { const unsigned n0 = 64 * j + 32 * e + 8 * g4 + 4 * h, head = n0 / 28, d0 = n0 - 28 * head;
      qoff[g4] = WS_Q + ((head * 128 + tt) * 64 + (d0 >> 3) * 16 + (r & 15)) * 16 + (d0 & 4) * 2;
      asm volatile("" : "+v"(qoff[g4])); }
    const unsigned n = 64 * j + 32 * e + r, head = n / 28, d = n - 28 * head;
    voff = WS_V + (((head * 64 + 2 * i + tb) * 2 + (d >> 4)) * 64 + h * 16 + (d & 15)) * 16; asm volatile("" : "+v"(voff)); }
  STAMP(1);
  BARS();
  STAMP(2);
  load_step(0);
#pragma unroll
  for (int n = 0; n < 28; ++n) {
    if ((n & 3) == 3 && n < 27) { if (n == 7) STAMP(8); if (n == 11) STAMP(10); BARS(); if (n == 7) { STAMP(9); CSTAMP(13); } if (n == 11) { STAMP(11); CSTAMP(14); } if (n == 15) STAMP(12); }
    if (n < 27) load_step(n + 1);
    aq = mfma32(f[n % 3][0], xf[n % 3], aq);
    ak = mfma32(f[n % 3][1], xf[n % 3], ak);
    av = mfma32(xf[n % 3], f[n % 3][2], av);
    if (!(n & 1)) {
      cq = mfma16(f[n % 3][0], csel, cq); ck = mfma16(f[n % 3][1], csel, ck); cv = mfma16(f[n % 3][2], csel, cv);
    }
    __builtin_amdgcn_sched_group_barrier(0x8, 1, 0); __builtin_amdgcn_sched_group_barrier(0x100, 3, 0);
    __builtin_amdgcn_sched_group_barrier(0x8, 1, 0); __builtin_amdgcn_sched_group_barrier(0x100, 2, 0);
    __builtin_amdgcn_sched_group_barrier(0x8, 1, 0); __builtin_amdgcn_sched_group_barrier(0x100, 2, 0);
    __builtin_amdgcn_sched_group_barrier(0x8, 3, 0);
    __builtin_amdgcn_sched_barrier(0);
  }
  STAMP(4);
  LAS float* cs = (LAS float*)(lds + K1_CS) + e * 192;
  if ((lane & 15) < 2) { LAS float* o = cs + tb * 96 + 16 * (lane & 1) + 4 * (lane >> 4);
    *(LAS f32x4*)o = cq; *(LAS f32x4*)(o + 32) = ck; *(LAS f32x4*)(o + 64) = cv; }
  BAR();
  STAMP(5);
  const LAS float* st = (const LAS float*)(lds + K1_ST);
  f32x4 cqk[2][4][2], m4[4], r4[4];
#pragma unroll
  for (int t = 0; t < 2; ++t)
#pragma unroll
    for (int g4 = 0; g4 < 4; ++g4) { cqk[t][g4][0] = *(const LAS f32x4*)(cs + t * 32 + 8 * g4 + 4 * h); cqk[t][g4][1] = *(const LAS f32x4*)(cs + 96 + t * 32 + 8 * g4 + 4 * h); }
  const float mean = st[32 * tb + r], rstd = st[64 + 32 * tb + r];
  const float cs2 = cs[64 + r] + cs[96 + 64 + r];
#pragma unroll
  for (int g4 = 0; g4 < 4; ++g4) { m4[g4] = *(const LAS f32x4*)(st + 32 * tb + 8 * g4 + 4 * h); r4[g4] = *(const LAS f32x4*)(st + 64 + 32 * tb + 8 * g4 + 4 * h); }
  __builtin_amdgcn_sched_barrier(0);
#pragma unroll
  for (int g4 = 0; g4 < 4; ++g4)
#pragma unroll
    for (int t = 0; t < 2; ++t) {
      const f32x16& a = t ? ak : aq;
      float v[4];
#pragma unroll
      for (int q = 0; q < 4; ++q) v[q] = rstd * (a[4 * g4 + q] - mean * (cqk[t][g4][0][q] + cqk[t][g4][1][q]));
      __builtin_amdgcn_raw_buffer_store_b64(u32x2{pk2(v[0], v[1]), pk2(v[2], v[3])}, rws, qoff[g4], t * (WS_K - WS_Q), 0);
    }
  {
    float v[16];
#pragma unroll
    for (int g4 = 0; g4 < 4; ++g4)
#pragma unroll
      for (int q = 0; q < 4; ++q) v[4 * g4 + q] = r4[g4][q] * (av[4 * g4 + q] - m4[g4][q] * cs2);
    const u32x4 oa = {pk2(v[0], v[1]), pk2(v[2], v[3]), pk2(v[8], v[9]), pk2(v[10], v[11])};
    const u32x4 ob = {pk2(v[4], v[5]), pk2(v[6], v[7]), pk2(v[12], v[13]), pk2(v[14], v[15])};
    __builtin_amdgcn_raw_buffer_store_b128(oa, rws, voff, 0, 0);
    __builtin_amdgcn_raw_buffer_store_b128(ob, rws, voff + 512, 0, 0);
  }
  STAMP(6);
  STAMP(7); ST_FLUSH(0, p.ws, b, w);
}

constexpr unsigned L_K = 0, L_V = 40960;
constexpr unsigned L_O = 81920;
constexpr unsigned L_H = 0;
constexpr unsigned L_Z = 32768;
constexpr unsigned L_X1 = 98304;
constexpr unsigned L_ST = 155648;
constexpr unsigned K2_LDS = 163840;
constexpr unsigned SPIN_MAX = 1u << 16;

DI u32x4 bload(__amdgpu_buffer_rsrc_t rs, unsigned voff, unsigned soff) { return __builtin_amdgcn_raw_buffer_load_b128(rs, voff, soff, 0); }
DI u32x4 bload_sc1(__amdgpu_buffer_rsrc_t rs, unsigned voff, unsigned soff) { return __builtin_amdgcn_raw_buffer_load_b128(rs, voff, soff, 16); }
DI void bstore_sc1(u32x4 v, __amdgpu_buffer_rsrc_t rs, unsigned voff, unsigned soff) { __builtin_amdgcn_raw_buffer_store_b128(v, rs, voff, soff, 16); }
DI unsigned umax4(u32x4 v) { return max(max(v[0], v[1]), max(v[2], v[3])); }
#define FENCE() asm volatile("" ::: "memory")
DI float gelu_tanh(float x) {
  const float e = __builtin_amdgcn_exp2f(x * fmaf(x * x, -0.10294324f, -2.3022082f));
  return x * __builtin_amdgcn_rcpf(1.f + e);
}
DI u32x4 pair_tiles(u32x2 a, u32x2 b) {
  const auto s0 = __builtin_amdgcn_permlane16_swap(a[0], b[0], false, false), s1 = __builtin_amdgcn_permlane16_swap(a[1], b[1], false, false);
  return u32x4{s0[0], s1[0], s0[1], s1[1]};
}
template <int NP> DI void fetch_payload(__amdgpu_buffer_rsrc_t rs, unsigned src, LAS char* lds, unsigned lds0, unsigned dst, int lane) {
  for (unsigned spin = 0;; ++spin) {
#pragma unroll
    for (int q = 0; q < NP; ++q) dma16_sc1(rs, lane * 16, src + q * KB, lds0 + dst + q * KB);
    WAIT_VM(0);
    unsigned m = 0;
#pragma unroll
    for (int q = 0; q < NP; ++q) m = max(m, umax4(lds128(lds + dst + q * KB + lane * 16)));
    if (!__any(m == SENT) || spin > SPIN_MAX) break;
  }
}

__global__ void __launch_bounds__(512, 2) k2_rest(Params p) {
  extern __shared__ __attribute__((aligned(16))) char smem[];
  LAS char* lds = (LAS char*)smem;
  const unsigned lds0 = (unsigned)(size_t)lds;
  const int b = blockIdx.x, tid = threadIdx.x, lane = tid & 63;
  const int w = rfl(tid >> 6);
  int i, j; map_block(b, i, j);
  ST_DECL; STAMP(0);
  const __amdgpu_buffer_rsrc_t rws = mk_rsrc(p.ws, WS_END);
  const int l15 = lane & 15, g = lane >> 4;
  const u32x4 ones = {ONES, ONES, ONES, ONES};
  const u32x4 sentv = {SENT, SENT, SENT, SENT};
  const int t32 = 2 * i + (j >> 2), c4 = j & 3, tt0 = 2 * t32;
  const int wf = w < 7 ? w : 6;
  const int Fg = 7 * c4 + wf;
  const unsigned act_off = (((tt0 + (g & 1)) * 14 + (Fg >> 1)) * 64 + (2 * (Fg & 1) + (g >> 1)) * 16 + l15) * 16;

  const int hl = w >> 2, a = w & 3, head = 2 * j + hl, tt = 4 * i + a;
  {
#pragma unroll
    for (int q = 0; q < 5; ++q) { const int n = w * 5 + q, hh = n / 20, kt = 4 * i - 16 + n % 20;
      dma16(rws, lane * 16, WS_K + ((2 * j + hh) * 128 + (kt < 0 ? 0 : kt)) * KB, lds0 + L_K + n * KB); }
#pragma unroll
    for (int q = 0; q < 5; ++q) { const int n = w * 5 + q, hh = n / 20, m = n % 20, kb = 2 * i - 8 + (m >> 1);
      dma16(rws, lane * 16, WS_V + (((2 * j + hh) * 64 + (kb < 0 ? 0 : kb)) * 2 + (m & 1)) * KB, lds0 + L_V + n * KB); }
  }
  const u32x4 qf = bload(rws, lane * 16, WS_Q + (head * 128 + tt) * KB);
  WAIT_VM(0);
  STAMP(1);
  BARS();
  if (w < 7) { bstore_sc1(sentv, rws, act_off, WS_H); bstore_sc1(sentv, rws, act_off, WS_X1); bstore_sc1(sentv, rws, act_off, WS_Z + 14 * KB); }
  if (j < 7) {
#pragma unroll
    for (int s = 0; s < 6; ++s) { const int c = (j + 1 + s) % 7; bstore_sc1(sentv, rws, lane * 16, WS_P + (((i * 7 + c) * 7 + j) * 8 + w) * KB); }
  }
  FENCE();
  u32x4 wo[16];
#pragma unroll
  for (int ks = 0; ks < 16; ++ks) wo[ks] = bload(rws, lane * 16, WS_WO + (Fg * 16 + ks) * KB);
  u32x2 xr[2];
  { const __amdgpu_buffer_rsrc_t rx = mk_rsrc(p.x, S * D * 2);
#pragma unroll
    for (int t = 0; t < 2; ++t) xr[t] = __builtin_amdgcn_raw_buffer_load_b64(rx, ((16 * (tt0 + t) + l15) * D + 16 * Fg + 4 * g) * 2, 0, 0); }
  FENCE();
  u32x4 opiece;
  {
    const int par = a & 1, kl0 = hl * 20 + (a & ~1), vl0 = hl * 20 + (a >> 1) * 2;
    f32x4 sc[18];
    const LAS char* kp = lds + L_K + kl0 * KB + lane * 16;
    const f32x4 zero4 = {0.f, 0.f, 0.f, 0.f};
#pragma unroll
    for (int n = 0; n < 18; ++n) sc[n] = mfma16(lds128(kp + n * KB), qf, zero4);
    const int d0 = 16 * (16 + par) + l15 - 4 * g;
    const float NEG = -__builtin_inff();
#pragma unroll
    for (int r = 0; r < 4; ++r) {
      sc[0][r] = (d0 - r < 256) ? sc[0][r] : NEG;       sc[1][r] = (d0 - 16 - r < 256) ? sc[1][r] : NEG;
      sc[16][r] = (d0 - 256 - r >= 0) ? sc[16][r] : NEG; sc[17][r] = (d0 - 272 - r >= 0) ? sc[17][r] : NEG;
    }
    const int nneg = 16 - (tt & ~1);
    if (nneg > 0) {
#pragma unroll
      for (int n = 0; n < 16; ++n) if (n < nneg) sc[n] = f32x4{NEG, NEG, NEG, NEG};
    }
    float m = NEG;
#pragma unroll
    for (int n = 0; n < 18; ++n) m = fmaxf(fmaxf(m, fmaxf(sc[n][0], sc[n][1])), fmaxf(sc[n][2], sc[n][3]));
    m = fmaxf(m, __shfl_xor(m, 16)); m = fmaxf(m, __shfl_xor(m, 32));
    const float cexp = 0.18898223650461363f * 1.4426950408889634f, mc = -m * cexp;
    f32x4 o0 = zero4, o1 = zero4;
    const LAS char* vp = lds + L_V + vl0 * KB + lane * 16;
#pragma unroll
    for (int mm = 0; mm < 9; ++mm) {
      float e[8];
#pragma unroll
      for (int r = 0; r < 4; ++r) { e[r] = __builtin_amdgcn_exp2f(fmaf(sc[2 * mm][r], cexp, mc)); e[4 + r] = __builtin_amdgcn_exp2f(fmaf(sc[2 * mm + 1][r], cexp, mc)); }
      const u32x4 pf = {pk2(e[0], e[1]), pk2(e[2], e[3]), pk2(e[4], e[5]), pk2(e[6], e[7])};
      o0 = mfma16(lds128(vp + (2 * mm) * KB), pf, o0);
      o1 = mfma16(lds128(vp + (2 * mm + 1) * KB), pf, o1);
    }
    const float rl = __builtin_amdgcn_rcpf(__shfl(o1[0], 48 + l15));
    opiece = pair_tiles(u32x2{pk2(o0[0] * rl, o0[1] * rl), pk2(o0[2] * rl, o0[3] * rl)}, u32x2{pk2(o1[0] * rl, o1[1] * rl), pk2(o1[2] * rl, o1[3] * rl)});
  }
  WAIT_VM(18);
  BARS();
  bstore_sc1(opiece, rws, ((tt * 16 + head) * 64 + ((g >> 1) + 2 * (g & 1)) * 16 + l15) * 16, WS_O);
  STAMP(2);
  WAIT_VM(0);

  fetch_payload<4>(rws, WS_O + (tt0 * 16 + 4 * w) * KB, lds, lds0, L_O + 4 * w * KB, lane);
  STAMP(3);
  BARS();
  u32x4 w1[14];
#pragma unroll
  for (int ks = 0; ks < 14; ++ks) w1[ks] = bload(rws, lane * 16, WS_W1 + (Fg * 14 + ks) * KB);
  u32x2 hp[2];
  {
    f32x4 acc[2] = {};
    const LAS char* op = lds + L_O + lane * 16;
#pragma unroll
    for (int ks = 0; ks < 16; ++ks)
#pragma unroll
      for (int t = 0; t < 2; ++t) acc[t] = mfma16(wo[ks], lds128(op + (t * 16 + ks) * KB), acc[t]);
#pragma unroll
    for (int t = 0; t < 2; ++t) hp[t] = u32x2{pk2(acc[t][0] + bflo(xr[t][0]), acc[t][1] + bfhi(xr[t][0])), pk2(acc[t][2] + bflo(xr[t][1]), acc[t][3] + bfhi(xr[t][1]))};
    const u32x4 pc = pair_tiles(hp[0], hp[1]);
    if (w < 7) bstore_sc1(pc, rws, act_off, WS_H);
  }
  STAMP(4);
  WAIT_VM(0);

  f32x4 cs1 = {};
#pragma unroll
  for (int ks = 0; ks < 14; ++ks) cs1 = mfma16(w1[ks], ones, cs1);
  if (w < 7) fetch_payload<4>(rws, WS_H + (tt0 * 14 + 4 * w) * KB, lds, lds0, L_H + 4 * w * KB, lane);
  STAMP(5);
  BARS();
  u32x4 w2[14];
#pragma unroll
  for (int ks = 0; ks < 14; ++ks) w2[ks] = bload(rws, lane * 16, WS_W2 + (Fg * 14 + ks) * KB);
  float yv[2][4];
  {
    const LAS char* hpz = lds + L_H + lane * 16;
    LAS float* stp = (LAS float*)(lds + L_ST);
    {
#pragma unroll
      for (int t = 0; t < 2; ++t) {
        f32x4 d1 = {}, d2 = {};
        if (w < 7) {
#pragma unroll
          for (int q = 0; q < 2; ++q) { const u32x4 v = lds128(hpz + (t * 14 + 2 * w + q) * KB); d1 = mfma16(ones, v, d1); d2 = mfma16(v, v, d2); } }
        const int r = l15 & 3;
        const float s2 = r == 0 ? d2[0] : r == 1 ? d2[1] : r == 2 ? d2[2] : d2[3];
        if (g == (l15 >> 2)) { stp[((t * 16 + l15) * 8 + w) * 2] = d1[0]; stp[((t * 16 + l15) * 8 + w) * 2 + 1] = s2; }
      }
    }
    f32x4 acc[2] = {};
#pragma unroll
    for (int ks = 0; ks < 14; ++ks)
#pragma unroll
      for (int t = 0; t < 2; ++t) acc[t] = mfma16(w1[ks], lds128(hpz + (t * 14 + ks) * KB), acc[t]);
    BARS();
    u32x2 zp[2];
#pragma unroll
    for (int t = 0; t < 2; ++t) {
      const f32x4 pt = *(const LAS f32x4*)(stp + ((t * 16 + l15) * 8 + 2 * g) * 2);
      float s1 = pt[0] + pt[2], s2 = pt[1] + pt[3];
      s1 += __shfl_xor(s1, 16); s2 += __shfl_xor(s2, 16); s1 += __shfl_xor(s1, 32); s2 += __shfl_xor(s2, 32);
      const float mean = s1 * (1.f / D), var = fmaxf(s2 * (1.f / D) - mean * mean, 0.f);
      const float r1 = __builtin_amdgcn_rsqf(var + 1e-5f);
      const float r12 = r1 * __builtin_amdgcn_rsqf(var * r1 * r1 + 1e-5f);
      const float hv[4] = {bflo(hp[t][0]), bfhi(hp[t][0]), bflo(hp[t][1]), bfhi(hp[t][1])};
      float z[4];
#pragma unroll
      for (int r = 0; r < 4; ++r) { yv[t][r] = (hv[r] - mean) * r1; z[r] = gelu_tanh(r12 * (acc[t][r] - mean * cs1[r])); }
      zp[t] = u32x2{pk2(z[0], z[1]), pk2(z[2], z[3])};
    }
    const u32x4 pc = pair_tiles(zp[0], zp[1]);
    if (w < 7) bstore_sc1(pc, rws, act_off, WS_Z + 14 * KB);
  }
  STAMP(6);
  WAIT_VM(0);

  {
    unsigned zo[6];
#pragma unroll
    for (int q = 0; q < 6; ++q) { const unsigned n = (w + 8 * q) * 64 + lane, row = n / 48, sl = n - 48 * row, tok = 32 * t32 + 14 + sl;
      zo[q] = (sl < 36 && row < 56) ? WS_Z + (((tok >> 4) * 14 + (row >> 2)) * 64 + (row & 3) * 16 + (tok & 15)) * 16 : 0xF0000000u; }
    for (unsigned spin = 0;; ++spin) {
#pragma unroll
      for (int q = 0; q < 6; ++q) if (q < 5 || w < 2) dma16_sc1(rws, zo[q], 0, lds0 + L_Z + (w + 8 * q) * KB);
      WAIT_VM(0);
      unsigned m = 0;
#pragma unroll
      for (int q = 0; q < 6; ++q) if (q < 5 || w < 2) m = max(m, umax4(lds128(lds + L_Z + (w + 8 * q) * KB + lane * 16)));
      if (!__any(m == SENT) || spin > SPIN_MAX) break;
    }
  }
  STAMP(7);
  BARS();
  u32x4 w3[2][14];
  const int jm = j < 7 ? j : 6;
#pragma unroll
  for (int f = 0; f < 2; ++f)
#pragma unroll
    for (int ks = 0; ks < 14; ++ks) w3[f][ks] = bload(rws, lane * 16, WS_W3 + ((16 * jm + 2 * w + f) * 14 + ks) * KB);
  {
    f32x4 acc[2] = {};
    const LAS char* zb = lds + L_Z + (g * 48 + 2 + l15) * 16;
    const int sh3 = g < 2 ? -1 : 1, sh10 = g < 2 ? -2 : 2;
#pragma unroll
    for (int ks = 0; ks < 14; ++ks) {
      const int sh = ks < 3 ? -1 : ks == 3 ? sh3 : ks < 7 ? 1 : ks < 10 ? -2 : ks == 10 ? sh10 : 2;
#pragma unroll
      for (int t = 0; t < 2; ++t) acc[t] = mfma16(w2[ks], lds128(zb + (ks * 192 + 16 * t + sh) * 16), acc[t]);
    }
    u32x2 xp[2];
#pragma unroll
    for (int t = 0; t < 2; ++t) xp[t] = u32x2{pk2(yv[t][0] + acc[t][0], yv[t][1] + acc[t][1]), pk2(yv[t][2] + acc[t][2], yv[t][3] + acc[t][3])};
    const u32x4 pc = pair_tiles(xp[0], xp[1]);
    WAIT_VM(28);
    if (w < 7) bstore_sc1(pc, rws, act_off, WS_X1);
  }
  STAMP(8);
  if (j == 7) { ST_FLUSH(1, p.ws, b, w); return; }
  WAIT_VM(0);

  f32x4 cs3[2] = {};
#pragma unroll
  for (int f = 0; f < 2; ++f)
#pragma unroll
    for (int ks = 0; ks < 14; ++ks) cs3[f] = mfma16(w3[f][ks], ones, cs3[f]);
  fetch_payload<7>(rws, WS_X1 + (4 * i * 14 + 7 * w) * KB, lds, lds0, L_X1 + 7 * w * KB, lane);
  STAMP(9);
  BARS();
  auto w4_issue = [&](int s) {
    const int c = (j + 1 + s) % 7; const unsigned slot = (s & 3) == 3 ? 131072u : (s & 3) * 32768u;
#pragma unroll
    for (int q = 0; q < 4; ++q) dma16(rws, lane * 16, WS_W4 + ((j * 7 + c) * 32 + 4 * w + q) * KB, lds0 + slot + (4 * w + q) * KB);
  };
  w4_issue(0); w4_issue(1); w4_issue(2);
  {
    const LAS char* xp = lds + L_X1 + lane * 16;
    LAS float* stp = (LAS float*)(lds + L_ST);
#pragma unroll
    for (int t = 0; t < 4; ++t) {
      f32x4 d1 = {}, d2 = {};
      { const u32x4 v = lds128(xp + (t * 14 + w) * KB); d1 = mfma16(ones, v, d1); d2 = mfma16(v, v, d2); }
      if (w < 6) { const u32x4 v = lds128(xp + (t * 14 + 8 + w) * KB); d1 = mfma16(ones, v, d1); d2 = mfma16(v, v, d2); }
      const int r = l15 & 3;
      const float s2 = r == 0 ? d2[0] : r == 1 ? d2[1] : r == 2 ? d2[2] : d2[3];
      if (g == (l15 >> 2)) { stp[((t * 16 + l15) * 8 + w) * 2] = d1[0]; stp[((t * 16 + l15) * 8 + w) * 2 + 1] = s2; }
    }
    f32x4 acc[2][4] = {};
#pragma unroll
    for (int ks = 0; ks < 14; ++ks) {
      u32x4 bfr[4];
#pragma unroll
      for (int t = 0; t < 4; ++t) bfr[t] = lds128(xp + (t * 14 + ks) * KB);
#pragma unroll
      for (int t = 0; t < 4; ++t) { acc[0][t] = mfma16(w3[0][ks], bfr[t], acc[0][t]); acc[1][t] = mfma16(w3[1][ks], bfr[t], acc[1][t]); }
    }
    BARS();
    STAMP(10);
#pragma unroll
    for (int t = 0; t < 4; ++t) {
      const f32x4 pt = *(const LAS f32x4*)(stp + ((t * 16 + l15) * 8 + 2 * g) * 2);
      float s1 = pt[0] + pt[2], s2 = pt[1] + pt[3];
      s1 += __shfl_xor(s1, 16); s2 += __shfl_xor(s2, 16); s1 += __shfl_xor(s1, 32); s2 += __shfl_xor(s2, 32);
      const float mean = s1 * (1.f / D), var = fmaxf(s2 * (1.f / D) - mean * mean, 0.f);
      const float rs = __builtin_amdgcn_rsqf(var + 1e-5f);
      float hv[8];
#pragma unroll
      for (int f = 0; f < 2; ++f)
#pragma unroll
        for (int r = 0; r < 4; ++r) hv[4 * f + r] = gelu_tanh(rs * (acc[f][t][r] - mean * cs3[f][r]));
      *(LAS u32x4*)(lds + L_X1 + (t * 8 + w) * KB + lane * 16) = u32x4{pk2(hv[0], hv[1]), pk2(hv[2], hv[3]), pk2(hv[4], hv[5]), pk2(hv[6], hv[7])};
    }
  }
  WAIT_VM(0);
  BARS();
  STAMP(11);
  w4_issue(3);
  const int tb = w & 1, ft = w >> 1;
  u32x4 hf[2][8];
#pragma unroll
  for (int t = 0; t < 2; ++t)
#pragma unroll
    for (int ks = 0; ks < 8; ++ks) hf[t][ks] = lds128(lds + L_X1 + ((2 * tb + t) * 8 + ks) * KB + lane * 16);
  f32x4 acc[2];
#pragma unroll
  for (int s = 0; s < 7; ++s) {
    const unsigned slot = (s & 3) == 3 ? 131072u : (s & 3) * 32768u;
    const LAS char* wp = lds + slot + ft * 8 * KB + lane * 16;
    acc[0] = f32x4{0.f, 0.f, 0.f, 0.f}; acc[1] = acc[0];
#pragma unroll
    for (int ks = 0; ks < 8; ++ks) { const u32x4 av = lds128(wp + ks * KB); acc[0] = mfma16(av, hf[0][ks], acc[0]); acc[1] = mfma16(av, hf[1][ks], acc[1]); }
    if (s < 6) {
      const int c = (j + 1 + s) % 7;
      bstore_sc1(u32x4{pk2(acc[0][0], acc[0][1]), pk2(acc[0][2], acc[0][3]), pk2(acc[1][0], acc[1][1]), pk2(acc[1][2], acc[1][3])}, rws, lane * 16, WS_P + (((i * 7 + c) * 7 + j) * 8 + w) * KB);
      if (s == 2) WAIT_VM(11); else if (s == 3) WAIT_VM(11); else if (s == 4) WAIT_VM(7); else if (s == 5) WAIT_VM(3);
      BARS();
      if (s < 3) w4_issue(s + 4);
    }
  }
  STAMP(12);
  {
    const __amdgpu_buffer_rsrc_t rout = mk_rsrc(p.out, S * D * 2);
    const int Fo = 4 * j + ft;
    u32x2 hres[2], xres[2];
    unsigned ro[2];
#pragma unroll
    for (int t = 0; t < 2; ++t) ro[t] = (((4 * i + 2 * tb + t) * 14 + (Fo >> 1)) * 64 + (2 * (Fo & 1) + (g >> 1)) * 16 + l15) * 16 + (g & 1) * 8;
    WAIT_VM(0);
    u32x4 pv[6];
    for (unsigned spin = 0;; ++spin) {
      FENCE();
      unsigned m = 0, m2 = 0;
#pragma unroll
      for (int t = 0; t < 2; ++t) { hres[t] = __builtin_amdgcn_raw_buffer_load_b64(rws, ro[t], WS_H, 16); xres[t] = __builtin_amdgcn_raw_buffer_load_b64(rws, ro[t], WS_X1, 16); }
#pragma unroll
      for (int s = 0; s < 6; ++s) { const int js = (j + 1 + s) % 7; pv[s] = bload_sc1(rws, lane * 16, WS_P + (((i * 7 + j) * 7 + js) * 8 + w) * KB); }
#pragma unroll
      for (int s = 0; s < 6; ++s) m = max(m, umax4(pv[s]));
#pragma unroll
      for (int t = 0; t < 2; ++t) m2 = max(m2, max(max(hres[t][0], hres[t][1]), max(xres[t][0], xres[t][1])));
      m = max(m, m2);
      if (!__any(m == SENT) || spin > SPIN_MAX) break;
    }
#pragma unroll
    for (int t = 0; t < 2; ++t) {
      float v[4] = {acc[t][0], acc[t][1], acc[t][2], acc[t][3]};
#pragma unroll
      for (int s = 0; s < 6; ++s) { v[0] += bflo(pv[s][2 * t]); v[1] += bfhi(pv[s][2 * t]); v[2] += bflo(pv[s][2 * t + 1]); v[3] += bfhi(pv[s][2 * t + 1]); }
      v[0] += bflo(xres[t][0]) + bflo(hres[t][0]); v[1] += bfhi(xres[t][0]) + bfhi(hres[t][0]); v[2] += bflo(xres[t][1]) + bflo(hres[t][1]); v[3] += bfhi(xres[t][1]) + bfhi(hres[t][1]);
      __builtin_amdgcn_raw_buffer_store_b64(u32x2{pk2(v[0], v[1]), pk2(v[2], v[3])}, rout, ((16 * (4 * i + 2 * tb + t) + l15) * D + 16 * Fo + 4 * g) * 2, 0, 0);
    }
  }
  STAMP(13);
  ST_FLUSH(1, p.ws, b, w);
}

extern "C" void kernel_launch(void* const* d_in, const int* in_sizes, int n_in, void* d_out, int out_size, void* d_ws, size_t ws_size, hipStream_t stream) {
  Params p;
  p.x = (const u16*)d_in[0]; p.wqkv = (const u16*)d_in[1]; p.wo = (const u16*)d_in[2];
  p.w1 = (const u16*)d_in[4]; p.w2 = (const u16*)d_in[5]; p.w3 = (const u16*)d_in[6]; p.w4 = (const u16*)d_in[7];
  p.out = (u16*)d_out; p.ws = (char*)d_ws;
  (void)hipFuncSetAttribute((const void*)k1_qkv, hipFuncAttributeMaxDynamicSharedMemorySize, K1_LDS);
  (void)hipFuncSetAttribute((const void*)k2_rest, hipFuncAttributeMaxDynamicSharedMemorySize, K2_LDS);
  k1_qkv<<<256, 512, K1_LDS, stream>>>(p);
  k2_rest<<<256, 512, K2_LDS, stream>>>(p);
}
```

```cpp
#include <hip/hip_runtime.h>
#include <hip/hip_bf16.h>
#include <stdint.h>

typedef unsigned short u16;
typedef __attribute__((ext_vector_type(8))) __bf16 bf16x8;
typedef __attribute__((ext_vector_type(2))) __bf16 bf16x2;
typedef __attribute__((ext_vector_type(4))) short s16x4;
typedef __attribute__((ext_vector_type(4))) float f32x4;
typedef __attribute__((ext_vector_type(16))) float f32x16;
typedef __attribute__((ext_vector_type(4))) unsigned u32x4;
typedef __attribute__((ext_vector_type(2))) unsigned u32x2;
#define LAS __attribute__((address_space(3)))
#define GAS __attribute__((address_space(1)))
#define DI __device__ __forceinline__

constexpr int S = 2048, D = 448, NH = 16, DH = 28, F = 1792;
constexpr unsigned SENT = 0xFFFFFFFFu;

constexpr unsigned KB = 1024;
constexpr unsigned WS_Q   = 0;
constexpr unsigned WS_K   = WS_Q + 2048 * KB;
constexpr unsigned WS_V   = WS_K + 2048 * KB;
constexpr unsigned WS_WO  = WS_V + 2048 * KB;
constexpr unsigned WS_W1  = WS_WO + 448 * KB;
constexpr unsigned WS_W2  = WS_W1 + 392 * KB;
constexpr unsigned WS_W3  = WS_W2 + 392 * KB;
constexpr unsigned WS_W4  = WS_W3 + 1568 * KB;
constexpr unsigned WS_O   = WS_W4 + 1568 * KB;
constexpr unsigned WS_H   = WS_O + 2048 * KB;
constexpr unsigned WS_X1  = WS_H + 1792 * KB;
constexpr unsigned WS_Z   = WS_X1 + 1792 * KB;
constexpr unsigned WS_P   = WS_Z + 1820 * KB;
constexpr unsigned WS_CS  = WS_P + 32 * 49 * 8 * KB;
constexpr unsigned WS_CNT = WS_CS + 18 * KB;
constexpr unsigned WS_END = WS_CNT + 28 * KB;

struct Params {
  const u16* x; const u16* wqkv; const u16* wo; const u16* w1; const u16* w2; const u16* w3; const u16* w4;
  u16* out; char* ws;
};

DI __amdgpu_buffer_rsrc_t mk_rsrc(const void* p, unsigned bytes) { return __builtin_amdgcn_make_buffer_rsrc((void*)p, (short)0, (int)bytes, 0x00020000); }
DI void dma16(__amdgpu_buffer_rsrc_t rs, unsigned voff, unsigned soff, unsigned ldsaddr) {
  asm volatile("s_mov_b32 m0, %0\n\ts_nop 0\n\tbuffer_load_dwordx4 %1, %2, %3 offen lds" :: "s"(ldsaddr), "v"(voff), "s"(rs), "s"(soff) : "memory");
}
DI void dma16_sc1(__amdgpu_buffer_rsrc_t rs, unsigned voff, unsigned soff, unsigned ldsaddr) {
  asm volatile("s_mov_b32 m0, %0\n\ts_nop 0\n\tbuffer_load_dwordx4 %1, %2, %3 offen sc1 lds" :: "s"(ldsaddr), "v"(voff), "s"(rs), "s"(soff) : "memory");
}
DI void dma4(__amdgpu_buffer_rsrc_t rs, unsigned voff, unsigned soff, unsigned ldsaddr) {
  asm volatile("s_mov_b32 m0, %0\n\ts_nop 0\n\tbuffer_load_dwordx4 %1, %2, %3 offen lds\n\tbuffer_load_dwordx4 %1, %2, %3 offen offset:1024 lds\n\t"
               "buffer_load_dwordx4 %1, %2, %3 offen offset:2048 lds\n\tbuffer_load_dwordx4 %1, %2, %3 offen offset:3072 lds" :: "s"(ldsaddr), "v"(voff), "s"(rs), "s"(soff) : "memory");
}
DI void dma3(__amdgpu_buffer_rsrc_t rs, unsigned voff, unsigned soff, unsigned ldsaddr) {
  asm volatile("s_mov_b32 m0, %0\n\ts_nop 0\n\tbuffer_load_dwordx4 %1, %2, %3 offen lds\n\tbuffer_load_dwordx4 %1, %2, %3 offen offset:1024 lds\n\t"
               "buffer_load_dwordx4 %1, %2, %3 offen offset:2048 lds" :: "s"(ldsaddr), "v"(voff), "s"(rs), "s"(soff) : "memory");
}
#define WAIT_VM(n) asm volatile("s_waitcnt vmcnt(" #n ")" ::: "memory")
#define BAR() asm volatile("s_waitcnt lgkmcnt(0)\n\ts_barrier" ::: "memory")
#define BARS() do { __builtin_amdgcn_sched_barrier(0); BAR(); __builtin_amdgcn_sched_barrier(0); } while (0)
DI unsigned rfl(unsigned v) { return __builtin_amdgcn_readfirstlane(v); }
DI float bflo(unsigned u) { return __uint_as_float(u << 16); }
DI float bfhi(unsigned u) { return __uint_as_float(u & 0xffff0000u); }
DI unsigned pk2(float a, float b) { bf16x2 v; v[0] = (__bf16)a; v[1] = (__bf16)b; return __builtin_bit_cast(unsigned, v); }
DI float dot2(unsigned a, unsigned b, float c) { return __builtin_amdgcn_fdot2_f32_bf16(__builtin_bit_cast(bf16x2, a), __builtin_bit_cast(bf16x2, b), c, false); }
DI f32x16 mfma32(u32x4 a, u32x4 b, f32x16 c) { return __builtin_amdgcn_mfma_f32_32x32x16_bf16(__builtin_bit_cast(bf16x8, a), __builtin_bit_cast(bf16x8, b), c, 0, 0, 0); }
DI f32x4 mfma16(u32x4 a, u32x4 b, f32x4 c) { return __builtin_amdgcn_mfma_f32_16x16x32_bf16(__builtin_bit_cast(bf16x8, a), __builtin_bit_cast(bf16x8, b), c, 0, 0, 0); }
DI u32x2 tr_read(const LAS char* p) { return __builtin_bit_cast(u32x2, __builtin_amdgcn_ds_read_tr16_b64_v4i16((LAS s16x4*)p)); }
DI u32x4 lds128(const LAS char* p) { return *(const LAS u32x4*)p; }
template <int AUX> DI void bst128(u32x4 v, __amdgpu_buffer_rsrc_t rs, unsigned off) { __builtin_amdgcn_raw_buffer_store_b128(v, rs, off, 0, AUX); }
#define XJ j
constexpr unsigned ONES = 0x3F803F80u;
#define AUX_SIDE 0
#define AUX_QKV 0
#define ST_DECL
#define CSTAMP(n)
#define STAMP(n)
#define ST_FLUSH(kern, ws, b, w)

#define XMAP 2
DI void map_block(int b, int& i, int& j) { const int x = b & 7, s = b >> 3; constexpr int JPX = 8 / XMAP; i = (x / XMAP) * 4 * XMAP + s / JPX; j = (x % XMAP) * JPX + s % JPX; }

constexpr unsigned K1_XS = 0;
constexpr unsigned K1_W0 = 7 * 8192;
constexpr unsigned K1_SLOT = 24576;
constexpr unsigned K1_ST = K1_W0 + 4 * K1_SLOT;
constexpr unsigned K1_CS = K1_ST + 512;
constexpr unsigned K1_LDS = K1_CS + 2 * 2 * 3 * 32 * 4;

DI void retile_load(const Params& p, int mat, int ks, int cb, int lane, u32x4 (&v)[4]) {
  const u16* W = mat == 0 ? p.wo : mat == 1 ? p.w1 : mat == 2 ? p.w2 : mat == 3 ? p.w3 : p.w4;
  const unsigned ldw = mat == 3 ? F : D, nrows = mat == 4 ? F : D, krow0 = (mat == 0 ? 28 : 32) * ks;
#pragma unroll
  for (int q = 0; q < 4; ++q) {
    unsigned row = krow0 + (lane >> 3) + 8 * q; row = row < nrows ? row : nrows - 1;
    v[q] = *(const u32x4*)(W + (size_t)row * ldw + cb * 64 + (lane & 7) * 8);
  }
}
template <bool SUMS>
DI void retile_store(const Params& p, LAS char* scr, int mat, int ks, int cb, int lane, const u32x4 (&v)[4], f32x4 (&cs)[4]) {
#pragma unroll
  for (int q = 0; q < 4; ++q) *(LAS u32x4*)(scr + q * 1024 + lane * 16) = v[q];
  const int g = lane >> 4, q4 = (lane & 15) >> 2, pp = lane & 3;
  const int r0 = (mat == 4 ? 4 * g : 8 * g) + q4, r1 = (mat == 4 ? 16 + 4 * g : 8 * g + 4) + q4;
  const __amdgpu_buffer_rsrc_t rws = mk_rsrc(p.ws, WS_END);
  const u32x4 ones = {ONES, ONES, ONES, ONES};
#pragma unroll
  for (int ft = 0; ft < 4; ++ft) {
    u32x2 lo = tr_read(scr + r0 * 128 + ft * 32 + pp * 8), hi = tr_read(scr + r1 * 128 + ft * 32 + pp * 8);
    if (mat == 0 && g == 3) { hi[0] = 0u; hi[1] = 0u; }
    const u32x4 f = {lo[0], lo[1], hi[0], hi[1]};
    const int Fi = 4 * cb + ft;
    unsigned dst;
    if (mat == 0) dst = WS_WO + (((Fi / 7) * 16 + ks) * 7 + Fi % 7) * KB;
    else if (mat <= 2) dst = (mat == 1 ? WS_W1 : WS_W2) + (((Fi / 7) * 14 + ks) * 7 + Fi % 7) * KB;
    else if (mat == 3) dst = WS_W3 + (((Fi >> 4) * 14 + ks) * 16 + (Fi & 15)) * KB;
    else dst = WS_W4 + ((((ks >> 3) * 7 + cb) * 4 + ft) * 8 + (ks & 7)) * KB;
    bst128<AUX_SIDE>(f, rws, lane * 16 + dst);
    if (SUMS) cs[ft] = mfma16(f, ones, cs[ft]);
  }
}

__global__ void __launch_bounds__(512, 2) k1_qkv(Params p) {
  extern __shared__ __attribute__((aligned(16))) char smem[];
  LAS char* lds = (LAS char*)smem;
  const unsigned lds0 = (unsigned)(size_t)lds;
  const int b = blockIdx.x, tid = threadIdx.x, lane = tid & 63;
  const int w = rfl(tid >> 6);
  int i, j; map_block(b, i, j);
  ST_DECL; STAMP(0);

  if (j == 7) {
    const int gw = i * 8 + w;
    const int colid = w < 2 ? w * 32 + i : (w == 2 && i < 6) ? 64 + i : -1;
    const int nid = w >= 3 ? i * 5 + (w - 3) : 160 + (i - 6);
    u32x4 v[7][4];
    auto other = [&](int m, int& mat, int& ks, int& cb) { if (m < 112) { mat = 0; ks = m / 7; cb = m % 7; } else if (m < 210) { mat = 2; ks = (m - 112) / 7; cb = (m - 112) % 7; } else { mat = 4; ks = (m - 210) / 7; cb = (m - 210) % 7; } };
    const int ccol = colid >> 1, chalf = colid & 1, cmat = ccol < 7 ? 1 : 3, ccb = ccol < 7 ? ccol : ccol - 7;
    if (colid >= 0) {
#pragma unroll
      for (int q = 0; q < 7; ++q) retile_load(p, cmat, 7 * chalf + q, ccb, lane, v[q]);
    } else {
#pragma unroll
      for (int q = 0; q < 4; ++q) { const int m = nid + 186 * q; if (m < 602) { int mat, ks, cb; other(m, mat, ks, cb); retile_load(p, mat, ks, cb, lane, v[q]); } }
    }
    const u32x4 sv = {SENT, SENT, SENT, SENT}, zv = {0u, 0u, 0u, 0u};
    const __amdgpu_buffer_rsrc_t rws = mk_rsrc(p.ws, WS_END);
    {
      const int pr = gw * 4 + (lane >> 4), gq = (lane >> 2) & 3, row = lane & 3;
      const unsigned o = row == 0 ? ONES : 0u;
      bst128<AUX_SIDE>(u32x4{o, o, o, o}, rws, WS_V + ((pr * 2 + 1) * 64 + gq * 16 + 12 + row) * 16);
    }
#pragma unroll
    for (int q = 0; q < 4; ++q) { const int pc = (gw * 4 + q) * 64 + lane;
      __builtin_amdgcn_raw_buffer_store_b64(u32x2{0u, 0u}, rws, ((pc >> 4) * 64 + 48 + (pc & 15)) * 16 + 8, WS_Q, AUX_SIDE); }
    if (gw < 28) bst128<AUX_SIDE>(zv, rws, lane * 16 + WS_Z + (gw < 14 ? gw : 129 * 14 + gw - 14) * KB);
    {
      if (gw < 224) { const int pc = gw * 64 + lane, tok = pc & 1, gq = (pc >> 1) & 3, rest = pc >> 3, ks = rest % 14, ce = rest / 14, edge = ce & 1, cl = ce >> 1;
        const int tt = 2 * cl + edge + 1;
        bst128<AUX_SIDE>(sv, rws, WS_Z + ((tt * 14 + ks) * 64 + gq * 16 + (edge ? 14 : 0) + tok) * 16); }
    }
    if (gw < 28) bst128<AUX_SIDE>(zv, rws, WS_CNT + gw * KB + lane * 16);
    f32x4 cs[4] = {};
    if (colid >= 0) {
#pragma unroll
      for (int q = 0; q < 7; ++q) retile_store<true>(p, lds + w * 4096, cmat, 7 * chalf + q, ccb, lane, v[q], cs);
      if ((lane & 15) == 0) {
#pragma unroll
        for (int ft = 0; ft < 4; ++ft) *(f32x4*)(p.ws + WS_CS + (chalf * 2240 + (cmat == 1 ? 0 : 448) + 64 * ccb + 16 * ft + 4 * (lane >> 4)) * 4) = cs[ft]; }
    } else {
#pragma unroll
      for (int q = 0; q < 4; ++q) { const int m = nid + 186 * q; if (m < 602) { int mat, ks, cb; other(m, mat, ks, cb); retile_store<false>(p, lds + w * 4096, mat, ks, cb, lane, v[q], cs); } }
    }
    STAMP(7); ST_FLUSH(0, p.ws, b, w);
    return;
  }

  if (w >= 6) {
    {
      const u32x4 sv = {SENT, SENT, SENT, SENT}; const __amdgpu_buffer_rsrc_t rws = mk_rsrc(p.ws, WS_END);
      for (int kb = (i * 7 + j) * 2 + (w - 6); kb < 2048; kb += 448) bst128<AUX_SIDE>(sv, rws, lane * 16 + WS_O + kb * KB); }
    const int tok = 32 * (w - 6) + (lane & 31), part = lane >> 5;
    const LAS char* xb[4];
#pragma unroll
    for (int q = 0; q < 4; ++q) xb[q] = lds + K1_XS + tok * 128 + ((4 * part + q) ^ ((tok >> 1) & 7)) * 16;
    float s1a = 0.f, s1b = 0.f, s2a = 0.f, s2b = 0.f;
    BARS();
#pragma unroll
    for (int c = 0; c < 7; ++c) {
      u32x4 v[4];
#pragma unroll
      for (int q = 0; q < 4; ++q) v[q] = lds128(xb[q] + c * 8192);
      __builtin_amdgcn_sched_barrier(0);
#pragma unroll
      for (int q = 0; q < 4; ++q)
#pragma unroll
        for (int u = 0; u < 4; ++u) { float lo = bflo(v[q][u]), hi = bfhi(v[q][u]); asm volatile("" : "+v"(lo), "+v"(hi));
          s1a += lo; s1b += hi; s2a = fmaf(lo, lo, s2a); s2b = fmaf(hi, hi, s2b); }
      asm volatile("" : "+v"(s1a), "+v"(s1b), "+v"(s2a), "+v"(s2b));
      if (c == 6) {
        float a1 = s1a + s1b, a2 = s2a + s2b;
        a1 += __shfl_xor(a1, 32); a2 += __shfl_xor(a2, 32);
        const float mean = a1 * (1.f / D), var = fmaxf(a2 * (1.f / D) - mean * mean, 0.f);
        if (lane < 32) { *(LAS float*)(lds + K1_ST + tok * 4) = mean; *(LAS float*)(lds + K1_ST + 256 + tok * 4) = __builtin_amdgcn_rsqf(var + 1e-5f); }
      }
      BARS();
    }
    STAMP(7); ST_FLUSH(0, p.ws, b, w);
    return;
  }
  if (w >= 4) {
    const int lw = w - 4;
    const __amdgpu_buffer_rsrc_t rx = mk_rsrc(p.x, S * D * 2), rw = mk_rsrc(p.wqkv, D * 3 * D * 2);
    const unsigned xv0 = (lane >> 3) * 896 + ((lane & 7) ^ (lane >> 4)) * 16, xv1 = (lane >> 3) * 896 + ((lane & 7) ^ (lane >> 4) ^ 4) * 16;
    const unsigned wv = (lane >> 3) * 2688 + (((lane & 7) ^ (((lane >> 4) & 1) * 4)) * 16);
    auto issue_chunk = [&](int c) {
#pragma unroll
      for (int q = 0; q < 4; ++q) { const unsigned n = lw * 4 + q; dma16(rx, (q & 1) ? xv1 : xv0, (64 * i + 8 * n) * 896 + c * 128, lds0 + K1_XS + c * 8192 + n * 1024); }
#pragma unroll
      for (int q = 0; q < 12; ++q) { const unsigned n = lw * 12 + q, type = n >> 3, rg = n & 7;
        dma16(rw, wv, (64 * c + 8 * rg) * 2688 + type * 896 + j * 128, lds0 + K1_W0 + (c & 3) * K1_SLOT + type * 8192 + rg * 1024); }
    };
    issue_chunk(0); issue_chunk(1);
    STAMP(1);
    WAIT_VM(16); STAMP(2); BARS();
    issue_chunk(2); issue_chunk(3);
    WAIT_VM(32); STAMP(3); BARS();
    issue_chunk(4);
    WAIT_VM(32); STAMP(8); BARS();
    issue_chunk(5);
    WAIT_VM(32); STAMP(9); BARS();
    issue_chunk(6);
    WAIT_VM(32); STAMP(10); BARS();
    WAIT_VM(16); STAMP(11); BARS();
    WAIT_VM(0); STAMP(5); BARS();
    BAR();
    STAMP(7); ST_FLUSH(0, p.ws, b, w);
    return;
  }
  const int tb = w & 1, e = w >> 1, r = lane & 31, h = lane >> 5;
  f32x16 aq = {}, ak = {}, av = {};
  f32x4 cq = {}, ck = {}, cv = {};
  const unsigned cs1 = ((lane & 15) == ((lane >> 4) & 1)) ? ONES : 0u;
  const u32x4 csel = {cs1, cs1, cs1, cs1};
  const int q4 = (lane & 15) >> 2, pp = lane & 3, blk = (lane >> 4) & 1;
  const LAS char* xb[4];
#pragma unroll
  for (int kl = 0; kl < 4; ++kl) xb[kl] = lds + K1_XS + (32 * tb + r) * 128 + ((2 * (kl ^ tb) + h) ^ ((r >> 1) & 7)) * 16;
  const unsigned wb = K1_W0 + (8 * h + q4) * 128 + (((8 * e + 4 * blk + pp) ^ (8 * ((q4 >> 1) & 1))) * 8);
  const LAS char* we = lds + wb + tb * 2048;
  const LAS char* wo = lds + wb + (1 - tb) * 2048;
  u32x4 xf[3], f[3][3];
  auto load_step = [&](int n) {
    xf[n % 3] = lds128(xb[n & 3] + (n >> 2) * 8192);
    const LAS char* wp = ((n & 1) ? wo : we) + ((n >> 2) & 3) * K1_SLOT + (n & 2) * 2048;
#pragma unroll
    for (int t = 0; t < 3; ++t) { const u32x2 lo = tr_read(wp + t * 8192), hi = tr_read(wp + t * 8192 + 512); f[n % 3][t] = u32x4{lo[0], lo[1], hi[0], hi[1]}; }
  };
  const __amdgpu_buffer_rsrc_t rws = mk_rsrc(p.ws, WS_END);
  unsigned qoff[4], voff;
  { const unsigned tt = 4 * i + 2 * tb + (r >> 4);
#pragma unroll
    for (int g4 = 0; g4 < 4; ++g4) { const unsigned n0 = 64 * j + 32 * e + 8 * g4 + 4 * h, head = n0 / 28, d0 = n0 - 28 * head;
      qoff[g4] = WS_Q + ((head * 128 + tt) * 64 + (d0 >> 3) * 16 + (r & 15)) * 16 + (d0 & 4) * 2;
      asm volatile("" : "+v"(qoff[g4])); }
    const unsigned n = 64 * j + 32 * e + r, head = n / 28, d = n - 28 * head;
    voff = WS_V + (((head * 64 + 2 * i + tb) * 2 + (d >> 4)) * 64 + h * 16 + (d & 15)) * 16; asm volatile("" : "+v"(voff)); }
  STAMP(1);
  BARS();
  STAMP(2);
  load_step(0);
#pragma unroll
  for (int n = 0; n < 28; ++n) {
    if ((n & 3) == 3 && n < 27) { if (n == 7) STAMP(8); if (n == 11) STAMP(10); BARS(); if (n == 7) { STAMP(9); CSTAMP(13); } if (n == 11) { STAMP(11); CSTAMP(14); } if (n == 15) STAMP(12); }
    if (n < 27) load_step(n + 1);
    aq = mfma32(f[n % 3][0], xf[n % 3], aq);
    ak = mfma32(f[n % 3][1], xf[n % 3], ak);
    av = mfma32(xf[n % 3], f[n % 3][2], av);
    if (!(n & 1)) {
      cq = mfma16(f[n % 3][0], csel, cq); ck = mfma16(f[n % 3][1], csel, ck); cv = mfma16(f[n % 3][2], csel, cv);
    }
    __builtin_amdgcn_sched_group_barrier(0x8, 1, 0); __builtin_amdgcn_sched_group_barrier(0x100, 3, 0);
    __builtin_amdgcn_sched_group_barrier(0x8, 1, 0); __builtin_amdgcn_sched_group_barrier(0x100, 2, 0);
    __builtin_amdgcn_sched_group_barrier(0x8, 1, 0); __builtin_amdgcn_sched_group_barrier(0x100, 2, 0);
    __builtin_amdgcn_sched_group_barrier(0x8, 3, 0);
    __builtin_amdgcn_sched_barrier(0);
  }
  STAMP(4);
  LAS float* cs = (LAS float*)(lds + K1_CS) + e * 192;
  if ((lane & 15) < 2) { LAS float* o = cs + tb * 96 + 16 * (lane & 1) + 4 * (lane >> 4);
    *(LAS f32x4*)o = cq; *(LAS f32x4*)(o + 32) = ck; *(LAS f32x4*)(o + 64) = cv; }
  BAR();
  STAMP(5);
  const LAS float* st = (const LAS float*)(lds + K1_ST);
  f32x4 cqk[2][4][2], m4[4], r4[4];
#pragma unroll
  for (int t = 0; t < 2; ++t)
#pragma unroll
    for (int g4 = 0; g4 < 4; ++g4) { cqk[t][g4][0] = *(const LAS f32x4*)(cs + t * 32 + 8 * g4 + 4 * h); cqk[t][g4][1] = *(const LAS f32x4*)(cs + 96 + t * 32 + 8 * g4 + 4 * h); }
  const float mean = st[32 * tb + r], rstd = st[64 + 32 * tb + r];
  const float cs2 = cs[64 + r] + cs[96 + 64 + r];
#pragma unroll
  for (int g4 = 0; g4 < 4; ++g4) { m4[g4] = *(const LAS f32x4*)(st + 32 * tb + 8 * g4 + 4 * h); r4[g4] = *(const LAS f32x4*)(st + 64 + 32 * tb + 8 * g4 + 4 * h); }
  __builtin_amdgcn_sched_barrier(0);
#pragma unroll
  for (int g4 = 0; g4 < 4; ++g4)
#pragma unroll
    for (int t = 0; t < 2; ++t) {
      const f32x16& a = t ? ak : aq;
      float v[4];
#pragma unroll
      for (int q = 0; q < 4; ++q) v[q] = rstd * (a[4 * g4 + q] - mean * (cqk[t][g4][0][q] + cqk[t][g4][1][q]));
      __builtin_amdgcn_raw_buffer_store_b64(u32x2{pk2(v[0], v[1]), pk2(v[2], v[3])}, rws, qoff[g4], t * (WS_K - WS_Q), AUX_QKV);
    }
  {
    float v[16];
#pragma unroll
    for (int g4 = 0; g4 < 4; ++g4)
#pragma unroll
      for (int q = 0; q < 4; ++q) v[4 * g4 + q] = r4[g4][q] * (av[4 * g4 + q] - m4[g4][q] * cs2);
    const u32x4 oa = {pk2(v[0], v[1]), pk2(v[2], v[3]), pk2(v[8], v[9]), pk2(v[10], v[11])};
    const u32x4 ob = {pk2(v[4], v[5]), pk2(v[6], v[7]), pk2(v[12], v[13]), pk2(v[14], v[15])};
    bst128<AUX_QKV>(oa, rws, voff);
    bst128<AUX_QKV>(ob, rws, voff + 512);
  }
  STAMP(6);
  STAMP(7); ST_FLUSH(0, p.ws, b, w);
}

constexpr unsigned L_K = 0, L_WOA = 40 * KB, L_V = 120 * KB, L_WOB = 0;
constexpr unsigned L_O = 128 * KB;
constexpr unsigned L_W = 0;
constexpr unsigned L_H = 98 * KB, L_ST4 = 126 * KB;
constexpr unsigned L_Z = 128 * KB;
constexpr unsigned L_X1 = 96 * KB, L_ST6 = 152 * KB;
constexpr unsigned L_HID = 96 * KB;
constexpr unsigned L_RA = 0, L_RB = 32 * KB, L_RC = 64 * KB, L_RD = 128 * KB;
constexpr unsigned K2_LDS = 163840;
constexpr unsigned SPIN_MAX = 1u << 16;

DI u32x4 bload(__amdgpu_buffer_rsrc_t rs, unsigned voff, unsigned soff) { return __builtin_amdgcn_raw_buffer_load_b128(rs, voff, soff, 0); }
DI u32x4 bload_sc1(__amdgpu_buffer_rsrc_t rs, unsigned voff, unsigned soff) { return __builtin_amdgcn_raw_buffer_load_b128(rs, voff, soff, 16); }
DI void bstore_sc1(u32x4 v, __amdgpu_buffer_rsrc_t rs, unsigned voff, unsigned soff) { bst128<16>(v, rs, voff + soff); }
DI unsigned umax4(u32x4 v) { return max(max(v[0], v[1]), max(v[2], v[3])); }
#define FENCE() asm volatile("" ::: "memory")
typedef __attribute__((ext_vector_type(2))) float f32x2_;
DI float gelu_tanh(float x) {
  const float e = __builtin_amdgcn_exp2f(x * fmaf(x * x, -0.10294324f, -2.3022082f));
  return x * __builtin_amdgcn_rcpf(1.f + e);
}
DI u32x4 pair_tiles(u32x2 a, u32x2 b) {
  const auto s0 = __builtin_amdgcn_permlane16_swap(a[0], b[0], false, false), s1 = __builtin_amdgcn_permlane16_swap(a[1], b[1], false, false);
  return u32x4{s0[0], s1[0], s0[1], s1[1]};
}
template <int NT, int NS, class F>
DI void fetch_payload(u32x4 (&v)[NT][2], __amdgpu_buffer_rsrc_t rs, unsigned voff0, unsigned voff1, unsigned soff, unsigned tstride, LAS char* lds, unsigned lds0, unsigned dst, int lane, F fill, void* dbg = nullptr) {
  for (unsigned spin = 0;; ++spin) {
#pragma unroll
    for (int t = 0; t < NT; ++t) { dma16_sc1(rs, voff0, soff + t * tstride, lds0 + dst + t * NS * KB); dma16_sc1(rs, voff1, soff + t * tstride, lds0 + dst + (t * NS + 1) * KB); }
    if (spin == 0) { fill(); __builtin_amdgcn_sched_barrier(0); }
    WAIT_VM(0);
    unsigned m = 0;
#pragma unroll
    for (int t = 0; t < NT; ++t)
#pragma unroll
      for (int q = 0; q < 2; ++q) { v[t][q] = lds128(lds + dst + (t * NS + q) * KB + lane * 16); m = max(m, max(max(v[t][q][0], v[t][q][1]), max(v[t][q][2], v[t][q][3]))); }
    if (!__any(m == SENT) || spin > SPIN_MAX) break;
  }
}
template <int NT> DI void row_stats(const u32x4 (&v)[NT][2], u32x4 ones, LAS float* stp, int w, int l15, int g) {
#pragma unroll
  for (int t = 0; t < NT; ++t) {
    f32x4 d1 = {}, d2 = {};
#pragma unroll
    for (int q = 0; q < 2; ++q) { d1 = __builtin_amdgcn_mfma_f32_16x16x32_bf16(__builtin_bit_cast(bf16x8, ones), __builtin_bit_cast(bf16x8, v[t][q]), d1, 0, 0, 0);
                                  d2 = __builtin_amdgcn_mfma_f32_16x16x32_bf16(__builtin_bit_cast(bf16x8, v[t][q]), __builtin_bit_cast(bf16x8, v[t][q]), d2, 0, 0, 0); }
    const float lo = (l15 & 1) ? d2[1] : d2[0], hi = (l15 & 1) ? d2[3] : d2[2], s2 = (l15 & 2) ? hi : lo;
    if (g == (l15 >> 2)) *(LAS f32x2_*)(stp + ((t * 16 + l15) * 8 + w) * 2) = f32x2_{d1[0], s2};
  }
}

DI void aload128(u32x4& d, __amdgpu_buffer_rsrc_t rs, unsigned voff, unsigned soff) { asm volatile("buffer_load_dwordx4 %0, %1, %2, %3 offen" : "=v"(d) : "v"(voff), "s"(rs), "s"(soff) : "memory"); }
DI void aload64(u32x2& d, __amdgpu_buffer_rsrc_t rs, unsigned voff, unsigned soff) { asm volatile("buffer_load_dwordx2 %0, %1, %2, %3 offen" : "=v"(d) : "v"(voff), "s"(rs), "s"(soff) : "memory"); }
DI void aload128_sc1(u32x4& d, __amdgpu_buffer_rsrc_t rs, unsigned voff, unsigned soff) { asm volatile("buffer_load_dwordx4 %0, %1, %2, %3 offen sc1" : "=v"(d) : "v"(voff), "s"(rs), "s"(soff) : "memory"); }
DI void aload64_sc1(u32x2& d, __amdgpu_buffer_rsrc_t rs, unsigned voff, unsigned soff) { asm volatile("buffer_load_dwordx2 %0, %1, %2, %3 offen sc1" : "=v"(d) : "v"(voff), "s"(rs), "s"(soff) : "memory"); }
#define RETIRE(x) asm volatile("; RETIRE %0" : "+v"(x))
#define SB() __builtin_amdgcn_sched_barrier(0)
typedef __attribute__((ext_vector_type(2))) float f32x2;
DI float max3(float a, float b, float c) { float r; asm("v_max3_f32 %0, %1, %2, %3" : "=v"(r) : "v"(a), "v"(b), "v"(c)); return r; }
DI float gsum(float v) {
  const auto a = __builtin_amdgcn_permlane16_swap(__float_as_uint(v), __float_as_uint(v), false, false); const float s = __uint_as_float(a[0]) + __uint_as_float(a[1]);
  const auto c = __builtin_amdgcn_permlane32_swap(__float_as_uint(s), __float_as_uint(s), false, false); return __uint_as_float(c[0]) + __uint_as_float(c[1]);
}
DI float gmax(float v) {
  const auto a = __builtin_amdgcn_permlane16_swap(__float_as_uint(v), __float_as_uint(v), false, false); const float s = max3(__uint_as_float(a[0]), __uint_as_float(a[1]), __uint_as_float(a[1]));
  const auto c = __builtin_amdgcn_permlane32_swap(__float_as_uint(s), __float_as_uint(s), false, false); return max3(__uint_as_float(c[0]), __uint_as_float(c[1]), __uint_as_float(c[1]));
}
DI unsigned gelu2(f32x2 x) {
  const f32x2 t = x * (x * x * f32x2{-0.10294324f, -0.10294324f} + f32x2{-2.3022082f, -2.3022082f});
  const f32x2 d = f32x2{__builtin_amdgcn_exp2f(t[0]), __builtin_amdgcn_exp2f(t[1])} + f32x2{1.f, 1.f};
  const f32x2 r = x * f32x2{__builtin_amdgcn_rcpf(d[0]), __builtin_amdgcn_rcpf(d[1])};
  return pk2(r[0], r[1]);
}
template <int N, int PD, class L, class U, class M> DI void lds_pipe(L load, U use, M mid) {
  u32x4 buf[PD + 1];
#pragma unroll
  for (int n = 0; n < PD; ++n) buf[n] = load(n);
  SB();
#pragma unroll
  for (int n = 0; n < N; ++n) {
    if (n + PD < N) buf[(n + PD) % (PD + 1)] = load(n + PD);
    SB();
    use(n, buf[n % (PD + 1)]);
    mid(n);
    SB();
  }
}

__global__ void __launch_bounds__(640, 3) k2_rest(Params p) {
  extern __shared__ __attribute__((aligned(16))) char smem[];
  LAS char* lds = (LAS char*)smem;
  const unsigned lds0 = (unsigned)(size_t)lds;
  const int b = blockIdx.x, tid = threadIdx.x, lane = tid & 63;
  const int w = rfl(tid >> 6);
  int i, j; map_block(b, i, j);
  ST_DECL; STAMP(0);
  const __amdgpu_buffer_rsrc_t rws = mk_rsrc(p.ws, WS_END);
  const int l15 = lane & 15, g = lane >> 4;
  const unsigned l16 = lane * 16;
  constexpr unsigned OOB = 0xF0000000u;
  const int t32 = 2 * i + (j >> 2), c4 = j & 3, tt0 = 2 * t32;
  const int sid = XMAP == 1 ? (((i & 3) << 1) | (j >> 2)) : XMAP == 2 ? (i & 7) : ((i & 15) >> 1);
  const int rot14 = (sid * 7) >> 2, rot16 = 2 * sid, rotc = XMAP == 1 ? ((i & 3) * 3) >> 1 : (i & 7) % 6;
  auto pcol = [&](int s) { int u = s + rotc; u = u >= 6 ? u - 6 : u; int c = j + 1 + u; return c >= 7 ? c - 7 : c; };
  const u32x4 sentv = {SENT, SENT, SENT, SENT};

  if (w >= 8) {
    const int lw = w - 8;
#pragma unroll
    for (int q = 0; q < 5; ++q) { const int n = 2 * q + lw, hh = n / 5, kt = 4 * i - 16 + 4 * (n % 5);
      dma4(rws, l16, WS_K + ((2 * j + hh) * 128 + (kt < 0 ? 0 : kt)) * KB, lds0 + L_K + n * 4 * KB); }
#pragma unroll
    for (int q = 0; q < 5; ++q) { const int n = 2 * q + lw, hh = n / 5, kb = 2 * i - 8 + 2 * (n % 5);
      dma4(rws, l16, WS_V + ((2 * j + hh) * 64 + (kb < 0 ? 0 : kb)) * 2 * KB, lds0 + L_V + n * 4 * KB); }
    STAMP(1); WAIT_VM(20); STAMP(2); BARS();
    {
      const unsigned F0 = 7 * c4, o = ((tt0 + lw) * 14 + (F0 >> 1)) * KB + (F0 & 1) * 512 + l16;
#pragma unroll
      for (int u = 0; u < 3; ++u) { const unsigned base = u == 0 ? WS_H : u == 1 ? WS_X1 : WS_Z + 14 * KB;
        bst128<16>(sentv, rws, o + base); bst128<16>(sentv, rws, o + base + KB); bst128<16>(sentv, rws, o + base + 2 * KB); bst128<16>(sentv, rws, lane < 32 ? o + base + 3 * KB : OOB); }
    }
    FENCE();
    auto wo_slot = [&](int q) { const int n = 2 * q + lw; const unsigned src = WS_WO + (c4 * 16 + ((n + rot16) & 15)) * 7 * KB, dst = lds0 + (n <= 10 ? L_WOA + n * 7 * KB : L_WOB + (n - 11) * 7 * KB);
      dma4(rws, l16, src, dst); dma3(rws, l16, src + 4 * KB, dst + 4 * KB); };
    wo_slot(0); wo_slot(1);
    STAMP(3); WAIT_VM(26); STAMP(4); BARS(); STAMP(5);
#pragma unroll
    for (int q = 2; q < 8; ++q) wo_slot(q);
    STAMP(6); WAIT_VM(0); STAMP(7); BARS(); STAMP(8);
    auto w_stage = [&](unsigned wsrc, int q0, int q1) {
#pragma unroll
      for (int q = q0; q < q1; ++q) { const int n = 2 * q + lw; int ks = n + rot14; ks = ks >= 14 ? ks - 14 : ks; const unsigned src = wsrc + (c4 * 14 + ks) * 7 * KB, dst = lds0 + L_W + n * 7 * KB;
        dma4(rws, l16, src, dst); dma3(rws, l16, src + 4 * KB, dst + 4 * KB); } };
    BARS(); STAMP(9);
    w_stage(WS_W1, 0, 2);
    BARS();
    w_stage(WS_W1, 2, 7);
    STAMP(10); WAIT_VM(0); STAMP(11); BARS(); STAMP(12);
    BARS(); STAMP(13);
    w_stage(WS_W2, 0, 2);
    BARS();
    w_stage(WS_W2, 2, 7);
    STAMP(14); WAIT_VM(0); STAMP(15); BARS(); STAMP(16);
    BARS(); STAMP(17);
    if (j == 7) { BARS(); BARS(); return; }
    auto w3_round = [&](int r, unsigned slot) { int ks = 2 * r + lw + rot14; ks = ks >= 14 ? ks - 14 : ks; const unsigned src = WS_W3 + (j * 14 + ks) * 16 * KB;
#pragma unroll
      for (int u = 0; u < 4; ++u) dma4(rws, l16, src + u * 4 * KB, lds0 + slot + (lw * 16 + u * 4) * KB); };
    auto w4_step = [&](int s, unsigned slot) { const unsigned src = WS_W4 + ((j * 7 + (s < 6 ? pcol(s) : j)) * 32 + lw * 16) * KB;
#pragma unroll
      for (int u = 0; u < 4; ++u) dma4(rws, l16, src + u * 4 * KB, lds0 + slot + (lw * 16 + u * 4) * KB); };
    auto arrive = [&](int s) { if (lw == 0 && lane == 0) __hip_atomic_fetch_add((GAS unsigned*)(p.ws + WS_CNT + (i * 7 + pcol(s)) * 128), 1u, __ATOMIC_RELAXED, __HIP_MEMORY_SCOPE_AGENT); };
    w3_round(0, L_RA);
    BARS();
    w3_round(1, L_RB); w3_round(2, L_RC);
    BARS();
    STAMP(18); WAIT_VM(0); STAMP(19); BARS();
    BARS(); STAMP(20);
    BARS();
    w3_round(3, L_RA); w3_round(4, L_RB);
    BARS();
    BARS();
    WAIT_VM(16); BARS(); w3_round(5, L_RC);
    WAIT_VM(16); BARS(); w3_round(6, L_RA);
    WAIT_VM(16); BARS(); w4_step(0, L_RB);
    WAIT_VM(16); BARS(); w4_step(1, L_RC);
    BARS(); w4_step(2, L_RA); w4_step(3, L_RD);
    STAMP(21); WAIT_VM(16); STAMP(22); BARS();
    BARS(); w4_step(4, L_RB);
    BARS(); w4_step(5, L_RC);
    WAIT_VM(32); BARS(); w4_step(6, L_RA);
    WAIT_VM(32); BARS(); arrive(0);
    WAIT_VM(16); BARS(); arrive(1);
    WAIT_VM(0); BARS(); arrive(2);
    BARS(); arrive(3); arrive(4); arrive(5);
    STAMP(23); ST_FLUSH(1, p.ws, b, w);
    return;
  }

  const int hl = w >> 2, a = w & 3, head = 2 * j + hl, tt = 4 * i + a;
  const bool act = w < 7;
  const int wf = act ? w : 6, Fg = 7 * c4 + wf;
  u32x4 qf; u32x2 xr[2]; u32x4 cs1u[2], cs3u[2][2];
  aload128(qf, rws, l16, WS_Q + (head * 128 + tt) * KB);
  const u32x4 ones = {ONES, ONES, ONES, ONES};
  unsigned act_off = act ? (((tt0 + (g & 1)) * 14 + (Fg >> 1)) * 64 + (2 * (Fg & 1) + (g >> 1)) * 16 + l15) * 16 : OOB;
  const int ka = (2 * w + rot14) % 14, kb14 = (2 * w + 1 + rot14) % 14;
  unsigned pv0 = l16 + ka * KB, pv1 = l16 + kb14 * KB, ov0 = l16 + ((2 * w + rot16) & 15) * KB, ov1 = l16 + ((2 * w + 1 + rot16) & 15) * KB;
  unsigned zv[2];
#pragma unroll
  for (int q = 0; q < 2; ++q) { const int ks = q ? kb14 : ka, f0 = 32 * ks + 8 * g, sh = f0 < 112 ? -1 : f0 < 224 ? 1 : f0 < 336 ? -2 : 2, tz = 16 * (tt0 + 1) + l15 + sh;
    zv[q] = (((tz >> 4) * 14 + ks) * 64 + g * 16 + (tz & 15)) * 16; }
  u32x4 selA, selB;
  { const unsigned v = g == (l15 >> 2) ? ((l15 & 1) ? 0x3F800000u : 0x00003F80u) : 0u, d0 = (l15 & 2) ? 0u : v, d1 = (l15 & 2) ? v : 0u;
    selA = u32x4{d0, d1, 0u, 0u}; selB = u32x4{0u, 0u, d0, d1}; }
  unsigned ooff = ((tt * 16 + head) * 64 + ((g >> 1) + 2 * (g & 1)) * 16 + l15) * 16 + WS_O;
  asm volatile("" : "+v"(act_off), "+v"(pv0), "+v"(pv1), "+v"(ov0), "+v"(ov1), "+v"(zv[0]), "+v"(zv[1]), "+v"(selA), "+v"(selB), "+v"(ooff));
  WAIT_VM(0); RETIRE(qf);
  STAMP(1);
  BARS();
  { const __amdgpu_buffer_rsrc_t rx = mk_rsrc(p.x, S * D * 2);
#pragma unroll
    for (int t = 0; t < 2; ++t) aload64(xr[t], rx, ((16 * (tt0 + t) + l15) * D + 16 * Fg + 4 * g) * 2, 0); }
#pragma unroll
  for (int hf = 0; hf < 2; ++hf) { aload128(cs1u[hf], rws, (16 * Fg + 4 * g) * 4, WS_CS + hf * 2240 * 4);
#pragma unroll
    for (int f = 0; f < 2; ++f) aload128(cs3u[hf][f], rws, (448 + 256 * (j < 7 ? j : 6) + 32 * w + 16 * f + 4 * g) * 4, WS_CS + hf * 2240 * 4); }

  u32x4 opiece;
  {
    const int par = a & 1, kl0 = hl * 20 + (a & ~1), vl0 = hl * 20 + (a >> 1) * 2;
    f32x4 sc[18];
    const LAS char* kp = lds + L_K + kl0 * KB + l16;
    const f32x4 zero4 = {0.f, 0.f, 0.f, 0.f};
    lds_pipe<18, 8>([&](int n) { return lds128(kp + n * KB); }, [&](int n, u32x4 kf) { sc[n] = mfma16(kf, qf, zero4); }, [](int) {});
    const int dq = l15 - 4 * g;
    const float NEG = -__builtin_inff();
    if (par == 0) {
#pragma unroll
      for (int r = 0; r < 4; ++r) { const bool ge = dq >= r; sc[0][r] = ge ? NEG : sc[0][r]; sc[16][r] = ge ? sc[16][r] : NEG; sc[17][r] = NEG; }
    } else {
#pragma unroll
      for (int r = 0; r < 4; ++r) { const bool ge = dq >= r; sc[1][r] = ge ? NEG : sc[1][r]; sc[17][r] = ge ? sc[17][r] : NEG; sc[0][r] = NEG; }
    }
    const int nneg = 16 - (tt & ~1);
    if (nneg > 0) {
#pragma unroll
      for (int n = 0; n < 16; ++n) if (n < nneg) sc[n] = f32x4{NEG, NEG, NEG, NEG};
    }
    float m = max3(sc[0][0], sc[0][1], sc[0][2]), m2 = max3(sc[0][3], sc[1][0], sc[1][1]);
    m = max3(m, sc[1][2], sc[1][3]);
#pragma unroll
    for (int n = 2; n < 18; ++n) { m = max3(m, sc[n][0], sc[n][1]); m2 = max3(m2, sc[n][2], sc[n][3]); }
    m = gmax(max3(m, m2, m2));
    const float cexp = 0.18898223650461363f * 1.4426950408889634f, mc = -m * cexp;
    STAMP(14);
    BARS();
    STAMP(15);
    f32x4 o0 = zero4, o1 = zero4;
    const LAS char* vp = lds + L_V + vl0 * KB + l16;
    u32x4 v0 = lds128(vp), v1 = lds128(vp + KB);
#pragma unroll
    for (int mm = 0; mm < 9; ++mm) {
      u32x4 n0, n1;
      if (mm < 8) { n0 = lds128(vp + (2 * mm + 2) * KB); n1 = lds128(vp + (2 * mm + 3) * KB); }
      u32x4 pf;
#pragma unroll
      for (int q = 0; q < 4; ++q) { const f32x4& sv = sc[2 * mm + (q >> 1)];
        pf[q] = pk2(__builtin_amdgcn_exp2f(fmaf(sv[2 * (q & 1)], cexp, mc)), __builtin_amdgcn_exp2f(fmaf(sv[2 * (q & 1) + 1], cexp, mc))); }
      o0 = mfma16(v0, pf, o0);
      o1 = mfma16(v1, pf, o1);
      if (mm < 8) { v0 = n0; v1 = n1; }
      SB();
    }
    const float rl = __builtin_amdgcn_rcpf(gsum(g == 3 ? o1[0] : 0.f));
    opiece = pair_tiles(u32x2{pk2(o0[0] * rl, o0[1] * rl), pk2(o0[2] * rl, o0[3] * rl)}, u32x2{pk2(o1[0] * rl, o1[1] * rl), pk2(o1[2] * rl, o1[3] * rl)});
  }
  STAMP(16);
  WAIT_VM(0); RETIRE(xr[0]); RETIRE(xr[1]); RETIRE(cs1u[0]); RETIRE(cs1u[1]); RETIRE(cs3u[0][0]); RETIRE(cs3u[0][1]); RETIRE(cs3u[1][0]); RETIRE(cs3u[1][1]);
  const f32x4 cs1 = __builtin_bit_cast(f32x4, cs1u[0]) + __builtin_bit_cast(f32x4, cs1u[1]);
  f32x4 cs3[2];
#pragma unroll
  for (int f = 0; f < 2; ++f) cs3[f] = __builtin_bit_cast(f32x4, cs3u[0][f]) + __builtin_bit_cast(f32x4, cs3u[1][f]);
  BARS();
  bst128<16>(opiece, rws, ooff);
  u32x4 wr[16];
#pragma unroll
  for (int n = 0; n < 16; ++n) wr[n] = lds128(lds + (n <= 10 ? L_WOA + n * 7 * KB : L_WOB + (n - 11) * 7 * KB) + wf * KB + l16);
  STAMP(2);
  WAIT_VM(0);

  { u32x4 v[2][2];
    fetch_payload<2, 16>(v, rws, ov0, ov1, WS_O + tt0 * 16 * KB, 16 * KB, lds, lds0, L_O + 2 * w * KB, lane, [] { BARS();   }, p.ws + (40u << 20)); }
  STAMP(3);
  BARS();
  u32x2 hp[2];
  {
    f32x4 acc[2];
#pragma unroll
    for (int t = 0; t < 2; ++t) acc[t] = mfma16(selA, u32x4{xr[t][0], xr[t][1], 0u, 0u}, f32x4{0.f, 0.f, 0.f, 0.f});
    const LAS char* op = lds + L_O + l16;
    lds_pipe<32, 8>([&](int n) { return lds128(op + ((n & 1) * 16 + (n >> 1)) * KB); }, [&](int n, u32x4 f) { acc[n & 1] = mfma16(wr[n >> 1], f, acc[n & 1]); }, [](int) {});
#pragma unroll
    for (int t = 0; t < 2; ++t) hp[t] = u32x2{pk2(acc[t][0], acc[t][1]), pk2(acc[t][2], acc[t][3])};
    bst128<16>(pair_tiles(hp[0], hp[1]), rws, act_off + WS_H);
  }
  STAMP(4);
  WAIT_VM(0);

  LAS float* st4 = (LAS float*)(lds + L_ST4);
  auto stage_weights = [&] { BARS();
#pragma unroll
    for (int n = 0; n < 14; ++n) wr[n] = lds128(lds + L_W + (n * 7 + wf) * KB + l16);
    BARS(); };
  { u32x4 v[2][2] = {};
    if (act) fetch_payload<2, 14>(v, rws, pv0, pv1, WS_H + tt0 * 14 * KB, 14 * KB, lds, lds0, L_H + 2 * w * KB, lane, stage_weights, p.ws + (40u << 20) + 4); else stage_weights();
    row_stats<2>(v, ones, st4, w, l15, g); }
  STAMP(5);
  BARS();
  f32x2 yv[2][2];
  {
    const LAS char* hpz = lds + L_H + l16;
    f32x4 pt[2];
#pragma unroll
    for (int t = 0; t < 2; ++t) pt[t] = *(const LAS f32x4*)(st4 + ((t * 16 + l15) * 8 + 2 * g) * 2);
    f32x4 acc[2] = {};
    lds_pipe<28, 8>([&](int n) { return lds128(hpz + ((n & 1) * 14 + (n >> 1)) * KB); }, [&](int n, u32x4 f) { acc[n & 1] = mfma16(wr[n >> 1], f, acc[n & 1]); }, [](int) {});
    u32x2 zp[2];
#pragma unroll
    for (int t = 0; t < 2; ++t) {
      const float s1 = gsum(pt[t][0] + pt[t][2]), s2 = gsum(pt[t][1] + pt[t][3]);
      const float mean = s1 * (1.f / D), var = fmaxf(s2 * (1.f / D) - mean * mean, 0.f);
      const float r1 = __builtin_amdgcn_rsqf(var + 1e-5f);
      const float r12 = r1 * __builtin_amdgcn_rsqf(var * r1 * r1 + 1e-5f);
      const f32x2 m2 = {mean, mean};
#pragma unroll
      for (int q = 0; q < 2; ++q) {
        yv[t][q] = (f32x2{bflo(hp[t][q]), bfhi(hp[t][q])} - m2) * f32x2{r1, r1};
        zp[t][q] = gelu2((f32x2{acc[t][2 * q], acc[t][2 * q + 1]} - m2 * f32x2{cs1[2 * q], cs1[2 * q + 1]}) * f32x2{r12, r12});
      }
    }
    bst128<16>(pair_tiles(zp[0], zp[1]), rws, act_off + WS_Z + 14 * KB);
  }
  STAMP(6);
  WAIT_VM(0);

  { u32x4 v[2][2];
    if (act) fetch_payload<2, 14>(v, rws, zv[0], zv[1], WS_Z, 14 * KB, lds, lds0, L_Z + 2 * w * KB, lane, stage_weights, p.ws + (40u << 20) + 8); else stage_weights(); }
  STAMP(7);
  BARS();
  {
    f32x4 acc[2] = {};
    const LAS char* zb = lds + L_Z + l16;
    lds_pipe<28, 8>([&](int n) { return lds128(zb + ((n & 1) * 14 + (n >> 1)) * KB); }, [&](int n, u32x4 f) { acc[n & 1] = mfma16(wr[n >> 1], f, acc[n & 1]); }, [](int) {});
    BARS();
    u32x2 xp[2];
#pragma unroll
    for (int t = 0; t < 2; ++t) xp[t] = u32x2{pk2(yv[t][0][0] + acc[t][0], yv[t][0][1] + acc[t][1]), pk2(yv[t][1][0] + acc[t][2], yv[t][1][1] + acc[t][3])};
    bst128<16>(pair_tiles(xp[0], xp[1]), rws, act_off + WS_X1);
  }
  STAMP(8);
  if (j == 7) { ST_FLUSH(1, p.ws, b, w); return; }
  WAIT_VM(0);

  LAS float* st6 = (LAS float*)(lds + L_ST6);
  u32x4 areg[4][2];
  { u32x4 v[4][2] = {};
    auto w3_regs = [&] { BARS();
#pragma unroll
      for (int n = 0; n < 4; ++n)
#pragma unroll
        for (int f = 0; f < 2; ++f) areg[n][f] = lds128(lds + n * 16 * KB + (2 * w + f) * KB + l16);
      BARS(); };
    if (act) fetch_payload<4, 14>(v, rws, pv0, pv1, WS_X1 + 4 * i * 14 * KB, 14 * KB, lds, lds0, L_X1 + 2 * w * KB, lane, w3_regs, p.ws + (40u << 20) + 12); else w3_regs();
    row_stats<4>(v, ones, st6, w, l15, g); }
  STAMP(9);
  BARS();
  {
    const LAS char* xp = lds + L_X1 + l16;
    const LAS char* ap = lds + 2 * w * KB + l16;
    f32x4 pt[4];
#pragma unroll
    for (int t = 0; t < 4; ++t) pt[t] = *(const LAS f32x4*)(st6 + ((t * 16 + l15) * 8 + 2 * g) * 2);
    f32x4 acc[2][4] = {};
    u32x4 bfr[2][4], afr[2][2];
    auto load_k = [&](int n) { const unsigned slot = ((n >> 1) % 3 == 2 ? L_RC : (n >> 1) % 3 == 0 ? L_RA : L_RB) + (n & 1) * 16 * KB;
#pragma unroll
      for (int f = 0; f < 2; ++f) afr[n & 1][f] = n < 4 ? areg[n][f] : lds128(ap + slot + f * KB);
#pragma unroll
      for (int t = 0; t < 4; ++t) bfr[n & 1][t] = lds128(xp + (t * 14 + n) * KB); };
    load_k(0);
    SB();
#pragma unroll
    for (int n = 0; n < 14; ++n) {
      if (n & 1) BARS();
      if (n < 13) load_k(n + 1);
      SB();
#pragma unroll
      for (int t = 0; t < 4; ++t) { acc[0][t] = mfma16(afr[n & 1][0], bfr[n & 1][t], acc[0][t]); acc[1][t] = mfma16(afr[n & 1][1], bfr[n & 1][t], acc[1][t]); }
      SB();
    }
    STAMP(20);
    float mean[4], rs[4];
#pragma unroll
    for (int t = 0; t < 4; ++t) {
      const float s1 = gsum(pt[t][0] + pt[t][2]), s2 = gsum(pt[t][1] + pt[t][3]);
      mean[t] = s1 * (1.f / D); rs[t] = __builtin_amdgcn_rsqf(fmaxf(s2 * (1.f / D) - mean[t] * mean[t], 0.f) + 1e-5f);
    }
#pragma unroll
    for (int t = 0; t < 4; ++t) {
      u32x4 hv;
#pragma unroll
      for (int q = 0; q < 4; ++q) { const int f = q >> 1, r = 2 * (q & 1);
        hv[q] = gelu2((f32x2{acc[f][t][r], acc[f][t][r + 1]} - f32x2{mean[t], mean[t]} * f32x2{cs3[f][r], cs3[f][r + 1]}) * f32x2{rs[t], rs[t]}); }
      *(LAS u32x4*)(lds + L_HID + (t * 8 + w) * KB + l16) = hv;
    }
  }
  STAMP(10);
  BARS();
  STAMP(11);
  const int tb = w & 1, ft = w >> 1;
  u32x4 hf[2][8];
#pragma unroll
  for (int ks = 0; ks < 8; ++ks)
#pragma unroll
    for (int t = 0; t < 2; ++t) hf[t][ks] = lds128(lds + L_HID + ((2 * tb + t) * 8 + ks) * KB + l16);
  const unsigned pbase = l16 + WS_P + ((i * 49 + j) * 8 + w) * KB;
  const int Fo = 4 * j + ft;
  u32x2 hres[2], xres[2];
  unsigned ro[2];
#pragma unroll
  for (int t = 0; t < 2; ++t) { ro[t] = (((4 * i + 2 * tb + t) * 14 + (Fo >> 1)) * 64 + (2 * (Fo & 1) + (g >> 1)) * 16 + l15) * 16 + (g & 1) * 8;
    aload64_sc1(hres[t], rws, ro[t], WS_H); aload64_sc1(xres[t], rws, ro[t], WS_X1); }
  f32x4 acc[2] = {};
  {
    constexpr int PD = 4;
    constexpr unsigned slots[7] = {L_RB, L_RC, L_RA, L_RD, L_RB, L_RC, L_RA};
    const LAS char* wp = lds + ft * 8 * KB + l16;
    u32x4 buf[PD + 1];
#pragma unroll
    for (int m = 0; m < PD; ++m) buf[m] = lds128(wp + slots[0] + m * KB);
    SB();
#pragma unroll
    for (int m = 0; m < 56; ++m) {
      const int s = m >> 3, ks = m & 7;
      if (ks == 8 - PD && s < 6) { if (s >= 3) WAIT_VM(2); BARS(); }
      if (m + PD < 56) buf[(m + PD) % (PD + 1)] = lds128(wp + slots[(m + PD) >> 3] + ((m + PD) & 7) * KB);
      SB();
      acc[0] = mfma16(buf[m % (PD + 1)], hf[0][ks], acc[0]); acc[1] = mfma16(buf[m % (PD + 1)], hf[1][ks], acc[1]);
      if (ks == 7 && s < 6) {
        bst128<16>(u32x4{pk2(acc[0][0], acc[0][1]), pk2(acc[0][2], acc[0][3]), pk2(acc[1][0], acc[1][1]), pk2(acc[1][2], acc[1][3])}, rws, pbase + pcol(s) * 56 * KB);
        acc[0] = f32x4{0.f, 0.f, 0.f, 0.f}; acc[1] = acc[0];
      }
      SB();
    }
  }
  STAMP(12);
  {
    const __amdgpu_buffer_rsrc_t rout = mk_rsrc(p.out, S * D * 2);
    u32x4 pv[6];
    const unsigned pin = WS_P + ((i * 7 + j) * 7 * 8 + w) * KB;
    WAIT_VM(0); RETIRE(hres[0]); RETIRE(hres[1]); RETIRE(xres[0]); RETIRE(xres[1]);
    BARS();
    for (unsigned spin = 0; spin < SPIN_MAX; ++spin) {
      const unsigned m = max(max(max(hres[0][0], hres[0][1]), max(xres[0][0], xres[0][1])), max(max(hres[1][0], hres[1][1]), max(xres[1][0], xres[1][1])));
      if (!__any(m == SENT)) break;
#pragma unroll
      for (int t = 0; t < 2; ++t) { aload64_sc1(hres[t], rws, ro[t], WS_H); aload64_sc1(xres[t], rws, ro[t], WS_X1); }
      WAIT_VM(0); RETIRE(hres[0]); RETIRE(hres[1]); RETIRE(xres[0]); RETIRE(xres[1]);
    }
    { const GAS unsigned* cnt = (const GAS unsigned*)(p.ws + WS_CNT + (i * 7 + j) * 128);
      for (unsigned spin = 0; rfl(__hip_atomic_load(cnt, __ATOMIC_RELAXED, __HIP_MEMORY_SCOPE_AGENT)) < 6u && spin < SPIN_MAX; ++spin) __builtin_amdgcn_s_sleep(1); }
#pragma unroll
    for (int s = 0; s < 6; ++s) { int js = j + 1 + s; js = js >= 7 ? js - 7 : js; aload128_sc1(pv[s], rws, l16, pin + js * 8 * KB); }
    WAIT_VM(0);
#pragma unroll
    for (int s = 0; s < 6; ++s) RETIRE(pv[s]);
    const u32x4 selAB = selA | selB;
#pragma unroll
    for (int s = 0; s < 6; ++s) { acc[0] = mfma16(selA, pv[s], acc[0]); acc[1] = mfma16(selB, pv[s], acc[1]); }
#pragma unroll
    for (int t = 0; t < 2; ++t) {
      acc[t] = mfma16(selAB, u32x4{hres[t][0], hres[t][1], xres[t][0], xres[t][1]}, acc[t]);
      __builtin_amdgcn_raw_buffer_store_b64(u32x2{pk2(acc[t][0], acc[t][1]), pk2(acc[t][2], acc[t][3])}, rout, ((16 * (4 * i + 2 * tb + t) + l15) * D + 16 * Fo + 4 * g) * 2, 0, 0);
    }
  }
  STAMP(13);
  ST_FLUSH(1, p.ws, b, w);
}

extern "C" void kernel_launch(void* const* d_in, const int* in_sizes, int n_in, void* d_out, int out_size, void* d_ws, size_t ws_size, hipStream_t stream) {
  Params p;
  p.x = (const u16*)d_in[0]; p.wqkv = (const u16*)d_in[1]; p.wo = (const u16*)d_in[2];
  p.w1 = (const u16*)d_in[4]; p.w2 = (const u16*)d_in[5]; p.w3 = (const u16*)d_in[6]; p.w4 = (const u16*)d_in[7];
  p.out = (u16*)d_out; p.ws = (char*)d_ws;
  (void)hipFuncSetAttribute((const void*)k1_qkv, hipFuncAttributeMaxDynamicSharedMemorySize, K1_LDS);
  (void)hipFuncSetAttribute((const void*)k2_rest, hipFuncAttributeMaxDynamicSharedMemorySize, K2_LDS);
  k1_qkv<<<256, 512, K1_LDS, stream>>>(p);
  k2_rest<<<256, 640, K2_LDS, stream>>>(p);
}
```

```cpp
#include <hip/hip_runtime.h>
#include <hip/hip_bf16.h>
#include <stdint.h>

typedef unsigned short u16;
typedef __attribute__((ext_vector_type(8))) __bf16 bf16x8;
typedef __attribute__((ext_vector_type(2))) __bf16 bf16x2;
typedef __attribute__((ext_vector_type(4))) short s16x4;
typedef __attribute__((ext_vector_type(4))) float f32x4;
typedef __attribute__((ext_vector_type(16))) float f32x16;
typedef __attribute__((ext_vector_type(4))) unsigned u32x4;
typedef __attribute__((ext_vector_type(2))) unsigned u32x2;
#define LAS __attribute__((address_space(3)))
#define DI __device__ __forceinline__

constexpr int S = 2048, D = 448, NH = 16, DH = 28, F = 1792;
constexpr unsigned SENT = 0xFFFFFFFFu;

constexpr unsigned KB = 1024;
constexpr unsigned WS_Q   = 0;
constexpr unsigned WS_K   = WS_Q + 2048 * KB;
constexpr unsigned WS_V   = WS_K + 2048 * KB;
constexpr unsigned WS_WO  = WS_V + 2048 * KB;
constexpr unsigned WS_W1  = WS_WO + 448 * KB;
constexpr unsigned WS_W2  = WS_W1 + 392 * KB;
constexpr unsigned WS_W3  = WS_W2 + 392 * KB;
constexpr unsigned WS_W4  = WS_W3 + 1568 * KB;
constexpr unsigned WS_O   = WS_W4 + 1568 * KB;
constexpr unsigned WS_H   = WS_O + 2048 * KB;
constexpr unsigned WS_X1  = WS_H + 1792 * KB;
constexpr unsigned WS_Z   = WS_X1 + 1792 * KB;
constexpr unsigned WS_P   = WS_Z + 1820 * KB;
constexpr unsigned WS_END = WS_P + 32 * 49 * 8 * KB;

struct Params {
  const u16* x; const u16* wqkv; const u16* wo; const u16* w1; const u16* w2; const u16* w3; const u16* w4;
  u16* out; char* ws;
};

DI __amdgpu_buffer_rsrc_t mk_rsrc(const void* p, unsigned bytes) { return __builtin_amdgcn_make_buffer_rsrc((void*)p, (short)0, (int)bytes, 0x00020000); }
DI void dma16(__amdgpu_buffer_rsrc_t rs, unsigned voff, unsigned soff, unsigned ldsaddr) {
  asm volatile("s_mov_b32 m0, %0\n\ts_nop 0\n\tbuffer_load_dwordx4 %1, %2, %3 offen lds" :: "s"(ldsaddr), "v"(voff), "s"(rs), "s"(soff) : "memory");
}
DI void dma16_sc1(__amdgpu_buffer_rsrc_t rs, unsigned voff, unsigned soff, unsigned ldsaddr) {
  asm volatile("s_mov_b32 m0, %0\n\ts_nop 0\n\tbuffer_load_dwordx4 %1, %2, %3 offen sc1 lds" :: "s"(ldsaddr), "v"(voff), "s"(rs), "s"(soff) : "memory");
}
#define WAIT_VM(n) asm volatile("s_waitcnt vmcnt(" #n ")" ::: "memory")
#define BAR() asm volatile("s_waitcnt lgkmcnt(0)\n\ts_barrier" ::: "memory")
#define BARS() do { __builtin_amdgcn_sched_barrier(0); BAR(); __builtin_amdgcn_sched_barrier(0); } while (0)
DI unsigned rfl(unsigned v) { return __builtin_amdgcn_readfirstlane(v); }
DI float bflo(unsigned u) { return __uint_as_float(u << 16); }
DI float bfhi(unsigned u) { return __uint_as_float(u & 0xffff0000u); }
DI unsigned pk2(float a, float b) { bf16x2 v; v[0] = (__bf16)a; v[1] = (__bf16)b; return __builtin_bit_cast(unsigned, v); }
DI float dot2(unsigned a, unsigned b, float c) { return __builtin_amdgcn_fdot2_f32_bf16(__builtin_bit_cast(bf16x2, a), __builtin_bit_cast(bf16x2, b), c, false); }
DI f32x16 mfma32(u32x4 a, u32x4 b, f32x16 c) { return __builtin_amdgcn_mfma_f32_32x32x16_bf16(__builtin_bit_cast(bf16x8, a), __builtin_bit_cast(bf16x8, b), c, 0, 0, 0); }
DI f32x4 mfma16(u32x4 a, u32x4 b, f32x4 c) { return __builtin_amdgcn_mfma_f32_16x16x32_bf16(__builtin_bit_cast(bf16x8, a), __builtin_bit_cast(bf16x8, b), c, 0, 0, 0); }
DI u32x2 tr_read(const LAS char* p) { return __builtin_bit_cast(u32x2, __builtin_amdgcn_ds_read_tr16_b64_v4i16((LAS s16x4*)p)); }
DI u32x4 lds128(const LAS char* p) { return *(const LAS u32x4*)p; }
template <int AUX> DI void bst128(u32x4 v, __amdgpu_buffer_rsrc_t rs, unsigned off) { __builtin_amdgcn_raw_buffer_store_b128(v, rs, off, 0, AUX); }
#define XJ j
constexpr unsigned ONES = 0x3F803F80u;
#define AUX_SIDE 0
#define AUX_QKV 0
#define ST_DECL
#define CSTAMP(n)
#define STAMP(n)
#define ST_FLUSH(kern, ws, b, w)

DI void map_block(int b, int& i, int& j) { const int s = b >> 3; i = (b & 7) * 4 + (s >> 3); j = s & 7; }

constexpr unsigned K1_XS = 0;
constexpr unsigned K1_W0 = 7 * 8192;
constexpr unsigned K1_SLOT = 24576;
constexpr unsigned K1_ST = K1_W0 + 4 * K1_SLOT;
constexpr unsigned K1_CS = K1_ST + 512;
constexpr unsigned K1_LDS = K1_CS + 2 * 2 * 3 * 32 * 4;

struct Retile { const u16* src; unsigned ldw, dst; int kind; };
DI void retile_load(const Params& p, int n, int lane, u32x4 (&v)[4], unsigned& dst, int& kind) {
  const u16* W; unsigned ldw, krow0, cb;
  if (n < 112)      { W = p.wo; ldw = D; const int ks = n / 7; cb = n % 7; krow0 = 28 * ks; kind = 1; dst = WS_WO + (4 * cb * 16 + ks) * KB; }
  else if (n < 308) { const int m = n < 210 ? n - 112 : n - 210; W = n < 210 ? p.w1 : p.w2; ldw = D; const int ks = m / 7; cb = m % 7; krow0 = 32 * ks; kind = 0;
                      dst = (n < 210 ? WS_W1 : WS_W2) + (4 * cb * 14 + ks) * KB; }
  else if (n < 700) { const int m = n - 308; W = p.w3; ldw = F; const int ks = m / 28; cb = m % 28; krow0 = 32 * ks; kind = 0; dst = WS_W3 + (4 * cb * 14 + ks) * KB; }
  else              { const int m = n - 700; W = p.w4; ldw = D; const int ks = m / 7; cb = m % 7; krow0 = 32 * ks; kind = 2;
                      dst = WS_W4 + ((((ks >> 3) * 7 + cb) * 4) * 8 + (ks & 7)) * KB; }
  const unsigned nrows = (kind == 2) ? F : D;
#pragma unroll
  for (int q = 0; q < 4; ++q) {
    unsigned row = krow0 + (lane >> 3) + 8 * q; row = row < nrows ? row : nrows - 1;
    v[q] = *(const u32x4*)(W + (size_t)row * ldw + cb * 64 + (lane & 7) * 8);
  }
}
DI void retile_store(const Params& p, LAS char* scr, int lane, const u32x4 (&v)[4], unsigned dst, int kind) {
#pragma unroll
  for (int q = 0; q < 4; ++q) *(LAS u32x4*)(scr + q * 1024 + lane * 16) = v[q];
  const unsigned fstride = (kind == 1 ? 16u : kind == 2 ? 8u : 14u) * KB;
  const int g = lane >> 4, q4 = (lane & 15) >> 2, pp = lane & 3;
  const int r0 = (kind == 2 ? 4 * g : 8 * g) + q4, r1 = (kind == 2 ? 16 + 4 * g : 8 * g + 4) + q4;
  const __amdgpu_buffer_rsrc_t rws = mk_rsrc(p.ws, WS_END);
#pragma unroll
  for (int ft = 0; ft < 4; ++ft) {
    u32x2 lo = tr_read(scr + r0 * 128 + ft * 32 + pp * 8), hi = tr_read(scr + r1 * 128 + ft * 32 + pp * 8);
    if (kind == 1 && g == 3) { hi[0] = 0u; hi[1] = 0u; }
    u32x4 f = {lo[0], lo[1], hi[0], hi[1]};
    bst128<AUX_SIDE>(f, rws, lane * 16 + dst + ft * fstride);
  }
}

__global__ void __launch_bounds__(512, 2) k1_qkv(Params p) {
  extern __shared__ __attribute__((aligned(16))) char smem[];
  LAS char* lds = (LAS char*)smem;
  const unsigned lds0 = (unsigned)(size_t)lds;
  const int b = blockIdx.x, tid = threadIdx.x, lane = tid & 63;
  const int w = rfl(tid >> 6);
  int i, j; map_block(b, i, j);
  ST_DECL; STAMP(0);

  if (j == 7) {
    const int gw = i * 8 + w;
    u32x4 v[5][4]; unsigned dst[5]; int kind[5];
#pragma unroll
    for (int q = 0; q < 5; ++q) if (gw + 256 * q < 1092) retile_load(p, gw + 256 * q, lane, v[q], dst[q], kind[q]);
    const u32x4 sv = {SENT, SENT, SENT, SENT}, zv = {0u, 0u, 0u, 0u};
    const __amdgpu_buffer_rsrc_t rws = mk_rsrc(p.ws, WS_END);
    {
      const int pr = gw * 4 + (lane >> 4), gq = (lane >> 2) & 3, row = lane & 3;
      const unsigned o = row == 0 ? ONES : 0u;
      bst128<AUX_SIDE>(u32x4{o, o, o, o}, rws, WS_V + ((pr * 2 + 1) * 64 + gq * 16 + 12 + row) * 16);
    }
#pragma unroll
    for (int q = 0; q < 4; ++q) { const int pc = (gw * 4 + q) * 64 + lane;
      __builtin_amdgcn_raw_buffer_store_b64(u32x2{0u, 0u}, rws, ((pc >> 4) * 64 + 48 + (pc & 15)) * 16 + 8, WS_Q, AUX_SIDE); }
    if (gw < 28) bst128<AUX_SIDE>(zv, rws, lane * 16 + WS_Z + (gw < 14 ? gw : 129 * 14 + gw - 14) * KB);
    {
      if (gw < 224) { const int pc = gw * 64 + lane, tok = pc & 1, gq = (pc >> 1) & 3, rest = pc >> 3, ks = rest % 14, ce = rest / 14, edge = ce & 1, cl = ce >> 1;
        const int tt = 2 * cl + edge + 1;
        bst128<AUX_SIDE>(sv, rws, WS_Z + ((tt * 14 + ks) * 64 + gq * 16 + (edge ? 14 : 0) + tok) * 16); }
    }
#pragma unroll
    for (int q = 0; q < 5; ++q) if (gw + 256 * q < 1092) retile_store(p, lds + w * 4096, lane, v[q], dst[q], kind[q]);
    STAMP(7); ST_FLUSH(0, p.ws, b, w);
    return;
  }

  if (w >= 6) {
    {
      const u32x4 sv = {SENT, SENT, SENT, SENT}; const __amdgpu_buffer_rsrc_t rws = mk_rsrc(p.ws, WS_END);
      for (int kb = (i * 7 + j) * 2 + (w - 6); kb < 2048; kb += 448) bst128<AUX_SIDE>(sv, rws, lane * 16 + WS_O + kb * KB); }
    const int tok = 32 * (w - 6) + (lane & 31), part = lane >> 5;
    const LAS char* xb[4];
#pragma unroll
    for (int q = 0; q < 4; ++q) xb[q] = lds + K1_XS + tok * 128 + ((4 * part + q) ^ ((tok >> 1) & 7)) * 16;
    float s1a = 0.f, s1b = 0.f, s2a = 0.f, s2b = 0.f;
    BARS();
#pragma unroll
    for (int c = 0; c < 7; ++c) {
      u32x4 v[4];
#pragma unroll
      for (int q = 0; q < 4; ++q) v[q] = lds128(xb[q] + c * 8192);
      __builtin_amdgcn_sched_barrier(0);
#pragma unroll
      for (int q = 0; q < 4; ++q)
#pragma unroll
        for (int u = 0; u < 4; ++u) { float lo = bflo(v[q][u]), hi = bfhi(v[q][u]); asm volatile("" : "+v"(lo), "+v"(hi));
          s1a += lo; s1b += hi; s2a = fmaf(lo, lo, s2a); s2b = fmaf(hi, hi, s2b); }
      asm volatile("" : "+v"(s1a), "+v"(s1b), "+v"(s2a), "+v"(s2b));
      if (c == 6) {
        float a1 = s1a + s1b, a2 = s2a + s2b;
        a1 += __shfl_xor(a1, 32); a2 += __shfl_xor(a2, 32);
        const float mean = a1 * (1.f / D), var = fmaxf(a2 * (1.f / D) - mean * mean, 0.f);
        if (lane < 32) { *(LAS float*)(lds + K1_ST + tok * 4) = mean; *(LAS float*)(lds + K1_ST + 256 + tok * 4) = __builtin_amdgcn_rsqf(var + 1e-5f); }
      }
      BARS();
    }
    STAMP(7); ST_FLUSH(0, p.ws, b, w);
    return;
  }
  if (w >= 4) {
    const int lw = w - 4;
    const __amdgpu_buffer_rsrc_t rx = mk_rsrc(p.x, S * D * 2), rw = mk_rsrc(p.wqkv, D * 3 * D * 2);
    const unsigned xv0 = (lane >> 3) * 896 + ((lane & 7) ^ (lane >> 4)) * 16, xv1 = (lane >> 3) * 896 + ((lane & 7) ^ (lane >> 4) ^ 4) * 16;
    const unsigned wv = (lane >> 3) * 2688 + (((lane & 7) ^ (((lane >> 4) & 1) * 4)) * 16);
    auto issue_chunk = [&](int c) {
#pragma unroll
      for (int q = 0; q < 4; ++q) { const unsigned n = lw * 4 + q; dma16(rx, (q & 1) ? xv1 : xv0, (64 * i + 8 * n) * 896 + c * 128, lds0 + K1_XS + c * 8192 + n * 1024); }
#pragma unroll
      for (int q = 0; q < 12; ++q) { const unsigned n = lw * 12 + q, type = n >> 3, rg = n & 7;
        dma16(rw, wv, (64 * c + 8 * rg) * 2688 + type * 896 + j * 128, lds0 + K1_W0 + (c & 3) * K1_SLOT + type * 8192 + rg * 1024); }
    };
    issue_chunk(0); issue_chunk(1);
    STAMP(1);
    WAIT_VM(16); STAMP(2); BARS();
    issue_chunk(2); issue_chunk(3);
    WAIT_VM(32); STAMP(3); BARS();
    issue_chunk(4);
    WAIT_VM(32); STAMP(8); BARS();
    issue_chunk(5);
    WAIT_VM(32); STAMP(9); BARS();
    issue_chunk(6);
    WAIT_VM(32); STAMP(10); BARS();
    WAIT_VM(16); STAMP(11); BARS();
    WAIT_VM(0); STAMP(5); BARS();
    BAR();
    STAMP(7); ST_FLUSH(0, p.ws, b, w);
    return;
  }
  const int tb = w & 1, e = w >> 1, r = lane & 31, h = lane >> 5;
  f32x16 aq = {}, ak = {}, av = {};
  f32x4 cq = {}, ck = {}, cv = {};
  const unsigned cs1 = ((lane & 15) == ((lane >> 4) & 1)) ? ONES : 0u;
  const u32x4 csel = {cs1, cs1, cs1, cs1};
  const int q4 = (lane & 15) >> 2, pp = lane & 3, blk = (lane >> 4) & 1;
  const LAS char* xb[4];
#pragma unroll
  for (int kl = 0; kl < 4; ++kl) xb[kl] = lds + K1_XS + (32 * tb + r) * 128 + ((2 * (kl ^ tb) + h) ^ ((r >> 1) & 7)) * 16;
  const unsigned wb = K1_W0 + (8 * h + q4) * 128 + (((8 * e + 4 * blk + pp) ^ (8 * ((q4 >> 1) & 1))) * 8);
  const LAS char* we = lds + wb + tb * 2048;
  const LAS char* wo = lds + wb + (1 - tb) * 2048;
  u32x4 xf[3], f[3][3];
  auto load_step = [&](int n) {
    xf[n % 3] = lds128(xb[n & 3] + (n >> 2) * 8192);
    const LAS char* wp = ((n & 1) ? wo : we) + ((n >> 2) & 3) * K1_SLOT + (n & 2) * 2048;
#pragma unroll
    for (int t = 0; t < 3; ++t) { const u32x2 lo = tr_read(wp + t * 8192), hi = tr_read(wp + t * 8192 + 512); f[n % 3][t] = u32x4{lo[0], lo[1], hi[0], hi[1]}; }
  };
  const __amdgpu_buffer_rsrc_t rws = mk_rsrc(p.ws, WS_END);
  unsigned qoff[4], voff;
  { const unsigned tt = 4 * i + 2 * tb + (r >> 4);
#pragma unroll
    for (int g4 = 0; g4 < 4; ++g4) { const unsigned n0 = 64 * j + 32 * e + 8 * g4 + 4 * h, head = n0 / 28, d0 = n0 - 28 * head;
      qoff[g4] = WS_Q + ((head * 128 + tt) * 64 + (d0 >> 3) * 16 + (r & 15)) * 16 + (d0 & 4) * 2;
      asm volatile("" : "+v"(qoff[g4])); }
    const unsigned n = 64 * j + 32 * e + r, head = n / 28, d = n - 28 * head;
    voff = WS_V + (((head * 64 + 2 * i + tb) * 2 + (d >> 4)) * 64 + h * 16 + (d & 15)) * 16; asm volatile("" : "+v"(voff)); }
  STAMP(1);
  BARS();
  STAMP(2);
  load_step(0);
#pragma unroll
  for (int n = 0; n < 28; ++n) {
    if ((n & 3) == 3 && n < 27) { if (n == 7) STAMP(8); if (n == 11) STAMP(10); BARS(); if (n == 7) { STAMP(9); CSTAMP(13); } if (n == 11) { STAMP(11); CSTAMP(14); } if (n == 15) STAMP(12); }
    if (n < 27) load_step(n + 1);
    aq = mfma32(f[n % 3][0], xf[n % 3], aq);
    ak = mfma32(f[n % 3][1], xf[n % 3], ak);
    av = mfma32(xf[n % 3], f[n % 3][2], av);
    if (!(n & 1)) {
      cq = mfma16(f[n % 3][0], csel, cq); ck = mfma16(f[n % 3][1], csel, ck); cv = mfma16(f[n % 3][2], csel, cv);
    }
    __builtin_amdgcn_sched_group_barrier(0x8, 1, 0); __builtin_amdgcn_sched_group_barrier(0x100, 3, 0);
    __builtin_amdgcn_sched_group_barrier(0x8, 1, 0); __builtin_amdgcn_sched_group_barrier(0x100, 2, 0);
    __builtin_amdgcn_sched_group_barrier(0x8, 1, 0); __builtin_amdgcn_sched_group_barrier(0x100, 2, 0);
    __builtin_amdgcn_sched_group_barrier(0x8, 3, 0);
    __builtin_amdgcn_sched_barrier(0);
  }
  STAMP(4);
  LAS float* cs = (LAS float*)(lds + K1_CS) + e * 192;
  if ((lane & 15) < 2) { LAS float* o = cs + tb * 96 + 16 * (lane & 1) + 4 * (lane >> 4);
    *(LAS f32x4*)o = cq; *(LAS f32x4*)(o + 32) = ck; *(LAS f32x4*)(o + 64) = cv; }
  BAR();
  STAMP(5);
  const LAS float* st = (const LAS float*)(lds + K1_ST);
  f32x4 cqk[2][4][2], m4[4], r4[4];
#pragma unroll
  for (int t = 0; t < 2; ++t)
#pragma unroll
    for (int g4 = 0; g4 < 4; ++g4) { cqk[t][g4][0] = *(const LAS f32x4*)(cs + t * 32 + 8 * g4 + 4 * h); cqk[t][g4][1] = *(const LAS f32x4*)(cs + 96 + t * 32 + 8 * g4 + 4 * h); }
  const float mean = st[32 * tb + r], rstd = st[64 + 32 * tb + r];
  const float cs2 = cs[64 + r] + cs[96 + 64 + r];
#pragma unroll
  for (int g4 = 0; g4 < 4; ++g4) { m4[g4] = *(const LAS f32x4*)(st + 32 * tb + 8 * g4 + 4 * h); r4[g4] = *(const LAS f32x4*)(st + 64 + 32 * tb + 8 * g4 + 4 * h); }
  __builtin_amdgcn_sched_barrier(0);
#pragma unroll
  for (int g4 = 0; g4 < 4; ++g4)
#pragma unroll
    for (int t = 0; t < 2; ++t) {
      const f32x16& a = t ? ak : aq;
      float v[4];
#pragma unroll
      for (int q = 0; q < 4; ++q) v[q] = rstd * (a[4 * g4 + q] - mean * (cqk[t][g4][0][q] + cqk[t][g4][1][q]));
      __builtin_amdgcn_raw_buffer_store_b64(u32x2{pk2(v[0], v[1]), pk2(v[2], v[3])}, rws, qoff[g4], t * (WS_K - WS_Q), AUX_QKV);
    }
  {
    float v[16];
#pragma unroll
    for (int g4 = 0; g4 < 4; ++g4)
#pragma unroll
      for (int q = 0; q < 4; ++q) v[4 * g4 + q] = r4[g4][q] * (av[4 * g4 + q] - m4[g4][q] * cs2);
    const u32x4 oa = {pk2(v[0], v[1]), pk2(v[2], v[3]), pk2(v[8], v[9]), pk2(v[10], v[11])};
    const u32x4 ob = {pk2(v[4], v[5]), pk2(v[6], v[7]), pk2(v[12], v[13]), pk2(v[14], v[15])};
    bst128<AUX_QKV>(oa, rws, voff);
    bst128<AUX_QKV>(ob, rws, voff + 512);
  }
  STAMP(6);
  STAMP(7); ST_FLUSH(0, p.ws, b, w);
}

constexpr unsigned L_K = 0, L_V = 40960;
constexpr unsigned L_O = 81920;
constexpr unsigned L_H = 0;
constexpr unsigned L_Z = 32768;
constexpr unsigned L_X1 = 98304;
constexpr unsigned L_ST = 155648;
constexpr unsigned K2_LDS = 163840;
constexpr unsigned SPIN_MAX = 1u << 16;

DI u32x4 bload(__amdgpu_buffer_rsrc_t rs, unsigned voff, unsigned soff) { return __builtin_amdgcn_raw_buffer_load_b128(rs, voff, soff, 0); }
DI u32x4 bload_sc1(__amdgpu_buffer_rsrc_t rs, unsigned voff, unsigned soff) { return __builtin_amdgcn_raw_buffer_load_b128(rs, voff, soff, 16); }
DI void bstore_sc1(u32x4 v, __amdgpu_buffer_rsrc_t rs, unsigned voff, unsigned soff) { bst128<16>(v, rs, voff + soff); }
DI unsigned umax4(u32x4 v) { return max(max(v[0], v[1]), max(v[2], v[3])); }
#define FENCE() asm volatile("" ::: "memory")
typedef __attribute__((ext_vector_type(2))) float f32x2_;
DI float gelu_tanh(float x) {
  const float e = __builtin_amdgcn_exp2f(x * fmaf(x * x, -0.10294324f, -2.3022082f));
  return x * __builtin_amdgcn_rcpf(1.f + e);
}
DI u32x4 pair_tiles(u32x2 a, u32x2 b) {
  const auto s0 = __builtin_amdgcn_permlane16_swap(a[0], b[0], false, false), s1 = __builtin_amdgcn_permlane16_swap(a[1], b[1], false, false);
  return u32x4{s0[0], s1[0], s0[1], s1[1]};
}
template <int NT, int NS, class F>
DI void fetch_payload(u32x4 (&v)[NT][2], __amdgpu_buffer_rsrc_t rs, unsigned voff0, unsigned voff1, unsigned soff, unsigned tstride, LAS char* lds, unsigned lds0, unsigned dst, int lane, F fill) {
  for (unsigned spin = 0;; ++spin) {
#pragma unroll
    for (int t = 0; t < NT; ++t) { dma16_sc1(rs, voff0, soff + t * tstride, lds0 + dst + t * NS * KB); dma16_sc1(rs, voff1, soff + t * tstride, lds0 + dst + (t * NS + 1) * KB); }
    if (spin == 0) { fill(); __builtin_amdgcn_sched_barrier(0); }
    WAIT_VM(0);
    unsigned m = 0;
#pragma unroll
    for (int t = 0; t < NT; ++t)
#pragma unroll
      for (int q = 0; q < 2; ++q) { v[t][q] = lds128(lds + dst + (t * NS + q) * KB + lane * 16); m = max(m, max(max(v[t][q][0], v[t][q][1]), max(v[t][q][2], v[t][q][3]))); }
    if (!__any(m == SENT) || spin > SPIN_MAX) break;
  }
}
template <int NT> DI void row_stats(const u32x4 (&v)[NT][2], u32x4 ones, LAS float* stp, int w, int l15, int g) {
#pragma unroll
  for (int t = 0; t < NT; ++t) {
    f32x4 d1 = {}, d2 = {};
#pragma unroll
    for (int q = 0; q < 2; ++q) { d1 = __builtin_amdgcn_mfma_f32_16x16x32_bf16(__builtin_bit_cast(bf16x8, ones), __builtin_bit_cast(bf16x8, v[t][q]), d1, 0, 0, 0);
                                  d2 = __builtin_amdgcn_mfma_f32_16x16x32_bf16(__builtin_bit_cast(bf16x8, v[t][q]), __builtin_bit_cast(bf16x8, v[t][q]), d2, 0, 0, 0); }
    const float lo = (l15 & 1) ? d2[1] : d2[0], hi = (l15 & 1) ? d2[3] : d2[2], s2 = (l15 & 2) ? hi : lo;
    if (g == (l15 >> 2)) *(LAS f32x2_*)(stp + ((t * 16 + l15) * 8 + w) * 2) = f32x2_{d1[0], s2};
  }
}

DI void aload128(u32x4& d, __amdgpu_buffer_rsrc_t rs, unsigned voff, unsigned soff) { asm volatile("buffer_load_dwordx4 %0, %1, %2, %3 offen" : "=v"(d) : "v"(voff), "s"(rs), "s"(soff) : "memory"); }
DI void aload64(u32x2& d, __amdgpu_buffer_rsrc_t rs, unsigned voff, unsigned soff) { asm volatile("buffer_load_dwordx2 %0, %1, %2, %3 offen" : "=v"(d) : "v"(voff), "s"(rs), "s"(soff) : "memory"); }
DI void aload128_sc1(u32x4& d, __amdgpu_buffer_rsrc_t rs, unsigned voff, unsigned soff) { asm volatile("buffer_load_dwordx4 %0, %1, %2, %3 offen sc1" : "=v"(d) : "v"(voff), "s"(rs), "s"(soff) : "memory"); }
DI void aload64_sc1(u32x2& d, __amdgpu_buffer_rsrc_t rs, unsigned voff, unsigned soff) { asm volatile("buffer_load_dwordx2 %0, %1, %2, %3 offen sc1" : "=v"(d) : "v"(voff), "s"(rs), "s"(soff) : "memory"); }
#define RETIRE(x) asm volatile("; RETIRE %0" : "+v"(x))
#define SB() __builtin_amdgcn_sched_barrier(0)
typedef __attribute__((ext_vector_type(2))) float f32x2;
DI float max3(float a, float b, float c) { float r; asm("v_max3_f32 %0, %1, %2, %3" : "=v"(r) : "v"(a), "v"(b), "v"(c)); return r; }
DI float gsum(float v) {
  const auto a = __builtin_amdgcn_permlane16_swap(__float_as_uint(v), __float_as_uint(v), false, false); const float s = __uint_as_float(a[0]) + __uint_as_float(a[1]);
  const auto c = __builtin_amdgcn_permlane32_swap(__float_as_uint(s), __float_as_uint(s), false, false); return __uint_as_float(c[0]) + __uint_as_float(c[1]);
}
DI float gmax(float v) {
  const auto a = __builtin_amdgcn_permlane16_swap(__float_as_uint(v), __float_as_uint(v), false, false); const float s = max3(__uint_as_float(a[0]), __uint_as_float(a[1]), __uint_as_float(a[1]));
  const auto c = __builtin_amdgcn_permlane32_swap(__float_as_uint(s), __float_as_uint(s), false, false); return max3(__uint_as_float(c[0]), __uint_as_float(c[1]), __uint_as_float(c[1]));
}
DI unsigned gelu2(f32x2 x) {
  const f32x2 t = x * (x * x * f32x2{-0.10294324f, -0.10294324f} + f32x2{-2.3022082f, -2.3022082f});
  const f32x2 d = f32x2{__builtin_amdgcn_exp2f(t[0]), __builtin_amdgcn_exp2f(t[1])} + f32x2{1.f, 1.f};
  const f32x2 r = x * f32x2{__builtin_amdgcn_rcpf(d[0]), __builtin_amdgcn_rcpf(d[1])};
  return pk2(r[0], r[1]);
}
template <int N, int PD, class L, class U, class M> DI void lds_pipe(L load, U use, M mid) {
  u32x4 buf[PD + 1];
#pragma unroll
  for (int n = 0; n < PD; ++n) buf[n] = load(n);
  SB();
#pragma unroll
  for (int n = 0; n < N; ++n) {
    if (n + PD < N) buf[(n + PD) % (PD + 1)] = load(n + PD);
    use(n, buf[n % (PD + 1)]);
    mid(n);
    SB();
  }
}

__global__ void __launch_bounds__(512, 2) k2_rest(Params p) {
  extern __shared__ __attribute__((aligned(16))) char smem[];
  LAS char* lds = (LAS char*)smem;
  const unsigned lds0 = (unsigned)(size_t)lds;
  const int b = blockIdx.x, tid = threadIdx.x, lane = tid & 63;
  const int w = rfl(tid >> 6);
  int i, j; map_block(b, i, j);
  ST_DECL; STAMP(0);
  const __amdgpu_buffer_rsrc_t rws = mk_rsrc(p.ws, WS_END);
  const int l15 = lane & 15, g = lane >> 4;
  const unsigned l16 = lane * 16;
  constexpr unsigned OOB = 0xF0000000u;

  const int hl = w >> 2, a = w & 3, head = 2 * j + hl, tt = 4 * i + a;
  u32x4 qf;
  aload128(qf, rws, l16, WS_Q + (head * 128 + tt) * KB);
  {
#pragma unroll
    for (int q = 0; q < 5; ++q) { const int n = w * 5 + q, hh = n / 20, kt = 4 * i - 16 + n % 20;
      dma16(rws, l16, WS_K + ((2 * j + hh) * 128 + (kt < 0 ? 0 : kt)) * KB, lds0 + L_K + n * KB); }
#pragma unroll
    for (int q = 0; q < 5; ++q) { const int n = w * 5 + q, hh = n / 20, m = n % 20, kb = 2 * i - 8 + (m >> 1);
      dma16(rws, l16, WS_V + (((2 * j + hh) * 64 + (kb < 0 ? 0 : kb)) * 2 + (m & 1)) * KB, lds0 + L_V + n * KB); }
  }
  SB();
  const u32x4 ones = {ONES, ONES, ONES, ONES};
  const u32x4 sentv = {SENT, SENT, SENT, SENT};
  const int t32 = 2 * i + (j >> 2), c4 = j & 3, tt0 = 2 * t32;
  const bool act = w < 7;
  const int Fg = 7 * c4 + (act ? w : 6);
  const unsigned l16a = act ? l16 : OOB;
  const unsigned act_off = act ? (((tt0 + (g & 1)) * 14 + (Fg >> 1)) * 64 + (2 * (Fg & 1) + (g >> 1)) * 16 + l15) * 16 : OOB;
  const int sid = ((i & 3) << 1) | (j >> 2);
  const int rot14 = (sid * 7) >> 2, rot16 = 2 * sid, rotc = ((i & 3) * 3) >> 1;
  unsigned koff[14];
#pragma unroll
  for (int n = 0; n < 14; ++n) { const int k = n + rot14; koff[n] = (k >= 14 ? k - 14 : k) * KB; }
  unsigned pc[6];
#pragma unroll
  for (int s = 0; s < 6; ++s) { int u = s + rotc; u = u >= 6 ? u - 6 : u; int c = j + 1 + u; c = c >= 7 ? c - 7 : c; pc[s] = c; }
  const unsigned pbase = WS_P + ((i * 49 + j) * 8 + w) * KB;
  const unsigned l16p = j < 7 ? l16 : OOB;
  const int ka = (2 * w + rot14) % 14, kb14 = (2 * w + 1 + rot14) % 14;
  const unsigned pv0 = l16 + ka * KB, pv1 = l16 + kb14 * KB;
  WAIT_VM(5); RETIRE(qf);
  STAMP(1);
  BARS();
  u32x4 wo[16]; u32x2 xr[2];
  u32x4 opiece;
  {
    const int par = a & 1, kl0 = hl * 20 + (a & ~1), vl0 = hl * 20 + (a >> 1) * 2;
    f32x4 sc[18];
    const LAS char* kp = lds + L_K + kl0 * KB + l16;
    const f32x4 zero4 = {0.f, 0.f, 0.f, 0.f};
    lds_pipe<18, 8>([&](int n) { return lds128(kp + n * KB); }, [&](int n, u32x4 kf) { sc[n] = mfma16(kf, qf, zero4); }, [](int) {});
    const int dq = l15 - 4 * g;
    const float NEG = -__builtin_inff();
    if (par == 0) {
#pragma unroll
      for (int r = 0; r < 4; ++r) { const bool ge = dq >= r; sc[0][r] = ge ? NEG : sc[0][r]; sc[16][r] = ge ? sc[16][r] : NEG; sc[17][r] = NEG; }
    } else {
#pragma unroll
      for (int r = 0; r < 4; ++r) { const bool ge = dq >= r; sc[1][r] = ge ? NEG : sc[1][r]; sc[17][r] = ge ? sc[17][r] : NEG; sc[0][r] = NEG; }
    }
    const int nneg = 16 - (tt & ~1);
    if (nneg > 0) {
#pragma unroll
      for (int n = 0; n < 16; ++n) if (n < nneg) sc[n] = f32x4{NEG, NEG, NEG, NEG};
    }
    float m = max3(sc[0][0], sc[0][1], sc[0][2]), m2 = max3(sc[0][3], sc[1][0], sc[1][1]);
    m = max3(m, sc[1][2], sc[1][3]);
#pragma unroll
    for (int n = 2; n < 18; ++n) { m = max3(m, sc[n][0], sc[n][1]); m2 = max3(m2, sc[n][2], sc[n][3]); }
    m = gmax(max3(m, m2, m2));
    const float cexp = 0.18898223650461363f * 1.4426950408889634f, mc = -m * cexp;
    STAMP(14);
    WAIT_VM(0);
    BARS();
    STAMP(15);
    bstore_sc1(sentv, rws, act_off, WS_H); bstore_sc1(sentv, rws, act_off, WS_X1); bstore_sc1(sentv, rws, act_off, WS_Z + 14 * KB);
    f32x4 o0 = zero4, o1 = zero4;
    const LAS char* vp = lds + L_V + vl0 * KB + l16;
    u32x4 v0 = lds128(vp), v1 = lds128(vp + KB);
    const __amdgpu_buffer_rsrc_t rx = mk_rsrc(p.x, S * D * 2);
    const unsigned wob = WS_WO + Fg * 16 * KB;
#pragma unroll
    for (int mm = 0; mm < 9; ++mm) {
      u32x4 n0, n1;
      if (mm < 8) { n0 = lds128(vp + (2 * mm + 2) * KB); n1 = lds128(vp + (2 * mm + 3) * KB); }
      u32x4 pf;
#pragma unroll
      for (int q = 0; q < 4; ++q) { const f32x4& sv = sc[2 * mm + (q >> 1)];
        const f32x2 t = f32x2{sv[2 * (q & 1)], sv[2 * (q & 1) + 1]} * f32x2{cexp, cexp} + f32x2{mc, mc};
        pf[q] = pk2(__builtin_amdgcn_exp2f(t[0]), __builtin_amdgcn_exp2f(t[1])); }
      o0 = mfma16(v0, pf, o0);
      o1 = mfma16(v1, pf, o1);
      if (mm < 8) {
 aload128(wo[2 * mm], rws, l16a, wob + ((2 * mm + rot16) & 15) * KB); aload128(wo[2 * mm + 1], rws, l16a, wob + ((2 * mm + 1 + rot16) & 15) * KB); }
      else if (mm == 8) {
#pragma unroll
        for (int t = 0; t < 2; ++t) aload64(xr[t], rx, ((16 * (tt0 + t) + l15) * D + 16 * Fg + 4 * g) * 2, 0);
      }
      if (mm < 8) { v0 = n0; v1 = n1; }
      SB();
    }
    const float rl = __builtin_amdgcn_rcpf(gsum(g == 3 ? o1[0] : 0.f));
    opiece = pair_tiles(u32x2{pk2(o0[0] * rl, o0[1] * rl), pk2(o0[2] * rl, o0[3] * rl)}, u32x2{pk2(o1[0] * rl, o1[1] * rl), pk2(o1[2] * rl, o1[3] * rl)});
  }
  WAIT_VM(18);
  BARS();
  bstore_sc1(opiece, rws, ((tt * 16 + head) * 64 + ((g >> 1) + 2 * (g & 1)) * 16 + l15) * 16, WS_O);
  STAMP(2);
  WAIT_VM(0);
#pragma unroll
  for (int ks = 0; ks < 16; ++ks) RETIRE(wo[ks]);
  RETIRE(xr[0]); RETIRE(xr[1]);

  { u32x4 v[2][2];
    fetch_payload<2, 16>(v, rws, l16 + ((2 * w + rot16) & 15) * KB, l16 + ((2 * w + 1 + rot16) & 15) * KB, WS_O + tt0 * 16 * KB, 16 * KB, lds, lds0, L_O + 2 * w * KB, lane, [] {}); }
  STAMP(3);
  BARS();
  u32x4 w1[14];
  u32x2 hp[2];
  const unsigned w1b = WS_W1 + Fg * 14 * KB;
  {
    f32x4 acc[2] = {};
    const LAS char* op = lds + L_O + l16;
    lds_pipe<32, 8>([&](int n) { return lds128(op + ((n & 1) * 16 + (n >> 1)) * KB); }, [&](int n, u32x4 f) { acc[n & 1] = mfma16(wo[n >> 1], f, acc[n & 1]); },
      [&](int n) { if ((n & 3) == 3) { const int q = n >> 2; if (q < 3) bstore_sc1(sentv, rws, l16p, pbase + pc[q] * 56 * KB); else aload128(w1[q - 3], rws, l16a, w1b + koff[q - 3]); } });
#pragma unroll
    for (int t = 0; t < 2; ++t) hp[t] = u32x2{pk2(acc[t][0] + bflo(xr[t][0]), acc[t][1] + bfhi(xr[t][0])), pk2(acc[t][2] + bflo(xr[t][1]), acc[t][3] + bfhi(xr[t][1]))};
    bstore_sc1(pair_tiles(hp[0], hp[1]), rws, act_off, WS_H);
  }
  STAMP(17);
#pragma unroll
  for (int n = 5; n < 14; ++n) aload128(w1[n], rws, l16a, w1b + koff[n]);
  STAMP(4);
  WAIT_VM(0);
#pragma unroll
  for (int n = 0; n < 14; ++n) RETIRE(w1[n]);

  f32x4 cs1 = {};
  LAS float* st4 = (LAS float*)(lds + L_ST);
  { u32x4 v[2][2] = {};
    if (act) fetch_payload<2, 14>(v, rws, pv0, pv1, WS_H + tt0 * 14 * KB, 14 * KB, lds, lds0, L_H + 2 * w * KB, lane, [&] {
#pragma unroll
        for (int n = 0; n < 14; ++n) cs1 = mfma16(w1[n], ones, cs1); });
    row_stats<2>(v, ones, st4, w, l15, g); }
  STAMP(5);
  BARS();
  u32x4 w2[14];
  f32x2 yv[2][2];
  const unsigned w2b = WS_W2 + Fg * 14 * KB;
  {
    const LAS char* hpz = lds + L_H + l16;
    f32x4 pt[2];
#pragma unroll
    for (int t = 0; t < 2; ++t) pt[t] = *(const LAS f32x4*)(st4 + ((t * 16 + l15) * 8 + 2 * g) * 2);
    f32x4 acc[2] = {};
    lds_pipe<28, 8>([&](int n) { return lds128(hpz + ((n & 1) * 14 + (n >> 1)) * KB); }, [&](int n, u32x4 f) { acc[n & 1] = mfma16(w1[n >> 1], f, acc[n & 1]); },
      [&](int n) { if ((n & 3) == 3) { const int q = n >> 2; if (q < 3) bstore_sc1(sentv, rws, l16p, pbase + pc[3 + q] * 56 * KB); else aload128(w2[q - 3], rws, l16a, w2b + koff[q - 3]); } });
    u32x2 zp[2];
#pragma unroll
    for (int t = 0; t < 2; ++t) {
      const float s1 = gsum(pt[t][0] + pt[t][2]), s2 = gsum(pt[t][1] + pt[t][3]);
      const float mean = s1 * (1.f / D), var = fmaxf(s2 * (1.f / D) - mean * mean, 0.f);
      const float r1 = __builtin_amdgcn_rsqf(var + 1e-5f);
      const float r12 = r1 * __builtin_amdgcn_rsqf(var * r1 * r1 + 1e-5f);
      const f32x2 m2 = {mean, mean};
#pragma unroll
      for (int q = 0; q < 2; ++q) {
        yv[t][q] = (f32x2{bflo(hp[t][q]), bfhi(hp[t][q])} - m2) * f32x2{r1, r1};
        zp[t][q] = gelu2((f32x2{acc[t][2 * q], acc[t][2 * q + 1]} - m2 * f32x2{cs1[2 * q], cs1[2 * q + 1]}) * f32x2{r12, r12});
      }
    }
    bstore_sc1(pair_tiles(zp[0], zp[1]), rws, act_off, WS_Z + 14 * KB);
  }
  STAMP(18);
#pragma unroll
  for (int n = 4; n < 14; ++n) aload128(w2[n], rws, l16a, w2b + koff[n]);
  unsigned zv[2];
#pragma unroll
  for (int q = 0; q < 2; ++q) { const int ks = q ? kb14 : ka, f0 = 32 * ks + 8 * g, sh = f0 < 112 ? -1 : f0 < 224 ? 1 : f0 < 336 ? -2 : 2, tz = 16 * (tt0 + 1) + l15 + sh;
    zv[q] = (((tz >> 4) * 14 + ks) * 64 + g * 16 + (tz & 15)) * 16; }
  STAMP(6);
  WAIT_VM(0);
#pragma unroll
  for (int n = 0; n < 14; ++n) RETIRE(w2[n]);

  { u32x4 v[2][2];
    if (act) fetch_payload<2, 14>(v, rws, zv[0], zv[1], WS_Z, 14 * KB, lds, lds0, L_Z + 2 * w * KB, lane, [] {}); }
  STAMP(7);
  BARS();
  u32x4 w3[2][14];
  const unsigned w3b = WS_W3 + (16 * (j < 7 ? j : 6) + 2 * w) * 14 * KB;
  {
    f32x4 acc[2] = {};
    const LAS char* zb = lds + L_Z + l16;
    lds_pipe<28, 8>([&](int n) { return lds128(zb + ((n & 1) * 14 + (n >> 1)) * KB); }, [&](int n, u32x4 f) { acc[n & 1] = mfma16(w2[n >> 1], f, acc[n & 1]); },
      [&](int n) { if ((n & 3) == 3) { const int q = n >> 2; aload128(w3[q & 1][q >> 1], rws, l16p, w3b + (q & 1) * 14 * KB + koff[q >> 1]); } });
    u32x2 xp[2];
#pragma unroll
    for (int t = 0; t < 2; ++t) xp[t] = u32x2{pk2(yv[t][0][0] + acc[t][0], yv[t][0][1] + acc[t][1]), pk2(yv[t][1][0] + acc[t][2], yv[t][1][1] + acc[t][3])};
    bstore_sc1(pair_tiles(xp[0], xp[1]), rws, act_off, WS_X1);
  }
  STAMP(19);
  if (j == 7) { STAMP(8); ST_FLUSH(1, p.ws, b, w); return; }
#pragma unroll
  for (int q = 7; q < 14; ++q) aload128(w3[q & 1][q >> 1], rws, l16, w3b + (q & 1) * 14 * KB + koff[q >> 1]);
  STAMP(8);
  WAIT_VM(0);
#pragma unroll
  for (int n = 0; n < 7; ++n) { RETIRE(w3[0][n]); RETIRE(w3[1][n]); }

  f32x4 cs3[2] = {};
  LAS float* st6 = (LAS float*)(lds + L_ST + 2048);
  { u32x4 v[4][2] = {};
    if (act) fetch_payload<4, 14>(v, rws, pv0, pv1, WS_X1 + 4 * i * 14 * KB, 14 * KB, lds, lds0, L_X1 + 2 * w * KB, lane, [&] {
#pragma unroll
        for (int n = 0; n < 7; ++n) { cs3[0] = mfma16(w3[0][n], ones, cs3[0]); cs3[1] = mfma16(w3[1][n], ones, cs3[1]); } });
    else {
#pragma unroll
      for (int n = 0; n < 7; ++n) { cs3[0] = mfma16(w3[0][n], ones, cs3[0]); cs3[1] = mfma16(w3[1][n], ones, cs3[1]); } }
    row_stats<4>(v, ones, st6, w, l15, g); }
  STAMP(9);
  BARS();
  auto w4_piece = [&](int s, int q) {
    const unsigned c = s < 6 ? pc[s] : j; const unsigned slot = (s & 3) == 3 ? 131072u : (s & 3) * 32768u;
    dma16(rws, l16, WS_W4 + ((j * 7 + c) * 32 + 4 * w + q) * KB, lds0 + slot + (4 * w + q) * KB);
  };
  {
    const LAS char* xp = lds + L_X1 + l16;
    f32x4 pt[4];
#pragma unroll
    for (int t = 0; t < 4; ++t) pt[t] = *(const LAS f32x4*)(st6 + ((t * 16 + l15) * 8 + 2 * g) * 2);
    f32x4 acc[2][4] = {};
    u32x4 bfr[2][4];
#pragma unroll
    for (int t = 0; t < 4; ++t) bfr[0][t] = lds128(xp + (t * 14) * KB);
    SB();
#pragma unroll
    for (int n = 0; n < 14; ++n) {
      if (n < 13) {
#pragma unroll
        for (int t = 0; t < 4; ++t) bfr[(n + 1) & 1][t] = lds128(xp + (t * 14 + n + 1) * KB); }
      if (n >= 7) { WAIT_VM(12); RETIRE(w3[0][n]); RETIRE(w3[1][n]); }
#pragma unroll
      for (int t = 0; t < 4; ++t) { acc[0][t] = mfma16(w3[0][n], bfr[n & 1][t], acc[0][t]); acc[1][t] = mfma16(w3[1][n], bfr[n & 1][t], acc[1][t]); }
      if (n >= 7) { cs3[0] = mfma16(w3[0][n], ones, cs3[0]); cs3[1] = mfma16(w3[1][n], ones, cs3[1]); }
      if (n < 7) { aload128(w3[0][7 + n], rws, l16, w3b + koff[7 + n]); aload128(w3[1][7 + n], rws, l16, w3b + 14 * KB + koff[7 + n]); }
      else if (n < 13) { w4_piece((2 * (n - 7)) >> 2, (2 * (n - 7)) & 3); w4_piece((2 * (n - 7) + 1) >> 2, (2 * (n - 7) + 1) & 3); }
      SB();
    }
    STAMP(20);
    float mean[4], rs[4];
#pragma unroll
    for (int t = 0; t < 4; ++t) {
      const float s1 = gsum(pt[t][0] + pt[t][2]), s2 = gsum(pt[t][1] + pt[t][3]);
      mean[t] = s1 * (1.f / D); rs[t] = __builtin_amdgcn_rsqf(fmaxf(s2 * (1.f / D) - mean[t] * mean[t], 0.f) + 1e-5f);
    }
    u32x4 hv[4];
#pragma unroll
    for (int t = 0; t < 4; ++t)
#pragma unroll
      for (int q = 0; q < 4; ++q) { const int f = q >> 1, r = 2 * (q & 1);
        hv[t][q] = gelu2((f32x2{acc[f][t][r], acc[f][t][r + 1]} - f32x2{mean[t], mean[t]} * f32x2{cs3[f][r], cs3[f][r + 1]}) * f32x2{rs[t], rs[t]}); }
    STAMP(10);
    BARS();
#pragma unroll
    for (int t = 0; t < 4; ++t) *(LAS u32x4*)(lds + L_X1 + (t * 8 + w) * KB + l16) = hv[t];
  }
  WAIT_VM(0);
  BARS();
  STAMP(11);
#pragma unroll
  for (int q = 0; q < 4; ++q) w4_piece(3, q);
  const int tb = w & 1, ft = w >> 1;
  u32x4 hf[2][8];
#pragma unroll
  for (int ks = 0; ks < 8; ++ks)
#pragma unroll
    for (int t = 0; t < 2; ++t) hf[t][ks] = lds128(lds + L_X1 + ((2 * tb + t) * 8 + ks) * KB + l16);
  const int Fo = 4 * j + ft;
  u32x2 hres[2], xres[2]; u32x4 pv[6];
  unsigned ro[2];
#pragma unroll
  for (int t = 0; t < 2; ++t) ro[t] = (((4 * i + 2 * tb + t) * 14 + (Fo >> 1)) * 64 + (2 * (Fo & 1) + (g >> 1)) * 16 + l15) * 16 + (g & 1) * 8;
  const unsigned pin = WS_P + ((i * 7 + j) * 7 * 8 + w) * KB;
  auto final_loads = [&] {
#pragma unroll
    for (int t = 0; t < 2; ++t) { aload64_sc1(hres[t], rws, ro[t], WS_H); aload64_sc1(xres[t], rws, ro[t], WS_X1); }
#pragma unroll
    for (int s = 0; s < 6; ++s) { int js = j + 1 + s; js = js >= 7 ? js - 7 : js; aload128_sc1(pv[s], rws, l16, pin + js * 8 * KB); }
  };
  f32x4 acc[2];
#pragma unroll
  for (int s = 0; s < 7; ++s) {
    const unsigned slot = (s & 3) == 3 ? 131072u : (s & 3) * 32768u;
    const LAS char* wp = lds + slot + ft * 8 * KB + l16;
    acc[0] = f32x4{0.f, 0.f, 0.f, 0.f}; acc[1] = acc[0];
    lds_pipe<8, 6>([&](int ks) { return lds128(wp + ks * KB); }, [&](int ks, u32x4 av) { acc[0] = mfma16(av, hf[0][ks], acc[0]); acc[1] = mfma16(av, hf[1][ks], acc[1]); }, [](int) {});
    if (s < 6) {
      bstore_sc1(u32x4{pk2(acc[0][0], acc[0][1]), pk2(acc[0][2], acc[0][3]), pk2(acc[1][0], acc[1][1]), pk2(acc[1][2], acc[1][3])}, rws, l16, pbase + pc[s] * 56 * KB);
      if (s == 2) WAIT_VM(11); else if (s == 3) WAIT_VM(11); else if (s == 4) WAIT_VM(7); else if (s == 5) WAIT_VM(3);
      BARS();
      if (s < 3) {
#pragma unroll
        for (int q = 0; q < 4; ++q) w4_piece(s + 4, q); }
    }
  }
  STAMP(12);
  {
    const __amdgpu_buffer_rsrc_t rout = mk_rsrc(p.out, S * D * 2);
    WAIT_VM(0);
    for (unsigned spin = 0;; ++spin) {
      final_loads();
      WAIT_VM(0);
#pragma unroll
      for (int s = 0; s < 6; ++s) RETIRE(pv[s]);
      RETIRE(hres[0]); RETIRE(hres[1]); RETIRE(xres[0]); RETIRE(xres[1]);
      unsigned m = 0;
#pragma unroll
      for (int s = 0; s < 6; ++s) m = max(m, max(max(pv[s][0], pv[s][1]), max(pv[s][2], pv[s][3])));
#pragma unroll
      for (int t = 0; t < 2; ++t) m = max(m, max(max(hres[t][0], hres[t][1]), max(xres[t][0], xres[t][1])));
      if (!__any(m == SENT) || spin > SPIN_MAX) break;
    }
#pragma unroll
    for (int t = 0; t < 2; ++t) {
      f32x2 va = {acc[t][0], acc[t][1]}, vb = {acc[t][2], acc[t][3]};
#pragma unroll
      for (int s = 0; s < 6; ++s) { va += f32x2{bflo(pv[s][2 * t]), bfhi(pv[s][2 * t])}; vb += f32x2{bflo(pv[s][2 * t + 1]), bfhi(pv[s][2 * t + 1])}; }
      va += f32x2{bflo(xres[t][0]), bfhi(xres[t][0])} + f32x2{bflo(hres[t][0]), bfhi(hres[t][0])};
      vb += f32x2{bflo(xres[t][1]), bfhi(xres[t][1])} + f32x2{bflo(hres[t][1]), bfhi(hres[t][1])};
      __builtin_amdgcn_raw_buffer_store_b64(u32x2{pk2(va[0], va[1]), pk2(vb[0], vb[1])}, rout, ((16 * (4 * i + 2 * tb + t) + l15) * D + 16 * Fo + 4 * g) * 2, 0, 0);
    }
  }
  STAMP(13);
  ST_FLUSH(1, p.ws, b, w);
}

extern "C" void kernel_launch(void* const* d_in, const int* in_sizes, int n_in, void* d_out, int out_size, void* d_ws, size_t ws_size, hipStream_t stream) {
  Params p;
  p.x = (const u16*)d_in[0]; p.wqkv = (const u16*)d_in[1]; p.wo = (const u16*)d_in[2];
  p.w1 = (const u16*)d_in[4]; p.w2 = (const u16*)d_in[5]; p.w3 = (const u16*)d_in[6]; p.w4 = (const u16*)d_in[7];
  p.out = (u16*)d_out; p.ws = (char*)d_ws;
  (void)hipFuncSetAttribute((const void*)k1_qkv, hipFuncAttributeMaxDynamicSharedMemorySize, K1_LDS);
  (void)hipFuncSetAttribute((const void*)k2_rest, hipFuncAttributeMaxDynamicSharedMemorySize, K2_LDS);
  k1_qkv<<<256, 512, K1_LDS, stream>>>(p);
  k2_rest<<<256, 512, K2_LDS, stream>>>(p);
}
```

```cpp
#include <hip/hip_runtime.h>
#include <hip/hip_bf16.h>
#include <stdint.h>

typedef unsigned short u16;
typedef __attribute__((ext_vector_type(8))) __bf16 bf16x8;
typedef __attribute__((ext_vector_type(2))) __bf16 bf16x2;
typedef __attribute__((ext_vector_type(4))) short s16x4;
typedef __attribute__((ext_vector_type(4))) float f32x4;
typedef __attribute__((ext_vector_type(16))) float f32x16;
typedef __attribute__((ext_vector_type(4))) unsigned u32x4;
typedef __attribute__((ext_vector_type(2))) unsigned u32x2;
#define LAS __attribute__((address_space(3)))
#define GAS __attribute__((address_space(1)))
#define DI __device__ __forceinline__

constexpr int S = 2048, D = 448, NH = 16, DH = 28, F = 1792;
constexpr unsigned SENT = 0xFFFFFFFFu;

constexpr unsigned KB = 1024;
constexpr unsigned WS_Q   = 0;
constexpr unsigned WS_K   = WS_Q + 2048 * KB;
constexpr unsigned WS_V   = WS_K + 2048 * KB;
constexpr unsigned WS_WO  = WS_V + 2048 * KB;
constexpr unsigned WS_W1  = WS_WO + 448 * KB;
constexpr unsigned WS_W2  = WS_W1 + 392 * KB;
constexpr unsigned WS_W3  = WS_W2 + 392 * KB;
constexpr unsigned WS_W4  = WS_W3 + 1568 * KB;
constexpr unsigned WS_O   = WS_W4 + 1568 * KB;
constexpr unsigned WS_H   = WS_O + 2048 * KB;
constexpr unsigned WS_X1  = WS_H + 1792 * KB;
constexpr unsigned WS_Z   = WS_X1 + 1792 * KB;
constexpr unsigned WS_P   = WS_Z + 1820 * KB;
constexpr unsigned WS_CS  = WS_P + 32 * 49 * 8 * KB;
constexpr unsigned WS_CNT = WS_CS + 18 * KB;
constexpr unsigned WS_END = WS_CNT + 28 * KB;

struct Params {
  const u16* x; const u16* wqkv; const u16* wo; const u16* w1; const u16* w2; const u16* w3; const u16* w4;
  u16* out; char* ws;
};

DI __amdgpu_buffer_rsrc_t mk_rsrc(const void* p, unsigned bytes) { return __builtin_amdgcn_make_buffer_rsrc((void*)p, (short)0, (int)bytes, 0x00020000); }
DI void dma16(__amdgpu_buffer_rsrc_t rs, unsigned voff, unsigned soff, unsigned ldsaddr) {
  asm volatile("s_mov_b32 m0, %0\n\ts_nop 0\n\tbuffer_load_dwordx4 %1, %2, %3 offen lds" :: "s"(ldsaddr), "v"(voff), "s"(rs), "s"(soff) : "memory");
}
DI void dma16_sc1(__amdgpu_buffer_rsrc_t rs, unsigned voff, unsigned soff, unsigned ldsaddr) {
  asm volatile("s_mov_b32 m0, %0\n\ts_nop 0\n\tbuffer_load_dwordx4 %1, %2, %3 offen sc1 lds" :: "s"(ldsaddr), "v"(voff), "s"(rs), "s"(soff) : "memory");
}
DI void dma4(__amdgpu_buffer_rsrc_t rs, unsigned voff, unsigned soff, unsigned ldsaddr) {
  asm volatile("s_mov_b32 m0, %0\n\ts_nop 0\n\tbuffer_load_dwordx4 %1, %2, %3 offen lds\n\tbuffer_load_dwordx4 %1, %2, %3 offen offset:1024 lds\n\t"
               "buffer_load_dwordx4 %1, %2, %3 offen offset:2048 lds\n\tbuffer_load_dwordx4 %1, %2, %3 offen offset:3072 lds" :: "s"(ldsaddr), "v"(voff), "s"(rs), "s"(soff) : "memory");
}
DI void dma3(__amdgpu_buffer_rsrc_t rs, unsigned voff, unsigned soff, unsigned ldsaddr) {
  asm volatile("s_mov_b32 m0, %0\n\ts_nop 0\n\tbuffer_load_dwordx4 %1, %2, %3 offen lds\n\tbuffer_load_dwordx4 %1, %2, %3 offen offset:1024 lds\n\t"
               "buffer_load_dwordx4 %1, %2, %3 offen offset:2048 lds" :: "s"(ldsaddr), "v"(voff), "s"(rs), "s"(soff) : "memory");
}
#define WAIT_VM(n) asm volatile("s_waitcnt vmcnt(" #n ")" ::: "memory")
#define BAR() asm volatile("s_waitcnt lgkmcnt(0)\n\ts_barrier" ::: "memory")
#define BARS() do { __builtin_amdgcn_sched_barrier(0); BAR(); __builtin_amdgcn_sched_barrier(0); } while (0)
DI unsigned rfl(unsigned v) { return __builtin_amdgcn_readfirstlane(v); }
DI float bflo(unsigned u) { return __uint_as_float(u << 16); }
DI float bfhi(unsigned u) { return __uint_as_float(u & 0xffff0000u); }
DI unsigned pk2(float a, float b) { bf16x2 v; v[0] = (__bf16)a; v[1] = (__bf16)b; return __builtin_bit_cast(unsigned, v); }
DI float dot2(unsigned a, unsigned b, float c) { return __builtin_amdgcn_fdot2_f32_bf16(__builtin_bit_cast(bf16x2, a), __builtin_bit_cast(bf16x2, b), c, false); }
DI f32x16 mfma32(u32x4 a, u32x4 b, f32x16 c) { return __builtin_amdgcn_mfma_f32_32x32x16_bf16(__builtin_bit_cast(bf16x8, a), __builtin_bit_cast(bf16x8, b), c, 0, 0, 0); }
DI f32x4 mfma16(u32x4 a, u32x4 b, f32x4 c) { return __builtin_amdgcn_mfma_f32_16x16x32_bf16(__builtin_bit_cast(bf16x8, a), __builtin_bit_cast(bf16x8, b), c, 0, 0, 0); }
DI u32x2 tr_read(const LAS char* p) { return __builtin_bit_cast(u32x2, __builtin_amdgcn_ds_read_tr16_b64_v4i16((LAS s16x4*)p)); }
DI u32x4 lds128(const LAS char* p) { return *(const LAS u32x4*)p; }
template <int AUX> DI void bst128(u32x4 v, __amdgpu_buffer_rsrc_t rs, unsigned off) { __builtin_amdgcn_raw_buffer_store_b128(v, rs, off, 0, AUX); }
#define XJ j
constexpr unsigned ONES = 0x3F803F80u;
#define AUX_SIDE 0
#define AUX_QKV 0
#define ST_DECL
#define CSTAMP(n)
#define STAMP(n)
#define ST_FLUSH(kern, ws, b, w)

constexpr unsigned CODE_K1 = 26 * 1024, CODE_K2_OFF = 27904, CODE_K2 = 25 * 1024;
DI unsigned touch_code(unsigned long long base, unsigned bytes, int b, int lane) {
  const __amdgpu_buffer_rsrc_t rc = mk_rsrc((const void*)base, bytes);
  const unsigned per = (bytes / 128 + 31) / 32, line = (b >> 3) * per + lane;
  unsigned d;
  asm volatile("buffer_load_dword %0, %1, %2, 0 offen" : "=v"(d) : "v"(lane < (int)per ? line * 128 : 0xF0000000u), "s"(rc) : "memory");
  return d;
}
DI unsigned long long code_base() { unsigned long long pc; asm volatile("s_getpc_b64 %0" : "=s"(pc)); return pc & ~0xFFull; }

#define XMAP 2
DI void map_block(int b, int& i, int& j) { const int x = b & 7, s = b >> 3; constexpr int JPX = 8 / XMAP; i = (x / XMAP) * 4 * XMAP + s / JPX; j = (x % XMAP) * JPX + s % JPX; }

constexpr unsigned K1_XS = 0;
constexpr unsigned K1_W0 = 7 * 8192;
constexpr unsigned K1_SLOT = 24576;
constexpr unsigned K1_ST = K1_W0 + 4 * K1_SLOT;
constexpr unsigned K1_CS = K1_ST + 512;
constexpr unsigned K1_LDS = K1_CS + 2 * 2 * 3 * 32 * 4;

DI void retile_load(const Params& p, int mat, int ks, int cb, int lane, u32x4 (&v)[4]) {
  const u16* W = mat == 0 ? p.wo : mat == 1 ? p.w1 : mat == 2 ? p.w2 : mat == 3 ? p.w3 : p.w4;
  const unsigned ldw = mat == 3 ? F : D, nrows = mat == 4 ? F : D, krow0 = (mat == 0 ? 28 : 32) * ks;
#pragma unroll
  for (int q = 0; q < 4; ++q) {
    unsigned row = krow0 + (lane >> 3) + 8 * q; row = row < nrows ? row : nrows - 1;
    v[q] = *(const u32x4*)(W + (size_t)row * ldw + cb * 64 + (lane & 7) * 8);
  }
}
template <bool SUMS>
DI void retile_store(const Params& p, LAS char* scr, int mat, int ks, int cb, int lane, const u32x4 (&v)[4], f32x4 (&cs)[4]) {
#pragma unroll
  for (int q = 0; q < 4; ++q) *(LAS u32x4*)(scr + q * 1024 + lane * 16) = v[q];
  const int g = lane >> 4, q4 = (lane & 15) >> 2, pp = lane & 3;
  const int r0 = (mat == 4 ? 4 * g : 8 * g) + q4, r1 = (mat == 4 ? 16 + 4 * g : 8 * g + 4) + q4;
  const __amdgpu_buffer_rsrc_t rws = mk_rsrc(p.ws, WS_END);
  const u32x4 ones = {ONES, ONES, ONES, ONES};
#pragma unroll
  for (int ft = 0; ft < 4; ++ft) {
    u32x2 lo = tr_read(scr + r0 * 128 + ft * 32 + pp * 8), hi = tr_read(scr + r1 * 128 + ft * 32 + pp * 8);
    if (mat == 0 && g == 3) { hi[0] = 0u; hi[1] = 0u; }
    const u32x4 f = {lo[0], lo[1], hi[0], hi[1]};
    const int Fi = 4 * cb + ft;
    unsigned dst;
    if (mat == 0) dst = WS_WO + (((Fi / 7) * 16 + ks) * 7 + Fi % 7) * KB;
    else if (mat <= 2) dst = (mat == 1 ? WS_W1 : WS_W2) + (((Fi / 7) * 14 + ks) * 7 + Fi % 7) * KB;
    else if (mat == 3) dst = WS_W3 + (((Fi >> 4) * 14 + ks) * 16 + (Fi & 15)) * KB;
    else dst = WS_W4 + ((((ks >> 3) * 7 + cb) * 4 + ft) * 8 + (ks & 7)) * KB;
    bst128<AUX_SIDE>(f, rws, lane * 16 + dst);
    if (SUMS) cs[ft] = mfma16(f, ones, cs[ft]);
  }
}

__global__ void __launch_bounds__(512, 2) k1_qkv(Params p) {
  extern __shared__ __attribute__((aligned(16))) char smem[];
  LAS char* lds = (LAS char*)smem;
  const unsigned lds0 = (unsigned)(size_t)lds;
  const int b = blockIdx.x, tid = threadIdx.x, lane = tid & 63;
  const int w = rfl(tid >> 6);
  const unsigned long long cbase = code_base();
  int i, j; map_block(b, i, j);
  ST_DECL; STAMP(0);
  if (w == 7) { unsigned d0 = touch_code(cbase, CODE_K1, b, lane), d1 = touch_code(cbase + CODE_K2_OFF, CODE_K2, b, lane); WAIT_VM(0); asm volatile("" :: "v"(d0), "v"(d1)); }

  if (j == 7) {
    const int gw = i * 8 + w;
    const int colid = w < 2 ? w * 32 + i : (w == 2 && i < 6) ? 64 + i : -1;
    const int nid = w >= 3 ? i * 5 + (w - 3) : 160 + (i - 6);
    u32x4 v[7][4];
    auto other = [&](int m, int& mat, int& ks, int& cb) { if (m < 112) { mat = 0; ks = m / 7; cb = m % 7; } else if (m < 210) { mat = 2; ks = (m - 112) / 7; cb = (m - 112) % 7; } else { mat = 4; ks = (m - 210) / 7; cb = (m - 210) % 7; } };
    const int ccol = colid >> 1, chalf = colid & 1, cmat = ccol < 7 ? 1 : 3, ccb = ccol < 7 ? ccol : ccol - 7;
    if (colid >= 0) {
#pragma unroll
      for (int q = 0; q < 7; ++q) retile_load(p, cmat, 7 * chalf + q, ccb, lane, v[q]);
    } else {
#pragma unroll
      for (int q = 0; q < 4; ++q) { const int m = nid + 186 * q; if (m < 602) { int mat, ks, cb; other(m, mat, ks, cb); retile_load(p, mat, ks, cb, lane, v[q]); } }
    }
    const u32x4 sv = {SENT, SENT, SENT, SENT}, zv = {0u, 0u, 0u, 0u};
    const __amdgpu_buffer_rsrc_t rws = mk_rsrc(p.ws, WS_END);
    {
      const int pr = gw * 4 + (lane >> 4), gq = (lane >> 2) & 3, row = lane & 3;
      const unsigned o = row == 0 ? ONES : 0u;
      bst128<AUX_SIDE>(u32x4{o, o, o, o}, rws, WS_V + ((pr * 2 + 1) * 64 + gq * 16 + 12 + row) * 16);
    }
#pragma unroll
    for (int q = 0; q < 4; ++q) { const int pc = (gw * 4 + q) * 64 + lane;
      __builtin_amdgcn_raw_buffer_store_b64(u32x2{0u, 0u}, rws, ((pc >> 4) * 64 + 48 + (pc & 15)) * 16 + 8, WS_Q, AUX_SIDE); }
    if (gw < 28) bst128<AUX_SIDE>(zv, rws, lane * 16 + WS_Z + (gw < 14 ? gw : 129 * 14 + gw - 14) * KB);
    {
      if (gw < 224) { const int pc = gw * 64 + lane, tok = pc & 1, gq = (pc >> 1) & 3, rest = pc >> 3, ks = rest % 14, ce = rest / 14, edge = ce & 1, cl = ce >> 1;
        const int tt = 2 * cl + edge + 1;
        bst128<AUX_SIDE>(sv, rws, WS_Z + ((tt * 14 + ks) * 64 + gq * 16 + (edge ? 14 : 0) + tok) * 16); }
    }
    if (gw < 28) bst128<AUX_SIDE>(zv, rws, WS_CNT + gw * KB + lane * 16);
    f32x4 cs[4] = {};
    if (colid >= 0) {
#pragma unroll
      for (int q = 0; q < 7; ++q) retile_store<true>(p, lds + w * 4096, cmat, 7 * chalf + q, ccb, lane, v[q], cs);
      if ((lane & 15) == 0) {
#pragma unroll
        for (int ft = 0; ft < 4; ++ft) *(f32x4*)(p.ws + WS_CS + (chalf * 2240 + (cmat == 1 ? 0 : 448) + 64 * ccb + 16 * ft + 4 * (lane >> 4)) * 4) = cs[ft]; }
    } else {
#pragma unroll
      for (int q = 0; q < 4; ++q) { const int m = nid + 186 * q; if (m < 602) { int mat, ks, cb; other(m, mat, ks, cb); retile_store<false>(p, lds + w * 4096, mat, ks, cb, lane, v[q], cs); } }
    }
    STAMP(7); ST_FLUSH(0, p.ws, b, w);
    return;
  }

  if (w >= 6) {
    {
      const u32x4 sv = {SENT, SENT, SENT, SENT}; const __amdgpu_buffer_rsrc_t rws = mk_rsrc(p.ws, WS_END);
      for (int kb = (i * 7 + j) * 2 + (w - 6); kb < 2048; kb += 448) bst128<AUX_SIDE>(sv, rws, lane * 16 + WS_O + kb * KB); }
    const int tok = 32 * (w - 6) + (lane & 31), part = lane >> 5;
    const LAS char* xb[4];
#pragma unroll
    for (int q = 0; q < 4; ++q) xb[q] = lds + K1_XS + tok * 128 + ((4 * part + q) ^ ((tok >> 1) & 7)) * 16;
    float s1a = 0.f, s1b = 0.f, s2a = 0.f, s2b = 0.f;
    BARS();
#pragma unroll
    for (int c = 0; c < 7; ++c) {
      u32x4 v[4];
#pragma unroll
      for (int q = 0; q < 4; ++q) v[q] = lds128(xb[q] + c * 8192);
      __builtin_amdgcn_sched_barrier(0);
#pragma unroll
      for (int q = 0; q < 4; ++q)
#pragma unroll
        for (int u = 0; u < 4; ++u) { float lo = bflo(v[q][u]), hi = bfhi(v[q][u]); asm volatile("" : "+v"(lo), "+v"(hi));
          s1a += lo; s1b += hi; s2a = fmaf(lo, lo, s2a); s2b = fmaf(hi, hi, s2b); }
      asm volatile("" : "+v"(s1a), "+v"(s1b), "+v"(s2a), "+v"(s2b));
      if (c == 6) {
        float a1 = s1a + s1b, a2 = s2a + s2b;
        a1 += __shfl_xor(a1, 32); a2 += __shfl_xor(a2, 32);
        const float mean = a1 * (1.f / D), var = fmaxf(a2 * (1.f / D) - mean * mean, 0.f);
        if (lane < 32) { *(LAS float*)(lds + K1_ST + tok * 4) = mean; *(LAS float*)(lds + K1_ST + 256 + tok * 4) = __builtin_amdgcn_rsqf(var + 1e-5f); }
      }
      BARS();
    }
    STAMP(7); ST_FLUSH(0, p.ws, b, w);
    return;
  }
  if (w >= 4) {
    const int lw = w - 4;
    const __amdgpu_buffer_rsrc_t rx = mk_rsrc(p.x, S * D * 2), rw = mk_rsrc(p.wqkv, D * 3 * D * 2);
    const unsigned xv0 = (lane >> 3) * 896 + ((lane & 7) ^ (lane >> 4)) * 16, xv1 = (lane >> 3) * 896 + ((lane & 7) ^ (lane >> 4) ^ 4) * 16;
    const unsigned wv = (lane >> 3) * 2688 + (((lane & 7) ^ (((lane >> 4) & 1) * 4)) * 16);
    auto issue_chunk = [&](int c) {
#pragma unroll
      for (int q = 0; q < 4; ++q) { const unsigned n = lw * 4 + q; dma16(rx, (q & 1) ? xv1 : xv0, (64 * i + 8 * n) * 896 + c * 128, lds0 + K1_XS + c * 8192 + n * 1024); }
#pragma unroll
      for (int q = 0; q < 12; ++q) { const unsigned n = lw * 12 + q, type = n >> 3, rg = n & 7;
        dma16(rw, wv, (64 * c + 8 * rg) * 2688 + type * 896 + j * 128, lds0 + K1_W0 + (c & 3) * K1_SLOT + type * 8192 + rg * 1024); }
    };
    issue_chunk(0); issue_chunk(1);
    STAMP(1);
    WAIT_VM(16); STAMP(2); BARS();
    issue_chunk(2); issue_chunk(3);
    WAIT_VM(32); STAMP(3); BARS();
    issue_chunk(4);
    WAIT_VM(32); STAMP(8); BARS();
    issue_chunk(5);
    WAIT_VM(32); STAMP(9); BARS();
    issue_chunk(6);
    WAIT_VM(32); STAMP(10); BARS();
    WAIT_VM(16); STAMP(11); BARS();
    WAIT_VM(0); STAMP(5); BARS();
    BAR();
    STAMP(7); ST_FLUSH(0, p.ws, b, w);
    return;
  }
  const int tb = w & 1, e = w >> 1, r = lane & 31, h = lane >> 5;
  f32x16 aq = {}, ak = {}, av = {};
  f32x4 cq = {}, ck = {}, cv = {};
  const unsigned cs1 = ((lane & 15) == ((lane >> 4) & 1)) ? ONES : 0u;
  const u32x4 csel = {cs1, cs1, cs1, cs1};
  const int q4 = (lane & 15) >> 2, pp = lane & 3, blk = (lane >> 4) & 1;
  const LAS char* xb[4];
#pragma unroll
  for (int kl = 0; kl < 4; ++kl) xb[kl] = lds + K1_XS + (32 * tb + r) * 128 + ((2 * (kl ^ tb) + h) ^ ((r >> 1) & 7)) * 16;
  const unsigned wb = K1_W0 + (8 * h + q4) * 128 + (((8 * e + 4 * blk + pp) ^ (8 * ((q4 >> 1) & 1))) * 8);
  const LAS char* we = lds + wb + tb * 2048;
  const LAS char* wo = lds + wb + (1 - tb) * 2048;
  u32x4 xf[3], f[3][3];
  auto load_step = [&](int n) {
    xf[n % 3] = lds128(xb[n & 3] + (n >> 2) * 8192);
    const LAS char* wp = ((n & 1) ? wo : we) + ((n >> 2) & 3) * K1_SLOT + (n & 2) * 2048;
#pragma unroll
    for (int t = 0; t < 3; ++t) { const u32x2 lo = tr_read(wp + t * 8192), hi = tr_read(wp + t * 8192 + 512); f[n % 3][t] = u32x4{lo[0], lo[1], hi[0], hi[1]}; }
  };
  const __amdgpu_buffer_rsrc_t rws = mk_rsrc(p.ws, WS_END);
  unsigned qoff[4], voff;
  { const unsigned tt = 4 * i + 2 * tb + (r >> 4);
#pragma unroll
    for (int g4 = 0; g4 < 4; ++g4) { const unsigned n0 = 64 * j + 32 * e + 8 * g4 + 4 * h, head = n0 / 28, d0 = n0 - 28 * head;
      qoff[g4] = WS_Q + ((head * 128 + tt) * 64 + (d0 >> 3) * 16 + (r & 15)) * 16 + (d0 & 4) * 2;
      asm volatile("" : "+v"(qoff[g4])); }
    const unsigned n = 64 * j + 32 * e + r, head = n / 28, d = n - 28 * head;
    voff = WS_V + (((head * 64 + 2 * i + tb) * 2 + (d >> 4)) * 64 + h * 16 + (d & 15)) * 16; asm volatile("" : "+v"(voff)); }
  STAMP(1);
  BARS();
  STAMP(2);
  load_step(0);
#pragma unroll
  for (int n = 0; n < 28; ++n) {
    if ((n & 3) == 3 && n < 27) { if (n == 7) STAMP(8); if (n == 11) STAMP(10); BARS(); if (n == 7) { STAMP(9); CSTAMP(13); } if (n == 11) { STAMP(11); CSTAMP(14); } if (n == 15) STAMP(12); }
    if (n < 27) load_step(n + 1);
    aq = mfma32(f[n % 3][0], xf[n % 3], aq);
    ak = mfma32(f[n % 3][1], xf[n % 3], ak);
    av = mfma32(xf[n % 3], f[n % 3][2], av);
    if (!(n & 1)) {
      cq = mfma16(f[n % 3][0], csel, cq); ck = mfma16(f[n % 3][1], csel, ck); cv = mfma16(f[n % 3][2], csel, cv);
    }
    __builtin_amdgcn_sched_group_barrier(0x8, 1, 0); __builtin_amdgcn_sched_group_barrier(0x100, 3, 0);
    __builtin_amdgcn_sched_group_barrier(0x8, 1, 0); __builtin_amdgcn_sched_group_barrier(0x100, 2, 0);
    __builtin_amdgcn_sched_group_barrier(0x8, 1, 0); __builtin_amdgcn_sched_group_barrier(0x100, 2, 0);
    __builtin_amdgcn_sched_group_barrier(0x8, 3, 0);
    __builtin_amdgcn_sched_barrier(0);
  }
  STAMP(4);
  LAS float* cs = (LAS float*)(lds + K1_CS) + e * 192;
  if ((lane & 15) < 2) { LAS float* o = cs + tb * 96 + 16 * (lane & 1) + 4 * (lane >> 4);
    *(LAS f32x4*)o = cq; *(LAS f32x4*)(o + 32) = ck; *(LAS f32x4*)(o + 64) = cv; }
  BAR();
  STAMP(5);
  const LAS float* st = (const LAS float*)(lds + K1_ST);
  f32x4 cqk[2][4][2], m4[4], r4[4];
#pragma unroll
  for (int t = 0; t < 2; ++t)
#pragma unroll
    for (int g4 = 0; g4 < 4; ++g4) { cqk[t][g4][0] = *(const LAS f32x4*)(cs + t * 32 + 8 * g4 + 4 * h); cqk[t][g4][1] = *(const LAS f32x4*)(cs + 96 + t * 32 + 8 * g4 + 4 * h); }
  const float mean = st[32 * tb + r], rstd = st[64 + 32 * tb + r];
  const float cs2 = cs[64 + r] + cs[96 + 64 + r];
#pragma unroll
  for (int g4 = 0; g4 < 4; ++g4) { m4[g4] = *(const LAS f32x4*)(st + 32 * tb + 8 * g4 + 4 * h); r4[g4] = *(const LAS f32x4*)(st + 64 + 32 * tb + 8 * g4 + 4 * h); }
  __builtin_amdgcn_sched_barrier(0);
#pragma unroll
  for (int g4 = 0; g4 < 4; ++g4)
#pragma unroll
    for (int t = 0; t < 2; ++t) {
      const f32x16& a = t ? ak : aq;
      float v[4];
#pragma unroll
      for (int q = 0; q < 4; ++q) v[q] = rstd * (a[4 * g4 + q] - mean * (cqk[t][g4][0][q] + cqk[t][g4][1][q]));
      __builtin_amdgcn_raw_buffer_store_b64(u32x2{pk2(v[0], v[1]), pk2(v[2], v[3])}, rws, qoff[g4], t * (WS_K - WS_Q), AUX_QKV);
    }
  {
    float v[16];
#pragma unroll
    for (int g4 = 0; g4 < 4; ++g4)
#pragma unroll
      for (int q = 0; q < 4; ++q) v[4 * g4 + q] = r4[g4][q] * (av[4 * g4 + q] - m4[g4][q] * cs2);
    const u32x4 oa = {pk2(v[0], v[1]), pk2(v[2], v[3]), pk2(v[8], v[9]), pk2(v[10], v[11])};
    const u32x4 ob = {pk2(v[4], v[5]), pk2(v[6], v[7]), pk2(v[12], v[13]), pk2(v[14], v[15])};
    bst128<AUX_QKV>(oa, rws, voff);
    bst128<AUX_QKV>(ob, rws, voff + 512);
  }
  STAMP(6);
  STAMP(7); ST_FLUSH(0, p.ws, b, w);
}

constexpr unsigned L_K = 0, L_WOA = 40 * KB, L_V = 120 * KB, L_WOB = 0;
constexpr unsigned L_O = 128 * KB;
constexpr unsigned L_W = 0;
constexpr unsigned L_H = 98 * KB, L_ST4 = 126 * KB;
constexpr unsigned L_Z = 128 * KB;
constexpr unsigned L_X1 = 96 * KB, L_ST6 = 152 * KB;
constexpr unsigned L_HID = 96 * KB;
constexpr unsigned L_RA = 0, L_RB = 32 * KB, L_RC = 64 * KB, L_RD = 128 * KB;
constexpr unsigned K2_LDS = 163840;
constexpr unsigned SPIN_MAX = 1u << 16;

DI u32x4 bload(__amdgpu_buffer_rsrc_t rs, unsigned voff, unsigned soff) { return __builtin_amdgcn_raw_buffer_load_b128(rs, voff, soff, 0); }
DI u32x4 bload_sc1(__amdgpu_buffer_rsrc_t rs, unsigned voff, unsigned soff) { return __builtin_amdgcn_raw_buffer_load_b128(rs, voff, soff, 16); }
DI void bstore_sc1(u32x4 v, __amdgpu_buffer_rsrc_t rs, unsigned voff, unsigned soff) { bst128<16>(v, rs, voff + soff); }
DI unsigned umax4(u32x4 v) { return max(max(v[0], v[1]), max(v[2], v[3])); }
#define FENCE() asm volatile("" ::: "memory")
typedef __attribute__((ext_vector_type(2))) float f32x2_;
DI float gelu_tanh(float x) {
  const float e = __builtin_amdgcn_exp2f(x * fmaf(x * x, -0.10294324f, -2.3022082f));
  return x * __builtin_amdgcn_rcpf(1.f + e);
}
DI u32x4 pair_tiles(u32x2 a, u32x2 b) {
  const auto s0 = __builtin_amdgcn_permlane16_swap(a[0], b[0], false, false), s1 = __builtin_amdgcn_permlane16_swap(a[1], b[1], false, false);
  return u32x4{s0[0], s1[0], s0[1], s1[1]};
}
template <int NT, int NS, class F>
DI void fetch_payload(u32x4 (&v)[NT][2], __amdgpu_buffer_rsrc_t rs, unsigned voff0, unsigned voff1, unsigned soff, unsigned tstride, LAS char* lds, unsigned lds0, unsigned dst, int lane, F fill, void* dbg = nullptr) {
  for (unsigned spin = 0;; ++spin) {
#pragma unroll
    for (int t = 0; t < NT; ++t) { dma16_sc1(rs, voff0, soff + t * tstride, lds0 + dst + t * NS * KB); dma16_sc1(rs, voff1, soff + t * tstride, lds0 + dst + (t * NS + 1) * KB); }
    if (spin == 0) { fill(); __builtin_amdgcn_sched_barrier(0); }
    WAIT_VM(0);
    unsigned m = 0;
#pragma unroll
    for (int t = 0; t < NT; ++t)
#pragma unroll
      for (int q = 0; q < 2; ++q) { v[t][q] = lds128(lds + dst + (t * NS + q) * KB + lane * 16); m = max(m, max(max(v[t][q][0], v[t][q][1]), max(v[t][q][2], v[t][q][3]))); }
    if (!__any(m == SENT) || spin > SPIN_MAX) break;
  }
}
template <int NT> DI void row_stats(const u32x4 (&v)[NT][2], u32x4 ones, LAS float* stp, int w, int l15, int g) {
#pragma unroll
  for (int t = 0; t < NT; ++t) {
    f32x4 d1 = {}, d2 = {};
#pragma unroll
    for (int q = 0; q < 2; ++q) { d1 = __builtin_amdgcn_mfma_f32_16x16x32_bf16(__builtin_bit_cast(bf16x8, ones), __builtin_bit_cast(bf16x8, v[t][q]), d1, 0, 0, 0);
                                  d2 = __builtin_amdgcn_mfma_f32_16x16x32_bf16(__builtin_bit_cast(bf16x8, v[t][q]), __builtin_bit_cast(bf16x8, v[t][q]), d2, 0, 0, 0); }
    const float lo = (l15 & 1) ? d2[1] : d2[0], hi = (l15 & 1) ? d2[3] : d2[2], s2 = (l15 & 2) ? hi : lo;
    if (g == (l15 >> 2)) *(LAS f32x2_*)(stp + ((t * 16 + l15) * 8 + w) * 2) = f32x2_{d1[0], s2};
  }
}

DI void aload128(u32x4& d, __amdgpu_buffer_rsrc_t rs, unsigned voff, unsigned soff) { asm volatile("buffer_load_dwordx4 %0, %1, %2, %3 offen" : "=v"(d) : "v"(voff), "s"(rs), "s"(soff) : "memory"); }
DI void aload64(u32x2& d, __amdgpu_buffer_rsrc_t rs, unsigned voff, unsigned soff) { asm volatile("buffer_load_dwordx2 %0, %1, %2, %3 offen" : "=v"(d) : "v"(voff), "s"(rs), "s"(soff) : "memory"); }
DI void aload128_sc1(u32x4& d, __amdgpu_buffer_rsrc_t rs, unsigned voff, unsigned soff) { asm volatile("buffer_load_dwordx4 %0, %1, %2, %3 offen sc1" : "=v"(d) : "v"(voff), "s"(rs), "s"(soff) : "memory"); }
DI void aload64_sc1(u32x2& d, __amdgpu_buffer_rsrc_t rs, unsigned voff, unsigned soff) { asm volatile("buffer_load_dwordx2 %0, %1, %2, %3 offen sc1" : "=v"(d) : "v"(voff), "s"(rs), "s"(soff) : "memory"); }
#define RETIRE(x) asm volatile("; RETIRE %0" : "+v"(x))
#define SB() __builtin_amdgcn_sched_barrier(0)
typedef __attribute__((ext_vector_type(2))) float f32x2;
DI float max3(float a, float b, float c) { float r; asm("v_max3_f32 %0, %1, %2, %3" : "=v"(r) : "v"(a), "v"(b), "v"(c)); return r; }
DI float gsum(float v) {
  const auto a = __builtin_amdgcn_permlane16_swap(__float_as_uint(v), __float_as_uint(v), false, false); const float s = __uint_as_float(a[0]) + __uint_as_float(a[1]);
  const auto c = __builtin_amdgcn_permlane32_swap(__float_as_uint(s), __float_as_uint(s), false, false); return __uint_as_float(c[0]) + __uint_as_float(c[1]);
}
DI float gmax(float v) {
  const auto a = __builtin_amdgcn_permlane16_swap(__float_as_uint(v), __float_as_uint(v), false, false); const float s = max3(__uint_as_float(a[0]), __uint_as_float(a[1]), __uint_as_float(a[1]));
  const auto c = __builtin_amdgcn_permlane32_swap(__float_as_uint(s), __float_as_uint(s), false, false); return max3(__uint_as_float(c[0]), __uint_as_float(c[1]), __uint_as_float(c[1]));
}
DI unsigned gelu2(f32x2 x) {
  const f32x2 t = x * (x * x * f32x2{-0.10294324f, -0.10294324f} + f32x2{-2.3022082f, -2.3022082f});
  const f32x2 d = f32x2{__builtin_amdgcn_exp2f(t[0]), __builtin_amdgcn_exp2f(t[1])} + f32x2{1.f, 1.f};
  const f32x2 r = x * f32x2{__builtin_amdgcn_rcpf(d[0]), __builtin_amdgcn_rcpf(d[1])};
  return pk2(r[0], r[1]);
}
template <int N, int PD, class L, class U, class M> DI void lds_pipe(L load, U use, M mid) {
  u32x4 buf[PD + 1];
#pragma unroll
  for (int n = 0; n < PD; ++n) buf[n] = load(n);
  SB();
#pragma unroll
  for (int n = 0; n < N; ++n) {
    if (n + PD < N) buf[(n + PD) % (PD + 1)] = load(n + PD);
    SB();
    use(n, buf[n % (PD + 1)]);
    mid(n);
    SB();
  }
}

__global__ void __launch_bounds__(640, 3) k2_rest(Params p) {
  extern __shared__ __attribute__((aligned(16))) char smem[];
  LAS char* lds = (LAS char*)smem;
  const unsigned lds0 = (unsigned)(size_t)lds;
  const int b = blockIdx.x, tid = threadIdx.x, lane = tid & 63;
  const int w = rfl(tid >> 6);
  const unsigned long long cbase = code_base();
  int i, j; map_block(b, i, j);
  ST_DECL; STAMP(0);
  const __amdgpu_buffer_rsrc_t rws = mk_rsrc(p.ws, WS_END);
  const int l15 = lane & 15, g = lane >> 4;
  const unsigned l16 = lane * 16;
  constexpr unsigned OOB = 0xF0000000u;
  const int t32 = 2 * i + (j >> 2), c4 = j & 3, tt0 = 2 * t32;
  const int sid = XMAP == 1 ? (((i & 3) << 1) | (j >> 2)) : XMAP == 2 ? (i & 7) : ((i & 15) >> 1);
  const int rot14 = (sid * 7) >> 2, rot16 = 2 * sid, rotc = XMAP == 1 ? ((i & 3) * 3) >> 1 : (i & 7) % 6;
  auto pcol = [&](int s) { int u = s + rotc; u = u >= 6 ? u - 6 : u; int c = j + 1 + u; return c >= 7 ? c - 7 : c; };
  const u32x4 sentv = {SENT, SENT, SENT, SENT};

  if (w == 7) { unsigned d0 = touch_code(cbase, CODE_K2, b, lane); WAIT_VM(0); asm volatile("" :: "v"(d0)); }
  if (w >= 8) {
    const int lw = w - 8;
#pragma unroll
    for (int q = 0; q < 5; ++q) { const int n = 2 * q + lw, hh = n / 5, kt = 4 * i - 16 + 4 * (n % 5);
      dma4(rws, l16, WS_K + ((2 * j + hh) * 128 + (kt < 0 ? 0 : kt)) * KB, lds0 + L_K + n * 4 * KB); }
#pragma unroll
    for (int q = 0; q < 5; ++q) { const int n = 2 * q + lw, hh = n / 5, kb = 2 * i - 8 + 2 * (n % 5);
      dma4(rws, l16, WS_V + ((2 * j + hh) * 64 + (kb < 0 ? 0 : kb)) * 2 * KB, lds0 + L_V + n * 4 * KB); }
    STAMP(1); WAIT_VM(20); STAMP(2); BARS();
    {
      const unsigned F0 = 7 * c4, o = ((tt0 + lw) * 14 + (F0 >> 1)) * KB + (F0 & 1) * 512 + l16;
#pragma unroll
      for (int u = 0; u < 3; ++u) { const unsigned base = u == 0 ? WS_H : u == 1 ? WS_X1 : WS_Z + 14 * KB;
        bst128<16>(sentv, rws, o + base); bst128<16>(sentv, rws, o + base + KB); bst128<16>(sentv, rws, o + base + 2 * KB); bst128<16>(sentv, rws, lane < 32 ? o + base + 3 * KB : OOB); }
    }
    FENCE();
    auto wo_slot = [&](int q) { const int n = 2 * q + lw; const unsigned src = WS_WO + (c4 * 16 + ((n + rot16) & 15)) * 7 * KB, dst = lds0 + (n <= 10 ? L_WOA + n * 7 * KB : L_WOB + (n - 11) * 7 * KB);
      dma4(rws, l16, src, dst); dma3(rws, l16, src + 4 * KB, dst + 4 * KB); };
    wo_slot(0); wo_slot(1);
    STAMP(3); WAIT_VM(26); STAMP(4); BARS(); STAMP(5);
#pragma unroll
    for (int q = 2; q < 8; ++q) wo_slot(q);
    STAMP(6); WAIT_VM(0); STAMP(7); BARS(); STAMP(8);
    auto w_stage = [&](unsigned wsrc, int q0, int q1) {
#pragma unroll
      for (int q = q0; q < q1; ++q) { const int n = 2 * q + lw; int ks = n + rot14; ks = ks >= 14 ? ks - 14 : ks; const unsigned src = wsrc + (c4 * 14 + ks) * 7 * KB, dst = lds0 + L_W + n * 7 * KB;
        dma4(rws, l16, src, dst); dma3(rws, l16, src + 4 * KB, dst + 4 * KB); } };
    BARS(); STAMP(9);
    w_stage(WS_W1, 0, 2);
    BARS();
    w_stage(WS_W1, 2, 7);
    STAMP(10); WAIT_VM(0); STAMP(11); BARS(); STAMP(12);
    BARS(); STAMP(13);
    w_stage(WS_W2, 0, 2);
    BARS();
    w_stage(WS_W2, 2, 7);
    STAMP(14); WAIT_VM(0); STAMP(15); BARS(); STAMP(16);
    BARS(); STAMP(17);
    if (j == 7) { BARS(); BARS(); return; }
    auto w3_round = [&](int r, unsigned slot) { int ks = 2 * r + lw + rot14; ks = ks >= 14 ? ks - 14 : ks; const unsigned src = WS_W3 + (j * 14 + ks) * 16 * KB;
#pragma unroll
      for (int u = 0; u < 4; ++u) dma4(rws, l16, src + u * 4 * KB, lds0 + slot + (lw * 16 + u * 4) * KB); };
    auto w4_step = [&](int s, unsigned slot) { const unsigned src = WS_W4 + ((j * 7 + (s < 6 ? pcol(s) : j)) * 32 + lw * 16) * KB;
#pragma unroll
      for (int u = 0; u < 4; ++u) dma4(rws, l16, src + u * 4 * KB, lds0 + slot + (lw * 16 + u * 4) * KB); };
    auto arrive = [&](int s) { if (lw == 0 && lane == 0) __hip_atomic_fetch_add((GAS unsigned*)(p.ws + WS_CNT + (i * 7 + pcol(s)) * 128), 1u, __ATOMIC_RELAXED, __HIP_MEMORY_SCOPE_AGENT); };
    w3_round(0, L_RA);
    BARS();
    w3_round(1, L_RB); w3_round(2, L_RC);
    BARS();
    STAMP(18); WAIT_VM(0); STAMP(19); BARS();
    BARS(); STAMP(20);
    BARS();
    w3_round(3, L_RA); w3_round(4, L_RB);
    BARS();
    BARS();
    WAIT_VM(16); BARS(); w3_round(5, L_RC);
    WAIT_VM(16); BARS(); w3_round(6, L_RA);
    WAIT_VM(16); BARS(); w4_step(0, L_RB);
    WAIT_VM(16); BARS(); w4_step(1, L_RC);
    BARS(); w4_step(2, L_RA); w4_step(3, L_RD);
    STAMP(21); WAIT_VM(16); STAMP(22); BARS();
    BARS(); w4_step(4, L_RB);
    BARS(); w4_step(5, L_RC);
    WAIT_VM(32); BARS(); w4_step(6, L_RA);
    WAIT_VM(32); BARS(); arrive(0);
    WAIT_VM(16); BARS(); arrive(1);
    WAIT_VM(0); BARS(); arrive(2);
    BARS(); arrive(3); arrive(4); arrive(5);
    STAMP(23); ST_FLUSH(1, p.ws, b, w);
    return;
  }

  const int hl = w >> 2, a = w & 3, head = 2 * j + hl, tt = 4 * i + a;
  const bool act = w < 7;
  const int wf = act ? w : 6, Fg = 7 * c4 + wf;
  u32x4 qf; u32x2 xr[2]; u32x4 cs1u[2], cs3u[2][2];
  aload128(qf, rws, l16, WS_Q + (head * 128 + tt) * KB);
  const u32x4 ones = {ONES, ONES, ONES, ONES};
  unsigned act_off = act ? (((tt0 + (g & 1)) * 14 + (Fg >> 1)) * 64 + (2 * (Fg & 1) + (g >> 1)) * 16 + l15) * 16 : OOB;
  const int ka = (2 * w + rot14) % 14, kb14 = (2 * w + 1 + rot14) % 14;
  unsigned pv0 = l16 + ka * KB, pv1 = l16 + kb14 * KB, ov0 = l16 + ((2 * w + rot16) & 15) * KB, ov1 = l16 + ((2 * w + 1 + rot16) & 15) * KB;
  unsigned zv[2];
#pragma unroll
  for (int q = 0; q < 2; ++q) { const int ks = q ? kb14 : ka, f0 = 32 * ks + 8 * g, sh = f0 < 112 ? -1 : f0 < 224 ? 1 : f0 < 336 ? -2 : 2, tz = 16 * (tt0 + 1) + l15 + sh;
    zv[q] = (((tz >> 4) * 14 + ks) * 64 + g * 16 + (tz & 15)) * 16; }
  u32x4 selA, selB;
  { const unsigned v = g == (l15 >> 2) ? ((l15 & 1) ? 0x3F800000u : 0x00003F80u) : 0u, d0 = (l15 & 2) ? 0u : v, d1 = (l15 & 2) ? v : 0u;
    selA = u32x4{d0, d1, 0u, 0u}; selB = u32x4{0u, 0u, d0, d1}; }
  unsigned ooff = ((tt * 16 + head) * 64 + ((g >> 1) + 2 * (g & 1)) * 16 + l15) * 16 + WS_O;
  asm volatile("" : "+v"(act_off), "+v"(pv0), "+v"(pv1), "+v"(ov0), "+v"(ov1), "+v"(zv[0]), "+v"(zv[1]), "+v"(selA), "+v"(selB), "+v"(ooff));
  WAIT_VM(0); RETIRE(qf);
  STAMP(1);
  BARS();
  { const __amdgpu_buffer_rsrc_t rx = mk_rsrc(p.x, S * D * 2);
#pragma unroll
    for (int t = 0; t < 2; ++t) aload64(xr[t], rx, ((16 * (tt0 + t) + l15) * D + 16 * Fg + 4 * g) * 2, 0); }
#pragma unroll
  for (int hf = 0; hf < 2; ++hf) { aload128(cs1u[hf], rws, (16 * Fg + 4 * g) * 4, WS_CS + hf * 2240 * 4);
#pragma unroll
    for (int f = 0; f < 2; ++f) aload128(cs3u[hf][f], rws, (448 + 256 * (j < 7 ? j : 6) + 32 * w + 16 * f + 4 * g) * 4, WS_CS + hf * 2240 * 4); }

  u32x4 opiece;
  {
    const int par = a & 1, kl0 = hl * 20 + (a & ~1), vl0 = hl * 20 + (a >> 1) * 2;
    f32x4 sc[18];
    const LAS char* kp = lds + L_K + kl0 * KB + l16;
    const f32x4 zero4 = {0.f, 0.f, 0.f, 0.f};
    lds_pipe<18, 8>([&](int n) { return lds128(kp + n * KB); }, [&](int n, u32x4 kf) { sc[n] = mfma16(kf, qf, zero4); }, [](int) {});
    const int dq = l15 - 4 * g;
    const float NEG = -__builtin_inff();
    if (par == 0) {
#pragma unroll
      for (int r = 0; r < 4; ++r) { const bool ge = dq >= r; sc[0][r] = ge ? NEG : sc[0][r]; sc[16][r] = ge ? sc[16][r] : NEG; sc[17][r] = NEG; }
    } else {
#pragma unroll
      for (int r = 0; r < 4; ++r) { const bool ge = dq >= r; sc[1][r] = ge ? NEG : sc[1][r]; sc[17][r] = ge ? sc[17][r] : NEG; sc[0][r] = NEG; }
    }
    const int nneg = 16 - (tt & ~1);
    if (nneg > 0) {
#pragma unroll
      for (int n = 0; n < 16; ++n) if (n < nneg) sc[n] = f32x4{NEG, NEG, NEG, NEG};
    }
    float m = max3(sc[0][0], sc[0][1], sc[0][2]), m2 = max3(sc[0][3], sc[1][0], sc[1][1]);
    m = max3(m, sc[1][2], sc[1][3]);
#pragma unroll
    for (int n = 2; n < 18; ++n) { m = max3(m, sc[n][0], sc[n][1]); m2 = max3(m2, sc[n][2], sc[n][3]); }
    m = gmax(max3(m, m2, m2));
    const float cexp = 0.18898223650461363f * 1.4426950408889634f, mc = -m * cexp;
    STAMP(14);
    BARS();
    STAMP(15);
    f32x4 o0 = zero4, o1 = zero4;
    const LAS char* vp = lds + L_V + vl0 * KB + l16;
    u32x4 v0 = lds128(vp), v1 = lds128(vp + KB);
#pragma unroll
    for (int mm = 0; mm < 9; ++mm) {
      u32x4 n0, n1;
      if (mm < 8) { n0 = lds128(vp + (2 * mm + 2) * KB); n1 = lds128(vp + (2 * mm + 3) * KB); }
      u32x4 pf;
#pragma unroll
      for (int q = 0; q < 4; ++q) { const f32x4& sv = sc[2 * mm + (q >> 1)];
        pf[q] = pk2(__builtin_amdgcn_exp2f(fmaf(sv[2 * (q & 1)], cexp, mc)), __builtin_amdgcn_exp2f(fmaf(sv[2 * (q & 1) + 1], cexp, mc))); }
      o0 = mfma16(v0, pf, o0);
      o1 = mfma16(v1, pf, o1);
      if (mm < 8) { v0 = n0; v1 = n1; }
      SB();
    }
    const float rl = __builtin_amdgcn_rcpf(gsum(g == 3 ? o1[0] : 0.f));
    opiece = pair_tiles(u32x2{pk2(o0[0] * rl, o0[1] * rl), pk2(o0[2] * rl, o0[3] * rl)}, u32x2{pk2(o1[0] * rl, o1[1] * rl), pk2(o1[2] * rl, o1[3] * rl)});
  }
  STAMP(16);
  WAIT_VM(0); RETIRE(xr[0]); RETIRE(xr[1]); RETIRE(cs1u[0]); RETIRE(cs1u[1]); RETIRE(cs3u[0][0]); RETIRE(cs3u[0][1]); RETIRE(cs3u[1][0]); RETIRE(cs3u[1][1]);
  const f32x4 cs1 = __builtin_bit_cast(f32x4, cs1u[0]) + __builtin_bit_cast(f32x4, cs1u[1]);
  f32x4 cs3[2];
#pragma unroll
  for (int f = 0; f < 2; ++f) cs3[f] = __builtin_bit_cast(f32x4, cs3u[0][f]) + __builtin_bit_cast(f32x4, cs3u[1][f]);
  BARS();
  bst128<16>(opiece, rws, ooff);
  u32x4 wr[16];
#pragma unroll
  for (int n = 0; n < 16; ++n) wr[n] = lds128(lds + (n <= 10 ? L_WOA + n * 7 * KB : L_WOB + (n - 11) * 7 * KB) + wf * KB + l16);
  STAMP(2);
  WAIT_VM(0);

  { u32x4 v[2][2];
    fetch_payload<2, 16>(v, rws, ov0, ov1, WS_O + tt0 * 16 * KB, 16 * KB, lds, lds0, L_O + 2 * w * KB, lane, [] { BARS();   }, p.ws + (40u << 20)); }
  STAMP(3);
  BARS();
  u32x2 hp[2];
  {
    f32x4 acc[2];
#pragma unroll
    for (int t = 0; t < 2; ++t) acc[t] = mfma16(selA, u32x4{xr[t][0], xr[t][1], 0u, 0u}, f32x4{0.f, 0.f, 0.f, 0.f});
    const LAS char* op = lds + L_O + l16;
    lds_pipe<32, 8>([&](int n) { return lds128(op + ((n & 1) * 16 + (n >> 1)) * KB); }, [&](int n, u32x4 f) { acc[n & 1] = mfma16(wr[n >> 1], f, acc[n & 1]); }, [](int) {});
#pragma unroll
    for (int t = 0; t < 2; ++t) hp[t] = u32x2{pk2(acc[t][0], acc[t][1]), pk2(acc[t][2], acc[t][3])};
    bst128<16>(pair_tiles(hp[0], hp[1]), rws, act_off + WS_H);
  }
  STAMP(4);
  WAIT_VM(0);

  LAS float* st4 = (LAS float*)(lds + L_ST4);
  auto stage_weights = [&] { BARS();
#pragma unroll
    for (int n = 0; n < 14; ++n) wr[n] = lds128(lds + L_W + (n * 7 + wf) * KB + l16);
    BARS(); };
  { u32x4 v[2][2] = {};
    if (act) fetch_payload<2, 14>(v, rws, pv0, pv1, WS_H + tt0 * 14 * KB, 14 * KB, lds, lds0, L_H + 2 * w * KB, lane, stage_weights, p.ws + (40u << 20) + 4); else stage_weights();
    row_stats<2>(v, ones, st4, w, l15, g); }
  STAMP(5);
  BARS();
  f32x2 yv[2][2];
  {
    const LAS char* hpz = lds + L_H + l16;
    f32x4 pt[2];
#pragma unroll
    for (int t = 0; t < 2; ++t) pt[t] = *(const LAS f32x4*)(st4 + ((t * 16 + l15) * 8 + 2 * g) * 2);
    f32x4 acc[2] = {};
    lds_pipe<28, 8>([&](int n) { return lds128(hpz + ((n & 1) * 14 + (n >> 1)) * KB); }, [&](int n, u32x4 f) { acc[n & 1] = mfma16(wr[n >> 1], f, acc[n & 1]); }, [](int) {});
    u32x2 zp[2];
#pragma unroll
    for (int t = 0; t < 2; ++t) {
      const float s1 = gsum(pt[t][0] + pt[t][2]), s2 = gsum(pt[t][1] + pt[t][3]);
      const float mean = s1 * (1.f / D), var = fmaxf(s2 * (1.f / D) - mean * mean, 0.f);
      const float r1 = __builtin_amdgcn_rsqf(var + 1e-5f);
      const float r12 = r1 * __builtin_amdgcn_rsqf(var * r1 * r1 + 1e-5f);
      const f32x2 m2 = {mean, mean};
#pragma unroll
      for (int q = 0; q < 2; ++q) {
        yv[t][q] = (f32x2{bflo(hp[t][q]), bfhi(hp[t][q])} - m2) * f32x2{r1, r1};
        zp[t][q] = gelu2((f32x2{acc[t][2 * q], acc[t][2 * q + 1]} - m2 * f32x2{cs1[2 * q], cs1[2 * q + 1]}) * f32x2{r12, r12});
      }
    }
    bst128<16>(pair_tiles(zp[0], zp[1]), rws, act_off + WS_Z + 14 * KB);
  }
  STAMP(6);
  WAIT_VM(0);

  { u32x4 v[2][2];
    if (act) fetch_payload<2, 14>(v, rws, zv[0], zv[1], WS_Z, 14 * KB, lds, lds0, L_Z + 2 * w * KB, lane, stage_weights, p.ws + (40u << 20) + 8); else stage_weights(); }
  STAMP(7);
  BARS();
  {
    f32x4 acc[2] = {};
    const LAS char* zb = lds + L_Z + l16;
    lds_pipe<28, 8>([&](int n) { return lds128(zb + ((n & 1) * 14 + (n >> 1)) * KB); }, [&](int n, u32x4 f) { acc[n & 1] = mfma16(wr[n >> 1], f, acc[n & 1]); }, [](int) {});
    BARS();
    u32x2 xp[2];
#pragma unroll
    for (int t = 0; t < 2; ++t) xp[t] = u32x2{pk2(yv[t][0][0] + acc[t][0], yv[t][0][1] + acc[t][1]), pk2(yv[t][1][0] + acc[t][2], yv[t][1][1] + acc[t][3])};
    bst128<16>(pair_tiles(xp[0], xp[1]), rws, act_off + WS_X1);
  }
  STAMP(8);
  if (j == 7) { ST_FLUSH(1, p.ws, b, w); return; }
  WAIT_VM(0);

  LAS float* st6 = (LAS float*)(lds + L_ST6);
  u32x4 areg[4][2];
  { u32x4 v[4][2] = {};
    auto w3_regs = [&] { BARS();
#pragma unroll
      for (int n = 0; n < 4; ++n)
#pragma unroll
        for (int f = 0; f < 2; ++f) areg[n][f] = lds128(lds + n * 16 * KB + (2 * w + f) * KB + l16);
      BARS(); };
    if (act) fetch_payload<4, 14>(v, rws, pv0, pv1, WS_X1 + 4 * i * 14 * KB, 14 * KB, lds, lds0, L_X1 + 2 * w * KB, lane, w3_regs, p.ws + (40u << 20) + 12); else w3_regs();
    row_stats<4>(v, ones, st6, w, l15, g); }
  STAMP(9);
  BARS();
  {
    const LAS char* xp = lds + L_X1 + l16;
    const LAS char* ap = lds + 2 * w * KB + l16;
    f32x4 pt[4];
#pragma unroll
    for (int t = 0; t < 4; ++t) pt[t] = *(const LAS f32x4*)(st6 + ((t * 16 + l15) * 8 + 2 * g) * 2);
    f32x4 acc[2][4] = {};
    u32x4 bfr[2][4], afr[2][2];
    auto load_k = [&](int n) { const unsigned slot = ((n >> 1) % 3 == 2 ? L_RC : (n >> 1) % 3 == 0 ? L_RA : L_RB) + (n & 1) * 16 * KB;
#pragma unroll
      for (int f = 0; f < 2; ++f) afr[n & 1][f] = n < 4 ? areg[n][f] : lds128(ap + slot + f * KB);
#pragma unroll
      for (int t = 0; t < 4; ++t) bfr[n & 1][t] = lds128(xp + (t * 14 + n) * KB); };
    load_k(0);
    SB();
#pragma unroll
    for (int n = 0; n < 14; ++n) {
      if (n & 1) BARS();
      if (n < 13) load_k(n + 1);
      SB();
#pragma unroll
      for (int t = 0; t < 4; ++t) { acc[0][t] = mfma16(afr[n & 1][0], bfr[n & 1][t], acc[0][t]); acc[1][t] = mfma16(afr[n & 1][1], bfr[n & 1][t], acc[1][t]); }
      SB();
    }
    STAMP(20);
    float mean[4], rs[4];
#pragma unroll
    for (int t = 0; t < 4; ++t) {
      const float s1 = gsum(pt[t][0] + pt[t][2]), s2 = gsum(pt[t][1] + pt[t][3]);
      mean[t] = s1 * (1.f / D); rs[t] = __builtin_amdgcn_rsqf(fmaxf(s2 * (1.f / D) - mean[t] * mean[t], 0.f) + 1e-5f);
    }
#pragma unroll
    for (int t = 0; t < 4; ++t) {
      u32x4 hv;
#pragma unroll
      for (int q = 0; q < 4; ++q) { const int f = q >> 1, r = 2 * (q & 1);
        hv[q] = gelu2((f32x2{acc[f][t][r], acc[f][t][r + 1]} - f32x2{mean[t], mean[t]} * f32x2{cs3[f][r], cs3[f][r + 1]}) * f32x2{rs[t], rs[t]}); }
      *(LAS u32x4*)(lds + L_HID + (t * 8 + w) * KB + l16) = hv;
    }
  }
  STAMP(10);
  BARS();
  STAMP(11);
  const int tb = w & 1, ft = w >> 1;
  u32x4 hf[2][8];
#pragma unroll
  for (int ks = 0; ks < 8; ++ks)
#pragma unroll
    for (int t = 0; t < 2; ++t) hf[t][ks] = lds128(lds + L_HID + ((2 * tb + t) * 8 + ks) * KB + l16);
  const unsigned pbase = l16 + WS_P + ((i * 49 + j) * 8 + w) * KB;
  const int Fo = 4 * j + ft;
  u32x2 hres[2], xres[2];
  unsigned ro[2];
#pragma unroll
  for (int t = 0; t < 2; ++t) { ro[t] = (((4 * i + 2 * tb + t) * 14 + (Fo >> 1)) * 64 + (2 * (Fo & 1) + (g >> 1)) * 16 + l15) * 16 + (g & 1) * 8;
    aload64_sc1(hres[t], rws, ro[t], WS_H); aload64_sc1(xres[t], rws, ro[t], WS_X1); }
  f32x4 acc[2] = {};
  {
    constexpr int PD = 4;
    constexpr unsigned slots[7] = {L_RB, L_RC, L_RA, L_RD, L_RB, L_RC, L_RA};
    const LAS char* wp = lds + ft * 8 * KB + l16;
    u32x4 buf[PD + 1];
#pragma unroll
    for (int m = 0; m < PD; ++m) buf[m] = lds128(wp + slots[0] + m * KB);
    SB();
#pragma unroll
    for (int m = 0; m < 56; ++m) {
      const int s = m >> 3, ks = m & 7;
      if (ks == 8 - PD && s < 6) { if (s >= 3) WAIT_VM(2); BARS(); }
      if (m + PD < 56) buf[(m + PD) % (PD + 1)] = lds128(wp + slots[(m + PD) >> 3] + ((m + PD) & 7) * KB);
      SB();
      acc[0] = mfma16(buf[m % (PD + 1)], hf[0][ks], acc[0]); acc[1] = mfma16(buf[m % (PD + 1)], hf[1][ks], acc[1]);
      if (ks == 7 && s < 6) {
        bst128<16>(u32x4{pk2(acc[0][0], acc[0][1]), pk2(acc[0][2], acc[0][3]), pk2(acc[1][0], acc[1][1]), pk2(acc[1][2], acc[1][3])}, rws, pbase + pcol(s) * 56 * KB);
        acc[0] = f32x4{0.f, 0.f, 0.f, 0.f}; acc[1] = acc[0];
      }
      SB();
    }
  }
  STAMP(12);
  {
    const __amdgpu_buffer_rsrc_t rout = mk_rsrc(p.out, S * D * 2);
    u32x4 pv[6];
    const unsigned pin = WS_P + ((i * 7 + j) * 7 * 8 + w) * KB;
    WAIT_VM(0); RETIRE(hres[0]); RETIRE(hres[1]); RETIRE(xres[0]); RETIRE(xres[1]);
    BARS();
    for (unsigned spin = 0; spin < SPIN_MAX; ++spin) {
      const unsigned m = max(max(max(hres[0][0], hres[0][1]), max(xres[0][0], xres[0][1])), max(max(hres[1][0], hres[1][1]), max(xres[1][0], xres[1][1])));
      if (!__any(m == SENT)) break;
#pragma unroll
      for (int t = 0; t < 2; ++t) { aload64_sc1(hres[t], rws, ro[t], WS_H); aload64_sc1(xres[t], rws, ro[t], WS_X1); }
      WAIT_VM(0); RETIRE(hres[0]); RETIRE(hres[1]); RETIRE(xres[0]); RETIRE(xres[1]);
    }
    { const GAS unsigned* cnt = (const GAS unsigned*)(p.ws + WS_CNT + (i * 7 + j) * 128);
      for (unsigned spin = 0; rfl(__hip_atomic_load(cnt, __ATOMIC_RELAXED, __HIP_MEMORY_SCOPE_AGENT)) < 6u && spin < SPIN_MAX; ++spin) __builtin_amdgcn_s_sleep(1); }
#pragma unroll
    for (int s = 0; s < 6; ++s) { int js = j + 1 + s; js = js >= 7 ? js - 7 : js; aload128_sc1(pv[s], rws, l16, pin + js * 8 * KB); }
    WAIT_VM(0);
#pragma unroll
    for (int s = 0; s < 6; ++s) RETIRE(pv[s]);
    const u32x4 selAB = selA | selB;
#pragma unroll
    for (int s = 0; s < 6; ++s) { acc[0] = mfma16(selA, pv[s], acc[0]); acc[1] = mfma16(selB, pv[s], acc[1]); }
#pragma unroll
    for (int t = 0; t < 2; ++t) {
      acc[t] = mfma16(selAB, u32x4{hres[t][0], hres[t][1], xres[t][0], xres[t][1]}, acc[t]);
      __builtin_amdgcn_raw_buffer_store_b64(u32x2{pk2(acc[t][0], acc[t][1]), pk2(acc[t][2], acc[t][3])}, rout, ((16 * (4 * i + 2 * tb + t) + l15) * D + 16 * Fo + 4 * g) * 2, 0, 0);
    }
  }
  STAMP(13);
  ST_FLUSH(1, p.ws, b, w);
}

extern "C" void kernel_launch(void* const* d_in, const int* in_sizes, int n_in, void* d_out, int out_size, void* d_ws, size_t ws_size, hipStream_t stream) {
  Params p;
  p.x = (const u16*)d_in[0]; p.wqkv = (const u16*)d_in[1]; p.wo = (const u16*)d_in[2];
  p.w1 = (const u16*)d_in[4]; p.w2 = (const u16*)d_in[5]; p.w3 = (const u16*)d_in[6]; p.w4 = (const u16*)d_in[7];
  p.out = (u16*)d_out; p.ws = (char*)d_ws;
  (void)hipFuncSetAttribute((const void*)k1_qkv, hipFuncAttributeMaxDynamicSharedMemorySize, K1_LDS);
  (void)hipFuncSetAttribute((const void*)k2_rest, hipFuncAttributeMaxDynamicSharedMemorySize, K2_LDS);
  k1_qkv<<<256, 512, K1_LDS, stream>>>(p);
  k2_rest<<<256, 640, K2_LDS, stream>>>(p);
}
```

```cpp
#include <hip/hip_runtime.h>
#include <hip/hip_bf16.h>
#include <stdint.h>

typedef unsigned short u16;
typedef __attribute__((ext_vector_type(8))) __bf16 bf16x8;
typedef __attribute__((ext_vector_type(2))) __bf16 bf16x2;
typedef __attribute__((ext_vector_type(4))) short s16x4;
typedef __attribute__((ext_vector_type(4))) float f32x4;
typedef __attribute__((ext_vector_type(16))) float f32x16;
typedef __attribute__((ext_vector_type(4))) unsigned u32x4;
typedef __attribute__((ext_vector_type(2))) unsigned u32x2;
#define LAS __attribute__((address_space(3)))
#define GAS __attribute__((address_space(1)))
#define DI __device__ __forceinline__

constexpr int S = 2048, D = 448, NH = 16, DH = 28, F = 1792;
constexpr unsigned SENT = 0xFFFFFFFFu;

constexpr unsigned KB = 1024;
constexpr unsigned WS_Q   = 0;
constexpr unsigned WS_K   = WS_Q + 2048 * KB;
constexpr unsigned WS_V   = WS_K + 2048 * KB;
constexpr unsigned WS_WO  = WS_V + 2048 * KB;
constexpr unsigned WS_W1  = WS_WO + 448 * KB;
constexpr unsigned WS_W2  = WS_W1 + 392 * KB;
constexpr unsigned WS_O   = WS_W2 + 392 * KB;
constexpr unsigned WS_H   = WS_O + 2048 * KB;
constexpr unsigned WS_X1  = WS_H + 1792 * KB;
constexpr unsigned WS_Z   = WS_X1 + 1792 * KB;
constexpr unsigned WS_P   = WS_Z + 1820 * KB;
constexpr unsigned WS_CS  = WS_P + 32 * 49 * 8 * KB;
constexpr unsigned WS_PF  = WS_CS + 18 * KB;
constexpr unsigned WS_FLG = WS_PF + 224 * KB;
constexpr unsigned WS_EP  = WS_FLG + 32 * KB;
constexpr unsigned WS_END = WS_EP + KB;

struct Params {
  const u16* x; const u16* wqkv; const u16* wo; const u16* w1; const u16* w2; const u16* w3; const u16* w4;
  u16* out; char* ws;
};

DI __amdgpu_buffer_rsrc_t mk_rsrc(const void* p, unsigned bytes) { return __builtin_amdgcn_make_buffer_rsrc((void*)p, (short)0, (int)bytes, 0x00020000); }
DI void dma16(__amdgpu_buffer_rsrc_t rs, unsigned voff, unsigned soff, unsigned ldsaddr) {
  asm volatile("s_mov_b32 m0, %0\n\ts_nop 0\n\tbuffer_load_dwordx4 %1, %2, %3 offen lds" :: "s"(ldsaddr), "v"(voff), "s"(rs), "s"(soff) : "memory");
}
DI void dma16_sc1(__amdgpu_buffer_rsrc_t rs, unsigned voff, unsigned soff, unsigned ldsaddr) {
  asm volatile("s_mov_b32 m0, %0\n\ts_nop 0\n\tbuffer_load_dwordx4 %1, %2, %3 offen sc1 lds" :: "s"(ldsaddr), "v"(voff), "s"(rs), "s"(soff) : "memory");
}
DI void dma4(__amdgpu_buffer_rsrc_t rs, unsigned voff, unsigned soff, unsigned ldsaddr) {
  asm volatile("s_mov_b32 m0, %0\n\ts_nop 0\n\tbuffer_load_dwordx4 %1, %2, %3 offen sc1 lds\n\tbuffer_load_dwordx4 %1, %2, %3 offen offset:1024 sc1 lds\n\t"
               "buffer_load_dwordx4 %1, %2, %3 offen offset:2048 sc1 lds\n\tbuffer_load_dwordx4 %1, %2, %3 offen offset:3072 sc1 lds" :: "s"(ldsaddr), "v"(voff), "s"(rs), "s"(soff) : "memory");
}
DI void dma3(__amdgpu_buffer_rsrc_t rs, unsigned voff, unsigned soff, unsigned ldsaddr) {
  asm volatile("s_mov_b32 m0, %0\n\ts_nop 0\n\tbuffer_load_dwordx4 %1, %2, %3 offen sc1 lds\n\tbuffer_load_dwordx4 %1, %2, %3 offen offset:1024 sc1 lds\n\t"
               "buffer_load_dwordx4 %1, %2, %3 offen offset:2048 sc1 lds" :: "s"(ldsaddr), "v"(voff), "s"(rs), "s"(soff) : "memory");
}
DI void dma4v(__amdgpu_buffer_rsrc_t rs, const unsigned (&v)[4], unsigned soff, unsigned ldsaddr) {
  asm volatile("s_mov_b32 m0, %0\n\ts_nop 0\n\tbuffer_load_dwordx4 %1, %5, %6 offen lds\n\tbuffer_load_dwordx4 %2, %5, %6 offen offset:1024 lds\n\t"
               "buffer_load_dwordx4 %3, %5, %6 offen offset:2048 lds\n\tbuffer_load_dwordx4 %4, %5, %6 offen offset:3072 lds"
               :: "s"(ldsaddr), "v"(v[0]), "v"(v[1]), "v"(v[2]), "v"(v[3]), "s"(rs), "s"(soff) : "memory");
}
#define WAIT_VM(n) asm volatile("s_waitcnt vmcnt(" #n ")" ::: "memory")
#define BAR() asm volatile("s_waitcnt lgkmcnt(0)\n\ts_barrier" ::: "memory")
#define BARS() do { __builtin_amdgcn_sched_barrier(0); BAR(); __builtin_amdgcn_sched_barrier(0); } while (0)
DI unsigned rfl(unsigned v) { return __builtin_amdgcn_readfirstlane(v); }
DI float bflo(unsigned u) { return __uint_as_float(u << 16); }
DI float bfhi(unsigned u) { return __uint_as_float(u & 0xffff0000u); }
DI unsigned pk2(float a, float b) { bf16x2 v; v[0] = (__bf16)a; v[1] = (__bf16)b; return __builtin_bit_cast(unsigned, v); }
DI float dot2(unsigned a, unsigned b, float c) { return __builtin_amdgcn_fdot2_f32_bf16(__builtin_bit_cast(bf16x2, a), __builtin_bit_cast(bf16x2, b), c, false); }
DI f32x16 mfma32(u32x4 a, u32x4 b, f32x16 c) { return __builtin_amdgcn_mfma_f32_32x32x16_bf16(__builtin_bit_cast(bf16x8, a), __builtin_bit_cast(bf16x8, b), c, 0, 0, 0); }
DI f32x4 mfma16(u32x4 a, u32x4 b, f32x4 c) { return __builtin_amdgcn_mfma_f32_16x16x32_bf16(__builtin_bit_cast(bf16x8, a), __builtin_bit_cast(bf16x8, b), c, 0, 0, 0); }
DI u32x2 tr_read(const LAS char* p) { return __builtin_bit_cast(u32x2, __builtin_amdgcn_ds_read_tr16_b64_v4i16((LAS s16x4*)p)); }
DI u32x4 lds128(const LAS char* p) { return *(const LAS u32x4*)p; }
template <int AUX> DI void bst128(u32x4 v, __amdgpu_buffer_rsrc_t rs, unsigned off) { __builtin_amdgcn_raw_buffer_store_b128(v, rs, off, 0, AUX); }
#define XJ j
constexpr unsigned ONES = 0x3F803F80u;
#define AUX_SIDE 16
#define AUX_QKV 16
#define ST_DECL
#define ST_PARAM
#define ST_DECLQ
#define ST_ARGQ
#define ST_FLUSHQ(ws, b, w)
#define CSTAMP(n)
#define STAMP(n)
#define ST_FLUSH(kern, ws, b, w)

DI void aload128(u32x4& d, __amdgpu_buffer_rsrc_t rs, unsigned voff, unsigned soff) { asm volatile("buffer_load_dwordx4 %0, %1, %2, %3 offen" : "=v"(d) : "v"(voff), "s"(rs), "s"(soff) : "memory"); }
DI void aload64(u32x2& d, __amdgpu_buffer_rsrc_t rs, unsigned voff, unsigned soff) { asm volatile("buffer_load_dwordx2 %0, %1, %2, %3 offen" : "=v"(d) : "v"(voff), "s"(rs), "s"(soff) : "memory"); }
DI void aload128_sc1(u32x4& d, __amdgpu_buffer_rsrc_t rs, unsigned voff, unsigned soff) { asm volatile("buffer_load_dwordx4 %0, %1, %2, %3 offen sc1" : "=v"(d) : "v"(voff), "s"(rs), "s"(soff) : "memory"); }
DI void aload64_sc1(u32x2& d, __amdgpu_buffer_rsrc_t rs, unsigned voff, unsigned soff) { asm volatile("buffer_load_dwordx2 %0, %1, %2, %3 offen sc1" : "=v"(d) : "v"(voff), "s"(rs), "s"(soff) : "memory"); }
#define RETIRE(x) asm volatile("; RETIRE %0" : "+v"(x))
constexpr unsigned CODE_BYTES = 50 * 1024;
DI unsigned touch_code(unsigned long long base, unsigned bytes, int b, int lane, bool on) {
  const __amdgpu_buffer_rsrc_t rc = mk_rsrc((const void*)base, bytes);
  const unsigned per = (bytes / 128 + 31) / 32, line = (b >> 3) * per + lane;
  unsigned d;
  asm volatile("buffer_load_dword %0, %1, %2, 0 offen" : "=v"(d) : "v"(on && lane < (int)per ? line * 128 : 0xF0000000u), "s"(rc) : "memory");
  return d;
}
DI unsigned long long code_base() { unsigned long long pc; asm volatile("s_getpc_b64 %0" : "=s"(pc)); return pc & ~0xFFull; }

#define ROTQ 0
#define XMAP 2
DI void map_block(int b, int& i, int& j) { const int x = b & 7, s = b >> 3; constexpr int JPX = 8 / XMAP; i = (x / XMAP) * 4 * XMAP + s / JPX; j = (x % XMAP) * JPX + s % JPX; }

constexpr unsigned K1_XS = 0;
constexpr unsigned K1_W0 = 7 * 8192;
constexpr unsigned K1_SLOT = 24576;
constexpr unsigned K1_ST = K1_W0 + 4 * K1_SLOT;
constexpr unsigned K1_CS = K1_ST + 512;
constexpr unsigned K1_EP = K1_CS + 2 * 2 * 3 * 32 * 4;

DI void retile_load(const Params& p, int mat, int ks, int cb, int lane, u32x4 (&v)[4]) {
  const u16* W = mat == 0 ? p.wo : mat == 1 ? p.w1 : mat == 2 ? p.w2 : p.w3;
  const unsigned ldw = mat == 3 ? F : D, nrows = D, krow0 = (mat == 0 ? 28 : 32) * ks;
#pragma unroll
  for (int q = 0; q < 4; ++q) {
    unsigned row = krow0 + (lane >> 3) + 8 * q; row = row < nrows ? row : nrows - 1;
    v[q] = *(const u32x4*)(W + (size_t)row * ldw + cb * 64 + (lane & 7) * 8);
  }
}
template <bool SUMS>
DI void retile_store(const Params& p, LAS char* scr, int mat, int ks, int cb, int lane, const u32x4 (&v)[4], f32x4 (&cs)[4]) {
#pragma unroll
  for (int q = 0; q < 4; ++q) *(LAS u32x4*)(scr + q * 1024 + lane * 16) = v[q];
  const int g = lane >> 4, q4 = (lane & 15) >> 2, pp = lane & 3;
  const int r0 = 8 * g + q4, r1 = 8 * g + 4 + q4;
  const __amdgpu_buffer_rsrc_t rws = mk_rsrc(p.ws, WS_END);
  const u32x4 ones = {ONES, ONES, ONES, ONES};
#pragma unroll
  for (int ft = 0; ft < 4; ++ft) {
    u32x2 lo = tr_read(scr + r0 * 128 + ft * 32 + pp * 8), hi = tr_read(scr + r1 * 128 + ft * 32 + pp * 8);
    if (mat == 0 && g == 3) { hi[0] = 0u; hi[1] = 0u; }
    const u32x4 f = {lo[0], lo[1], hi[0], hi[1]};
    const int Fi = 4 * cb + ft;
    unsigned dst;
    if (mat == 0) dst = WS_WO + (((Fi / 7) * 16 + ks) * 7 + Fi % 7) * KB;
    else dst = (mat == 1 ? WS_W1 : WS_W2) + (((Fi / 7) * 14 + ks) * 7 + Fi % 7) * KB;
    if (mat != 3) bst128<AUX_SIDE>(f, rws, lane * 16 + dst);
    if (SUMS) cs[ft] = mfma16(f, ones, cs[ft]);
  }
}

DI int qk_slot(int n0, int& half, bool& last) {
  const int head = n0 / 28, hs = 28 * head, bnd = (hs / 64 + 1) * 64;
  const bool split = bnd < hs + 28, second = split && n0 >= bnd;
  const int seg0 = second ? bnd : hs, seg1 = (split && !second) ? bnd : hs + 28, off = n0 - seg0;
  half = (off >> 2) & 1;
  last = n0 + 4 == seg1 && half == 0;
  return 4 * head + (second ? (bnd - hs + 7) >> 3 : 0) + (off >> 3);
}
DI void fill_sentinels(__amdgpu_buffer_rsrc_t rws, int i, int j, int lw, int lane) {
  const u32x4 sv = {SENT, SENT, SENT, SENT};
  const unsigned l16 = lane * 16, tt0 = 4 * i + 2 * (j >> 2), F0 = 7 * (j & 3);
#pragma unroll
  for (int t = 0; t < 2; ++t) { const unsigned o = WS_O + ((4 * i + 2 * lw + t) * 16 + 2 * j) * KB + l16; bst128<16>(sv, rws, o); bst128<16>(sv, rws, o + KB); }
  const unsigned o = ((tt0 + lw) * 14 + (F0 >> 1)) * KB + (F0 & 1) * 512 + l16;
#pragma unroll
  for (int u = 0; u < 3; ++u) { const unsigned base = u == 0 ? WS_H : u == 1 ? WS_X1 : WS_Z + 14 * KB;
    bst128<16>(sv, rws, o + base); bst128<16>(sv, rws, o + base + KB); bst128<16>(sv, rws, o + base + 2 * KB); bst128<16>(sv, rws, lane < 32 ? o + base + 3 * KB : 0xF0000000u); }
}

DI void phase_qkv(const Params& p, LAS char* lds, unsigned lds0, unsigned long long cbase, int b, int i, int j, int w, int lane ST_PARAM) {
  STAMP(0);
  unsigned epv = 0, cwarm = 0, twarm = 0;
  auto errands = [&] {
    asm volatile("buffer_load_dword %0, %1, %2, 0 offen sc1" : "=v"(epv) : "v"(WS_EP), "s"(mk_rsrc(p.ws, WS_END)) : "memory");
    cwarm = touch_code(cbase, CODE_BYTES, b, lane, true);
  };
  auto errands_done = [&] { WAIT_VM(0); RETIRE(epv); RETIRE(cwarm); RETIRE(twarm); if (lane == 0) *(LAS unsigned*)(lds + K1_EP) = epv + 1u; };

  if (j == 7) {
    const __amdgpu_buffer_rsrc_t rws = mk_rsrc(p.ws, WS_END);
    if (w >= 8) {
      const int lw = w - 8;
      if (lw == 1) { errands(); errands_done(); }
      fill_sentinels(rws, i, j, lw, lane);
#pragma unroll
      for (int n = 0; n < 4; ++n) { const int pr = (lw * 4 + n) * 4 + (lane >> 4), gq = (lane >> 2) & 3, row = lane & 3; const unsigned o = row == 0 ? ONES : 0u;
        bst128<16>(u32x4{o, o, o, o}, rws, WS_V + ((((pr >> 1) * 64 + 2 * i + (pr & 1)) * 2 + 1) * 64 + gq * 16 + 12 + row) * 16); }
      if (i == 0 || i == 31) {
#pragma unroll
        for (int n = 0; n < 7; ++n) bst128<16>(u32x4{0u, 0u, 0u, 0u}, rws, WS_Z + ((i == 0 ? 0 : 129) * 14 + lw * 7 + n) * KB + lane * 16); }
    } else {
    const int colid = w < 2 ? w * 32 + i : (w == 2 && i < 6) ? 64 + i : -1;
    const int nid = w >= 3 ? i * 5 + (w - 3) : 160 + (i - 6);
    u32x4 v[7][4];
    auto other = [&](int m, int& mat, int& ks, int& cb) { if (m < 112) { mat = 0; ks = m / 7; cb = m % 7; } else { mat = 2; ks = (m - 112) / 7; cb = (m - 112) % 7; } };
    const int ccol = colid >> 1, chalf = colid & 1, cmat = ccol < 7 ? 1 : 3, ccb = ccol < 7 ? ccol : ccol - 7;
    f32x4 cs[4] = {};
    if (colid >= 0) {
#pragma unroll
      for (int q = 0; q < 7; ++q) retile_load(p, cmat, 7 * chalf + q, ccb, lane, v[q]);
#pragma unroll
      for (int q = 0; q < 7; ++q) retile_store<true>(p, lds + (2 * w + (q & 1)) * 4096, cmat, 7 * chalf + q, ccb, lane, v[q], cs);
      if ((lane & 15) == 0) {
#pragma unroll
        for (int ft = 0; ft < 4; ++ft) bst128<16>(__builtin_bit_cast(u32x4, cs[ft]), rws, WS_CS + (chalf * 2240 + (cmat == 1 ? 0 : 448) + 64 * ccb + 16 * ft + 4 * (lane >> 4)) * 4); }
    } else {
#pragma unroll
      for (int q = 0; q < 2; ++q) { const int m = nid + 186 * q; if (m < 210) { int mat, ks, cb; other(m, mat, ks, cb); retile_load(p, mat, ks, cb, lane, v[q]); } }
#pragma unroll
      for (int q = 0; q < 2; ++q) { const int m = nid + 186 * q; if (m < 210) { int mat, ks, cb; other(m, mat, ks, cb); retile_store<false>(p, lds + (2 * w + (q & 1)) * 4096, mat, ks, cb, lane, v[q], cs); } }
    }
    }
    STAMP(7);
    WAIT_VM(0); BARS();
    return;
  }
  if (w == 6 || w == 7) {
    if (w == 7) {
      errands();
#pragma unroll
      for (int c = 0; c < 3; ++c) BARS();
      errands_done();
    } else {
#pragma unroll
      for (int c = 0; c < 3; ++c) BARS();
    }
    fill_sentinels(mk_rsrc(p.ws, WS_END), i, j, w - 6, lane);
#pragma unroll
    for (int c = 3; c < 9; ++c) BARS();
    WAIT_VM(0); BARS();
    return;
  }
  if (w == 4 || w == 5) {
    const int tok = 32 * (w - 4) + (lane & 31), part = lane >> 5;
    const LAS char* xb[4];
#pragma unroll
    for (int q = 0; q < 4; ++q) xb[q] = lds + K1_XS + tok * 128 + ((4 * part + q) ^ ((tok >> 1) & 7)) * 16;
    float s1a = 0.f, s1b = 0.f, s2a = 0.f, s2b = 0.f;
    BARS();
#pragma unroll
    for (int c = 0; c < 7; ++c) {
      u32x4 v[4];
#pragma unroll
      for (int q = 0; q < 4; ++q) v[q] = lds128(xb[q] + c * 8192);
      __builtin_amdgcn_sched_barrier(0);
#pragma unroll
      for (int q = 0; q < 4; ++q)
#pragma unroll
        for (int u = 0; u < 4; ++u) { float lo = bflo(v[q][u]), hi = bfhi(v[q][u]); asm volatile("" : "+v"(lo), "+v"(hi));
          s1a += lo; s1b += hi; s2a = fmaf(lo, lo, s2a); s2b = fmaf(hi, hi, s2b); }
      asm volatile("" : "+v"(s1a), "+v"(s1b), "+v"(s2a), "+v"(s2b));
      if (c == 6) {
        float a1 = s1a + s1b, a2 = s2a + s2b;
        a1 += __shfl_xor(a1, 32); a2 += __shfl_xor(a2, 32);
        const float mean = a1 * (1.f / D), var = fmaxf(a2 * (1.f / D) - mean * mean, 0.f);
        if (lane < 32) { *(LAS float*)(lds + K1_ST + tok * 4) = mean; *(LAS float*)(lds + K1_ST + 256 + tok * 4) = __builtin_amdgcn_rsqf(var + 1e-5f); }
      }
      BARS();
    }
    BARS();
    STAMP(7);
    WAIT_VM(0); BARS();
    return;
  }
  if (w >= 8) {
    const int lw = w - 8;
    const __amdgpu_buffer_rsrc_t rx = mk_rsrc(p.x, S * D * 2), rw = mk_rsrc(p.wqkv, D * 3 * D * 2);
    const unsigned xv0 = (lane >> 3) * 896 + ((lane & 7) ^ (lane >> 4)) * 16, xv1 = (lane >> 3) * 896 + ((lane & 7) ^ (lane >> 4) ^ 4) * 16;
    const unsigned wv = (lane >> 3) * 2688 + (((lane & 7) ^ (((lane >> 4) & 1) * 4)) * 16);
    const int rotq = ROTQ;
    auto issue_chunk = [&](int c) {
      int cc = c + rotq; cc = cc >= 7 ? cc - 7 : cc;
#pragma unroll
      for (int q = 0; q < 4; ++q) { const unsigned n = lw * 4 + q; dma16(rx, (q & 1) ? xv1 : xv0, (64 * i + 8 * n) * 896 + cc * 128, lds0 + K1_XS + c * 8192 + n * 1024); }
#pragma unroll
      for (int q = 0; q < 12; ++q) { const unsigned n = lw * 12 + q, type = n >> 3, rg = n & 7;
        dma16(rw, wv, (64 * cc + 8 * rg) * 2688 + type * 896 + j * 128, lds0 + K1_W0 + (c & 3) * K1_SLOT + type * 8192 + rg * 1024); }
    };
    issue_chunk(0); issue_chunk(1);
    STAMP(1);
    WAIT_VM(16); STAMP(2); BARS();
    issue_chunk(2); issue_chunk(3);
    WAIT_VM(32); STAMP(3); BARS();
    issue_chunk(4);
    WAIT_VM(32); STAMP(8); BARS();
    issue_chunk(5);
    WAIT_VM(32); STAMP(9); BARS();
    issue_chunk(6);
    WAIT_VM(32); STAMP(10); BARS();
    WAIT_VM(16); STAMP(11); BARS();
    WAIT_VM(0); STAMP(5); BARS();
    BAR();
    BARS();
    STAMP(7);
    WAIT_VM(0); BARS();
    return;
  }
  const int tb = w & 1, e = w >> 1, r = lane & 31, h = lane >> 5;
  f32x16 aq = {}, ak = {}, av = {};
  f32x4 cq = {}, ck = {}, cv = {};
  const unsigned cs1 = ((lane & 15) == ((lane >> 4) & 1)) ? ONES : 0u;
  const u32x4 csel = {cs1, cs1, cs1, cs1};
  const int q4 = (lane & 15) >> 2, pp = lane & 3, blk = (lane >> 4) & 1;
  const LAS char* xb[4];
#pragma unroll
  for (int kl = 0; kl < 4; ++kl) xb[kl] = lds + K1_XS + (32 * tb + r) * 128 + ((2 * (kl ^ tb) + h) ^ ((r >> 1) & 7)) * 16;
  const unsigned wb = K1_W0 + (8 * h + q4) * 128 + (((8 * e + 4 * blk + pp) ^ (8 * ((q4 >> 1) & 1))) * 8);
  const LAS char* we = lds + wb + tb * 2048;
  const LAS char* wo = lds + wb + (1 - tb) * 2048;
  u32x4 xf[3], f[3][3];
  auto load_step = [&](int n) {
    xf[n % 3] = lds128(xb[n & 3] + (n >> 2) * 8192);
    const LAS char* wp = ((n & 1) ? wo : we) + ((n >> 2) & 3) * K1_SLOT + (n & 2) * 2048;
#pragma unroll
    for (int t = 0; t < 3; ++t) { const u32x2 lo = tr_read(wp + t * 8192), hi = tr_read(wp + t * 8192 + 512); f[n % 3][t] = u32x4{lo[0], lo[1], hi[0], hi[1]}; }
  };
  const __amdgpu_buffer_rsrc_t rws = mk_rsrc(p.ws, WS_END);
  constexpr unsigned K1_IMG = K1_W0 + 3 * K1_SLOT;
  int g0half; bool g0last; const int G0 = qk_slot(64 * j, g0half, g0last);
  unsigned qdst[4], zdst[4], voff;
  {
#pragma unroll
    for (int g4 = 0; g4 < 4; ++g4) { int half; bool last; const int G = qk_slot(64 * j + 32 * e + 8 * g4 + 4 * h, half, last);
      qdst[g4] = K1_IMG + (G - G0) * KB + ((2 * tb + (r >> 4)) * 16 + (r & 15)) * 16 + half * 8;
      zdst[g4] = last ? qdst[g4] + 8 : K1_IMG + 2 * 10 * KB + lane * 8;
      asm volatile("" : "+v"(qdst[g4]), "+v"(zdst[g4])); }
    const unsigned n = 64 * j + 32 * e + r, head = n / 28, d = n - 28 * head;
    voff = WS_V + (((head * 64 + 2 * i + tb) * 2 + (d >> 4)) * 64 + h * 16 + (d & 15)) * 16; asm volatile("" : "+v"(voff)); }
  STAMP(1);
  BARS();
  STAMP(2);
  load_step(0);
#pragma unroll
  for (int n = 0; n < 28; ++n) {
    if ((n & 3) == 3 && n < 27) { if (n == 7) STAMP(8); if (n == 11) STAMP(10); BARS(); if (n == 7) { STAMP(9); CSTAMP(13); } if (n == 11) { STAMP(11); CSTAMP(14); } if (n == 15) STAMP(12); }
    if (n < 27) load_step(n + 1);
    aq = mfma32(f[n % 3][0], xf[n % 3], aq);
    ak = mfma32(f[n % 3][1], xf[n % 3], ak);
    av = mfma32(xf[n % 3], f[n % 3][2], av);
    if (!(n & 1)) {
      cq = mfma16(f[n % 3][0], csel, cq); ck = mfma16(f[n % 3][1], csel, ck); cv = mfma16(f[n % 3][2], csel, cv);
    }
    __builtin_amdgcn_sched_group_barrier(0x8, 1, 0); __builtin_amdgcn_sched_group_barrier(0x100, 3, 0);
    __builtin_amdgcn_sched_group_barrier(0x8, 1, 0); __builtin_amdgcn_sched_group_barrier(0x100, 2, 0);
    __builtin_amdgcn_sched_group_barrier(0x8, 1, 0); __builtin_amdgcn_sched_group_barrier(0x100, 2, 0);
    __builtin_amdgcn_sched_group_barrier(0x8, 3, 0);
    __builtin_amdgcn_sched_barrier(0);
  }
  STAMP(4);
  LAS float* cs = (LAS float*)(lds + K1_CS) + e * 192;
  if ((lane & 15) < 2) { LAS float* o = cs + tb * 96 + 16 * (lane & 1) + 4 * (lane >> 4);
    *(LAS f32x4*)o = cq; *(LAS f32x4*)(o + 32) = ck; *(LAS f32x4*)(o + 64) = cv; }
  BAR();
  STAMP(5);
  const LAS float* st = (const LAS float*)(lds + K1_ST);
  f32x4 cqk[2][4][2], m4[4], r4[4];
#pragma unroll
  for (int t = 0; t < 2; ++t)
#pragma unroll
    for (int g4 = 0; g4 < 4; ++g4) { cqk[t][g4][0] = *(const LAS f32x4*)(cs + t * 32 + 8 * g4 + 4 * h); cqk[t][g4][1] = *(const LAS f32x4*)(cs + 96 + t * 32 + 8 * g4 + 4 * h); }
  const float mean = st[32 * tb + r], rstd = st[64 + 32 * tb + r];
  const float cs2 = cs[64 + r] + cs[96 + 64 + r];
#pragma unroll
  for (int g4 = 0; g4 < 4; ++g4) { m4[g4] = *(const LAS f32x4*)(st + 32 * tb + 8 * g4 + 4 * h); r4[g4] = *(const LAS f32x4*)(st + 64 + 32 * tb + 8 * g4 + 4 * h); }
  __builtin_amdgcn_sched_barrier(0);
#pragma unroll
  for (int g4 = 0; g4 < 4; ++g4)
#pragma unroll
    for (int t = 0; t < 2; ++t) {
      const f32x16& a = t ? ak : aq;
      float v[4];
#pragma unroll
      for (int q = 0; q < 4; ++q) v[q] = rstd * (a[4 * g4 + q] - mean * (cqk[t][g4][0][q] + cqk[t][g4][1][q]));
      *(LAS u32x2*)(lds + qdst[g4] + t * 10 * KB) = u32x2{pk2(v[0], v[1]), pk2(v[2], v[3])};
      *(LAS u32x2*)(lds + zdst[g4] + (zdst[g4] < K1_IMG + 2 * 10 * KB ? t * 10 * KB : 0)) = u32x2{0u, 0u};
    }
  {
    float v[16];
#pragma unroll
    for (int g4 = 0; g4 < 4; ++g4)
#pragma unroll
      for (int q = 0; q < 4; ++q) v[4 * g4 + q] = r4[g4][q] * (av[4 * g4 + q] - m4[g4][q] * cs2);
    const u32x4 oa = {pk2(v[0], v[1]), pk2(v[2], v[3]), pk2(v[8], v[9]), pk2(v[10], v[11])};
    const u32x4 ob = {pk2(v[4], v[5]), pk2(v[6], v[7]), pk2(v[12], v[13]), pk2(v[14], v[15])};
    bst128<AUX_QKV>(oa, rws, voff);
    bst128<AUX_QKV>(ob, rws, voff + 512);
  }
  BARS();
  { int lh; bool ll; const int NS = qk_slot(64 * j + 60, lh, ll) - G0 + 1;
#pragma unroll
    for (int q = 0; q < 3; ++q) { const int ls = w + 4 * q;
      if (ls < NS) { const int G = G0 + ls; const unsigned o = WS_Q + (((G >> 2) * 128 + 4 * i + (lane >> 4)) * 64 + (G & 3) * 16 + (lane & 15)) * 16;
        bst128<AUX_QKV>(lds128(lds + K1_IMG + ls * KB + lane * 16), rws, o);
        bst128<AUX_QKV>(lds128(lds + K1_IMG + (10 + ls) * KB + lane * 16), rws, o + (WS_K - WS_Q)); } }
  }
  STAMP(6);
  WAIT_VM(0);
  STAMP(7);
  WAIT_VM(0); BARS();
}

constexpr unsigned L_K = 0, L_WOA = 40 * KB, L_V = 120 * KB, L_WOB = 0;
constexpr unsigned L_O = 128 * KB;
constexpr unsigned L_W = 0;
constexpr unsigned L_H = 98 * KB, L_ST4 = 126 * KB;
constexpr unsigned L_Z = 128 * KB;
constexpr unsigned L_X1 = 96 * KB, L_ST6 = 152 * KB;
constexpr unsigned L_HID = 96 * KB;
constexpr unsigned L_RA = 0, L_RB = 32 * KB, L_RC = 64 * KB, L_RD = 128 * KB;
constexpr unsigned K2_LDS = 163840;
constexpr unsigned SPIN_MAX = 1u << 16;

DI u32x4 bload(__amdgpu_buffer_rsrc_t rs, unsigned voff, unsigned soff) { return __builtin_amdgcn_raw_buffer_load_b128(rs, voff, soff, 0); }
DI u32x4 bload_sc1(__amdgpu_buffer_rsrc_t rs, unsigned voff, unsigned soff) { return __builtin_amdgcn_raw_buffer_load_b128(rs, voff, soff, 16); }
DI void bstore_sc1(u32x4 v, __amdgpu_buffer_rsrc_t rs, unsigned voff, unsigned soff) { bst128<16>(v, rs, voff + soff); }
DI unsigned umax4(u32x4 v) { return max(max(v[0], v[1]), max(v[2], v[3])); }
#define FENCE() asm volatile("" ::: "memory")
typedef __attribute__((ext_vector_type(2))) float f32x2_;
DI float gelu_tanh(float x) {
  const float e = __builtin_amdgcn_exp2f(x * fmaf(x * x, -0.10294324f, -2.3022082f));
  return x * __builtin_amdgcn_rcpf(1.f + e);
}
DI u32x4 pair_tiles(u32x2 a, u32x2 b) {
  const auto s0 = __builtin_amdgcn_permlane16_swap(a[0], b[0], false, false), s1 = __builtin_amdgcn_permlane16_swap(a[1], b[1], false, false);
  return u32x4{s0[0], s1[0], s0[1], s1[1]};
}
template <int NT, int NS, class F>
DI void fetch_payload(u32x4 (&v)[NT][2], __amdgpu_buffer_rsrc_t rs, unsigned voff0, unsigned voff1, unsigned soff, unsigned tstride, LAS char* lds, unsigned lds0, unsigned dst, int lane, F fill, void* dbg = nullptr) {
  for (unsigned spin = 0;; ++spin) {
#pragma unroll
    for (int t = 0; t < NT; ++t) { dma16_sc1(rs, voff0, soff + t * tstride, lds0 + dst + t * NS * KB); dma16_sc1(rs, voff1, soff + t * tstride, lds0 + dst + (t * NS + 1) * KB); }
    if (spin == 0) { fill(); __builtin_amdgcn_sched_barrier(0); }
    WAIT_VM(0);
    unsigned m = 0;
#pragma unroll
    for (int t = 0; t < NT; ++t)
#pragma unroll
      for (int q = 0; q < 2; ++q) { v[t][q] = lds128(lds + dst + (t * NS + q) * KB + lane * 16); m = max(m, max(max(v[t][q][0], v[t][q][1]), max(v[t][q][2], v[t][q][3]))); }
    if (!__any(m == SENT) || spin > SPIN_MAX) break;
  }
}
template <int NT> DI void row_stats(const u32x4 (&v)[NT][2], u32x4 ones, LAS float* stp, int w, int l15, int g) {
#pragma unroll
  for (int t = 0; t < NT; ++t) {
    f32x4 d1 = {}, d2 = {};
#pragma unroll
    for (int q = 0; q < 2; ++q) { d1 = __builtin_amdgcn_mfma_f32_16x16x32_bf16(__builtin_bit_cast(bf16x8, ones), __builtin_bit_cast(bf16x8, v[t][q]), d1, 0, 0, 0);
                                  d2 = __builtin_amdgcn_mfma_f32_16x16x32_bf16(__builtin_bit_cast(bf16x8, v[t][q]), __builtin_bit_cast(bf16x8, v[t][q]), d2, 0, 0, 0); }
    const float lo = (l15 & 1) ? d2[1] : d2[0], hi = (l15 & 1) ? d2[3] : d2[2], s2 = (l15 & 2) ? hi : lo;
    if (g == (l15 >> 2)) *(LAS f32x2_*)(stp + ((t * 16 + l15) * 8 + w) * 2) = f32x2_{d1[0], s2};
  }
}

#define SB() __builtin_amdgcn_sched_barrier(0)
typedef __attribute__((ext_vector_type(2))) float f32x2;
DI float max3(float a, float b, float c) { float r; asm("v_max3_f32 %0, %1, %2, %3" : "=v"(r) : "v"(a), "v"(b), "v"(c)); return r; }
DI float gsum(float v) {
  const auto a = __builtin_amdgcn_permlane16_swap(__float_as_uint(v), __float_as_uint(v), false, false); const float s = __uint_as_float(a[0]) + __uint_as_float(a[1]);
  const auto c = __builtin_amdgcn_permlane32_swap(__float_as_uint(s), __float_as_uint(s), false, false); return __uint_as_float(c[0]) + __uint_as_float(c[1]);
}
DI float gmax(float v) {
  const auto a = __builtin_amdgcn_permlane16_swap(__float_as_uint(v), __float_as_uint(v), false, false); const float s = max3(__uint_as_float(a[0]), __uint_as_float(a[1]), __uint_as_float(a[1]));
  const auto c = __builtin_amdgcn_permlane32_swap(__float_as_uint(s), __float_as_uint(s), false, false); return max3(__uint_as_float(c[0]), __uint_as_float(c[1]), __uint_as_float(c[1]));
}
DI unsigned gelu2(f32x2 x) {
  const f32x2 t = x * (x * x * f32x2{-0.10294324f, -0.10294324f} + f32x2{-2.3022082f, -2.3022082f});
  const f32x2 d = f32x2{__builtin_amdgcn_exp2f(t[0]), __builtin_amdgcn_exp2f(t[1])} + f32x2{1.f, 1.f};
  const f32x2 r = x * f32x2{__builtin_amdgcn_rcpf(d[0]), __builtin_amdgcn_rcpf(d[1])};
  return pk2(r[0], r[1]);
}
template <int N, int PD, class L, class U, class M> DI void lds_pipe(L load, U use, M mid) {
  u32x4 buf[PD + 1];
#pragma unroll
  for (int n = 0; n < PD; ++n) buf[n] = load(n);
  SB();
#pragma unroll
  for (int n = 0; n < N; ++n) {
    if (n + PD < N) buf[(n + PD) % (PD + 1)] = load(n + PD);
    SB();
    use(n, buf[n % (PD + 1)]);
    mid(n);
    SB();
  }
}

DI void wait_flags(const Params& p, bool on, unsigned off, unsigned epoch) {
  const GAS unsigned* f = (const GAS unsigned*)(p.ws + (on ? off : WS_EP));
  for (unsigned spin = 0; spin < SPIN_MAX; ++spin) {
    const unsigned v = __hip_atomic_load(f, __ATOMIC_RELAXED, __HIP_MEMORY_SCOPE_AGENT);
    if (__all(!on || v == epoch)) break;
    __builtin_amdgcn_s_sleep(2);
  }
}

__global__ void __launch_bounds__(640, 3) block_fused(Params p) {
  extern __shared__ __attribute__((aligned(16))) char smem[];
  LAS char* lds = (LAS char*)smem;
  const unsigned lds0 = (unsigned)(size_t)lds;
  const int b = blockIdx.x, tid = threadIdx.x, lane = tid & 63;
  const int w = rfl(tid >> 6);
  const unsigned long long cbase = code_base();
  int i, j; map_block(b, i, j);
  const __amdgpu_buffer_rsrc_t rws = mk_rsrc(p.ws, WS_END);
  ST_DECLQ;
  phase_qkv(p, lds, lds0, cbase, b, i, j, w, lane ST_ARGQ);
  const unsigned epoch = rfl(*(const LAS unsigned*)(lds + K1_EP));
  ST_DECL; STAMP(0);
  const int l15 = lane & 15, g = lane >> 4;
  const unsigned l16 = lane * 16;
  constexpr unsigned OOB = 0xF0000000u;
  const int t32 = 2 * i + (j >> 2), c4 = j & 3, tt0 = 2 * t32;
  const int sid = XMAP == 1 ? (((i & 3) << 1) | (j >> 2)) : XMAP == 2 ? (i & 7) : ((i & 15) >> 1);
  const int rot14 = (sid * 7) >> 2, rot16 = 2 * sid, rotc = XMAP == 1 ? ((i & 3) * 3) >> 1 : (i & 7) % 6;
  auto pcol = [&](int s) { int u = s + rotc; u = u >= 6 ? u - 6 : u; int c = j + 1 + u; return c >= 7 ? c - 7 : c; };
  const u32x4 sentv = {SENT, SENT, SENT, SENT};

  if (w >= 8) {
    const int lw = w - 8;
    if (lw == 0 && lane == 0) __hip_atomic_store((GAS unsigned*)(p.ws + WS_FLG + (i * 8 + j) * 128), epoch, __ATOMIC_RELAXED, __HIP_MEMORY_SCOPE_AGENT);
    { const int t = i - 4 + (lane >> 3);
      if (lw == 0) wait_flags(p, lane < 48 && t >= 0 && t < 32, WS_FLG + (t * 8 + (lane & 7)) * 128, epoch);
      else wait_flags(p, lane < 32, WS_FLG + (lane * 8 + 7) * 128, epoch); }
    STAMP(6);
    BARS();
#pragma unroll
    for (int q = 0; q < 5; ++q) { const int n = 2 * q + lw, hh = n / 5, kt = 4 * i - 16 + 4 * (n % 5);
      dma4(rws, l16, WS_K + ((2 * j + hh) * 128 + (kt < 0 ? 0 : kt)) * KB, lds0 + L_K + n * 4 * KB); }
#pragma unroll
    for (int q = 0; q < 5; ++q) { const int n = 2 * q + lw, hh = n / 5, kb = 2 * i - 8 + 2 * (n % 5);
      dma4(rws, l16, WS_V + ((2 * j + hh) * 64 + (kb < 0 ? 0 : kb)) * 2 * KB, lds0 + L_V + n * 4 * KB); }
    STAMP(1); WAIT_VM(20); STAMP(2); BARS();
    auto wo_slot = [&](int q) { const int n = 2 * q + lw; const unsigned src = WS_WO + (c4 * 16 + ((n + rot16) & 15)) * 7 * KB, dst = lds0 + (n <= 10 ? L_WOA + n * 7 * KB : L_WOB + (n - 11) * 7 * KB);
      dma4(rws, l16, src, dst); dma3(rws, l16, src + 4 * KB, dst + 4 * KB); };
    wo_slot(0); wo_slot(1);
    STAMP(3); WAIT_VM(14); STAMP(4); BARS(); STAMP(5);
#pragma unroll
    for (int q = 2; q < 8; ++q) wo_slot(q);
    WAIT_VM(0); STAMP(7); BARS(); STAMP(8);
    auto w_stage = [&](unsigned wsrc, int q0, int q1) {
#pragma unroll
      for (int q = q0; q < q1; ++q) { const int n = 2 * q + lw; int ks = n + rot14; ks = ks >= 14 ? ks - 14 : ks; const unsigned src = wsrc + (c4 * 14 + ks) * 7 * KB, dst = lds0 + L_W + n * 7 * KB;
        dma4(rws, l16, src, dst); dma3(rws, l16, src + 4 * KB, dst + 4 * KB); } };
    BARS(); STAMP(9);
    w_stage(WS_W1, 0, 2);
    BARS();
    w_stage(WS_W1, 2, 7);
    STAMP(10); WAIT_VM(0); STAMP(11); BARS(); STAMP(12);
    BARS(); STAMP(13);
    w_stage(WS_W2, 0, 2);
    BARS();
    w_stage(WS_W2, 2, 7);
    STAMP(14); WAIT_VM(0); STAMP(15); BARS(); STAMP(16);
    BARS(); STAMP(17);
    if (j == 7) { BARS(); BARS(); ST_FLUSH(1, p.ws, b, w); ST_FLUSHQ(p.ws, b, w); return; }
    const __amdgpu_buffer_rsrc_t rw3 = mk_rsrc(p.w3, D * F * 2), rw4 = mk_rsrc(p.w4, F * D * 2);
    unsigned v3[2][4], v4[4];
#pragma unroll
    for (int k = 0; k < 4; ++k) {
#pragma unroll
      for (int e = 0; e < 2; ++e) { const int rr = lane >> 5, f = (2 * (k & 1) + rr) | (e << 2); v3[e][k] = rr * 3584 + (((lane & 31) ^ (f << 1)) * 16) + k * 6144; }
      const int rr = lane >> 3; v4[k] = rr * 896 + (((lane & 7) ^ (((rr >> 1) & 3) << 1)) * 16) + k * 6144;
    }
    auto w3_round = [&](int r, unsigned slot) { int ks = 2 * r + lw + rot14; ks = ks >= 14 ? ks - 14 : ks;
#pragma unroll
      for (int u = 0; u < 4; ++u) dma4v(rw3, v3[u & 1], (ks * 32 + 8 * u) * 3584 + j * 512, lds0 + slot + (lw * 16 + u * 4) * KB); };
    auto w4_step = [&](int s, unsigned slot) { const int c = s < 6 ? pcol(s) : j;
#pragma unroll
      for (int u = 0; u < 4; ++u) dma4v(rw4, v4, (256 * j + 128 * lw + 32 * u) * 896 + c * 128, lds0 + slot + (lw * 16 + u * 4) * KB); };
    auto arrive = [&](int s) { if (lw == 0 && lane == 0) __hip_atomic_store((GAS unsigned*)(p.ws + WS_PF + ((i * 7 + pcol(s)) * 8 + j) * 128), epoch, __ATOMIC_RELAXED, __HIP_MEMORY_SCOPE_AGENT); };
    w3_round(0, L_RA);
    BARS();
    w3_round(1, L_RB); w3_round(2, L_RC);
    BARS();
    STAMP(18); WAIT_VM(0); STAMP(19); BARS();
    BARS(); STAMP(20);
    BARS();
    w3_round(3, L_RA); w3_round(4, L_RB);
    BARS();
    BARS();
    WAIT_VM(16); BARS(); w3_round(5, L_RC);
    WAIT_VM(16); BARS(); w3_round(6, L_RA);
    WAIT_VM(16); BARS(); w4_step(0, L_RB);
    WAIT_VM(16); BARS(); w4_step(1, L_RC);
    BARS(); w4_step(2, L_RA); w4_step(3, L_RD);
    STAMP(21); WAIT_VM(16); STAMP(22); BARS();
    BARS(); w4_step(4, L_RB);
    BARS(); w4_step(5, L_RC);
    WAIT_VM(32); BARS(); w4_step(6, L_RA);
    WAIT_VM(32); BARS(); arrive(0);
    WAIT_VM(16); BARS(); arrive(1);
    WAIT_VM(0); BARS(); arrive(2);
    BARS(); arrive(3); arrive(4); arrive(5);
    if (b == 0 && lw == 0) {
#pragma unroll
      for (int q = 0; q < 4; ++q) wait_flags(p, true, WS_FLG + (q * 64 + lane) * 128, epoch);
      if (lane == 0) __hip_atomic_store((GAS unsigned*)(p.ws + WS_EP), epoch, __ATOMIC_RELAXED, __HIP_MEMORY_SCOPE_AGENT);
    }
    STAMP(23); ST_FLUSH(1, p.ws, b, w); ST_FLUSHQ(p.ws, b, w);
    return;
  }

  const int hl = w >> 2, a = w & 3, head = 2 * j + hl, tt = 4 * i + a;
  const bool act = w < 7;
  const int wf = act ? w : 6, Fg = 7 * c4 + wf;
  u32x4 qf; u32x2 xr[2]; u32x4 cs1u[2], cs3u[2][2];
  const u32x4 ones = {ONES, ONES, ONES, ONES};
  unsigned act_off = act ? (((tt0 + (g & 1)) * 14 + (Fg >> 1)) * 64 + (2 * (Fg & 1) + (g >> 1)) * 16 + l15) * 16 : OOB;
  const int ka = (2 * w + rot14) % 14, kb14 = (2 * w + 1 + rot14) % 14;
  unsigned pv0 = l16 + ka * KB, pv1 = l16 + kb14 * KB, ov0 = l16 + ((2 * w + rot16) & 15) * KB, ov1 = l16 + ((2 * w + 1 + rot16) & 15) * KB;
  unsigned zv[2];
#pragma unroll
  for (int q = 0; q < 2; ++q) { const int ks = q ? kb14 : ka, f0 = 32 * ks + 8 * g, sh = f0 < 112 ? -1 : f0 < 224 ? 1 : f0 < 336 ? -2 : 2, tz = 16 * (tt0 + 1) + l15 + sh;
    zv[q] = (((tz >> 4) * 14 + ks) * 64 + g * 16 + (tz & 15)) * 16; }
  u32x4 selA, selB;
  { const unsigned v = g == (l15 >> 2) ? ((l15 & 1) ? 0x3F800000u : 0x00003F80u) : 0u, d0 = (l15 & 2) ? 0u : v, d1 = (l15 & 2) ? v : 0u;
    selA = u32x4{d0, d1, 0u, 0u}; selB = u32x4{0u, 0u, d0, d1}; }
  unsigned ooff = ((tt * 16 + head) * 64 + ((g >> 1) + 2 * (g & 1)) * 16 + l15) * 16 + WS_O;
  asm volatile("" : "+v"(act_off), "+v"(pv0), "+v"(pv1), "+v"(ov0), "+v"(ov1), "+v"(zv[0]), "+v"(zv[1]), "+v"(selA), "+v"(selB), "+v"(ooff));
  BARS();
  aload128_sc1(qf, rws, l16, WS_Q + (head * 128 + tt) * KB);
  WAIT_VM(0); RETIRE(qf);
  STAMP(1);
  BARS();
  { const __amdgpu_buffer_rsrc_t rx = mk_rsrc(p.x, S * D * 2);
#pragma unroll
    for (int t = 0; t < 2; ++t) aload64(xr[t], rx, ((16 * (tt0 + t) + l15) * D + 16 * Fg + 4 * g) * 2, 0); }
#pragma unroll
  for (int hf = 0; hf < 2; ++hf) { aload128_sc1(cs1u[hf], rws, (16 * Fg + 4 * g) * 4, WS_CS + hf * 2240 * 4);
#pragma unroll
    for (int f = 0; f < 2; ++f) aload128_sc1(cs3u[hf][f], rws, (448 + 256 * (j < 7 ? j : 6) + 32 * w + 16 * f + 4 * g) * 4, WS_CS + hf * 2240 * 4); }

  u32x4 opiece;
  {
    const int par = a & 1, kl0 = hl * 20 + (a & ~1), vl0 = hl * 20 + (a >> 1) * 2;
    f32x4 sc[18];
    const LAS char* kp = lds + L_K + kl0 * KB + l16;
    const f32x4 zero4 = {0.f, 0.f, 0.f, 0.f};
    lds_pipe<18, 8>([&](int n) { return lds128(kp + n * KB); }, [&](int n, u32x4 kf) { sc[n] = mfma16(kf, qf, zero4); }, [](int) {});
    const int dq = l15 - 4 * g;
    const float NEG = -__builtin_inff();
    if (par == 0) {
#pragma unroll
      for (int r = 0; r < 4; ++r) { const bool ge = dq >= r; sc[0][r] = ge ? NEG : sc[0][r]; sc[16][r] = ge ? sc[16][r] : NEG; sc[17][r] = NEG; }
    } else {
#pragma unroll
      for (int r = 0; r < 4; ++r) { const bool ge = dq >= r; sc[1][r] = ge ? NEG : sc[1][r]; sc[17][r] = ge ? sc[17][r] : NEG; sc[0][r] = NEG; }
    }
    const int nneg = 16 - (tt & ~1);
    if (nneg > 0) {
#pragma unroll
      for (int n = 0; n < 16; ++n) if (n < nneg) sc[n] = f32x4{NEG, NEG, NEG, NEG};
    }
    float m = max3(sc[0][0], sc[0][1], sc[0][2]), m2 = max3(sc[0][3], sc[1][0], sc[1][1]);
    m = max3(m, sc[1][2], sc[1][3]);
#pragma unroll
    for (int n = 2; n < 18; ++n) { m = max3(m, sc[n][0], sc[n][1]); m2 = max3(m2, sc[n][2], sc[n][3]); }
    m = gmax(max3(m, m2, m2));
    const float cexp = 0.18898223650461363f * 1.4426950408889634f, mc = -m * cexp;
    STAMP(14);
    BARS();
    STAMP(15);
    f32x4 o0 = zero4, o1 = zero4;
    const LAS char* vp = lds + L_V + vl0 * KB + l16;
    u32x4 v0 = lds128(vp), v1 = lds128(vp + KB);
#pragma unroll
    for (int mm = 0; mm < 9; ++mm) {
      u32x4 n0, n1;
      if (mm < 8) { n0 = lds128(vp + (2 * mm + 2) * KB); n1 = lds128(vp + (2 * mm + 3) * KB); }
      u32x4 pf;
#pragma unroll
      for (int q = 0; q < 4; ++q) { const f32x4& sv = sc[2 * mm + (q >> 1)];
        pf[q] = pk2(__builtin_amdgcn_exp2f(fmaf(sv[2 * (q & 1)], cexp, mc)), __builtin_amdgcn_exp2f(fmaf(sv[2 * (q & 1) + 1], cexp, mc))); }
      o0 = mfma16(v0, pf, o0);
      o1 = mfma16(v1, pf, o1);
      if (mm < 8) { v0 = n0; v1 = n1; }
      SB();
    }
    const float rl = __builtin_amdgcn_rcpf(gsum(g == 3 ? o1[0] : 0.f));
    opiece = pair_tiles(u32x2{pk2(o0[0] * rl, o0[1] * rl), pk2(o0[2] * rl, o0[3] * rl)}, u32x2{pk2(o1[0] * rl, o1[1] * rl), pk2(o1[2] * rl, o1[3] * rl)});
  }
  STAMP(16);
  WAIT_VM(0); RETIRE(xr[0]); RETIRE(xr[1]); RETIRE(cs1u[0]); RETIRE(cs1u[1]); RETIRE(cs3u[0][0]); RETIRE(cs3u[0][1]); RETIRE(cs3u[1][0]); RETIRE(cs3u[1][1]);
  const f32x4 cs1 = __builtin_bit_cast(f32x4, cs1u[0]) + __builtin_bit_cast(f32x4, cs1u[1]);
  f32x4 cs3[2];
#pragma unroll
  for (int f = 0; f < 2; ++f) cs3[f] = __builtin_bit_cast(f32x4, cs3u[0][f]) + __builtin_bit_cast(f32x4, cs3u[1][f]);
  BARS();
  bst128<16>(opiece, rws, ooff);
  u32x4 wr[16];
#pragma unroll
  for (int n = 0; n < 16; ++n) wr[n] = lds128(lds + (n <= 10 ? L_WOA + n * 7 * KB : L_WOB + (n - 11) * 7 * KB) + wf * KB + l16);
  STAMP(2);
  WAIT_VM(0);

  { u32x4 v[2][2];
    fetch_payload<2, 16>(v, rws, ov0, ov1, WS_O + tt0 * 16 * KB, 16 * KB, lds, lds0, L_O + 2 * w * KB, lane, [] { BARS();   }, p.ws + (40u << 20)); }
  STAMP(3);
  BARS();
  u32x2 hp[2];
  {
    f32x4 acc[2];
#pragma unroll
    for (int t = 0; t < 2; ++t) acc[t] = mfma16(selA, u32x4{xr[t][0], xr[t][1], 0u, 0u}, f32x4{0.f, 0.f, 0.f, 0.f});
    const LAS char* op = lds + L_O + l16;
    lds_pipe<32, 8>([&](int n) { return lds128(op + ((n & 1) * 16 + (n >> 1)) * KB); }, [&](int n, u32x4 f) { acc[n & 1] = mfma16(wr[n >> 1], f, acc[n & 1]); }, [](int) {});
#pragma unroll
    for (int t = 0; t < 2; ++t) hp[t] = u32x2{pk2(acc[t][0], acc[t][1]), pk2(acc[t][2], acc[t][3])};
    bst128<16>(pair_tiles(hp[0], hp[1]), rws, act_off + WS_H);
  }
  STAMP(4);
  WAIT_VM(0);

  LAS float* st4 = (LAS float*)(lds + L_ST4);
  auto stage_weights = [&] { BARS();
#pragma unroll
    for (int n = 0; n < 14; ++n) wr[n] = lds128(lds + L_W + (n * 7 + wf) * KB + l16);
    BARS(); };
  { u32x4 v[2][2] = {};
    if (act) fetch_payload<2, 14>(v, rws, pv0, pv1, WS_H + tt0 * 14 * KB, 14 * KB, lds, lds0, L_H + 2 * w * KB, lane, stage_weights, p.ws + (40u << 20) + 4); else stage_weights();
    row_stats<2>(v, ones, st4, w, l15, g); }
  STAMP(5);
  BARS();
  f32x2 yv[2][2];
  {
    const LAS char* hpz = lds + L_H + l16;
    f32x4 pt[2];
#pragma unroll
    for (int t = 0; t < 2; ++t) pt[t] = *(const LAS f32x4*)(st4 + ((t * 16 + l15) * 8 + 2 * g) * 2);
    f32x4 acc[2] = {};
    lds_pipe<28, 8>([&](int n) { return lds128(hpz + ((n & 1) * 14 + (n >> 1)) * KB); }, [&](int n, u32x4 f) { acc[n & 1] = mfma16(wr[n >> 1], f, acc[n & 1]); }, [](int) {});
    u32x2 zp[2];
#pragma unroll
    for (int t = 0; t < 2; ++t) {
      const float s1 = gsum(pt[t][0] + pt[t][2]), s2 = gsum(pt[t][1] + pt[t][3]);
      const float mean = s1 * (1.f / D), var = fmaxf(s2 * (1.f / D) - mean * mean, 0.f);
      const float r1 = __builtin_amdgcn_rsqf(var + 1e-5f);
      const float r12 = r1 * __builtin_amdgcn_rsqf(var * r1 * r1 + 1e-5f);
      const f32x2 m2 = {mean, mean};
#pragma unroll
      for (int q = 0; q < 2; ++q) {
        yv[t][q] = (f32x2{bflo(hp[t][q]), bfhi(hp[t][q])} - m2) * f32x2{r1, r1};
        zp[t][q] = gelu2((f32x2{acc[t][2 * q], acc[t][2 * q + 1]} - m2 * f32x2{cs1[2 * q], cs1[2 * q + 1]}) * f32x2{r12, r12});
      }
    }
    bst128<16>(pair_tiles(zp[0], zp[1]), rws, act_off + WS_Z + 14 * KB);
  }
  STAMP(6);
  WAIT_VM(0);

  { u32x4 v[2][2];
    if (act) fetch_payload<2, 14>(v, rws, zv[0], zv[1], WS_Z, 14 * KB, lds, lds0, L_Z + 2 * w * KB, lane, stage_weights, p.ws + (40u << 20) + 8); else stage_weights(); }
  STAMP(7);
  BARS();
  {
    f32x4 acc[2] = {};
    const LAS char* zb = lds + L_Z + l16;
    lds_pipe<28, 8>([&](int n) { return lds128(zb + ((n & 1) * 14 + (n >> 1)) * KB); }, [&](int n, u32x4 f) { acc[n & 1] = mfma16(wr[n >> 1], f, acc[n & 1]); }, [](int) {});
    BARS();
    u32x2 xp[2];
#pragma unroll
    for (int t = 0; t < 2; ++t) xp[t] = u32x2{pk2(yv[t][0][0] + acc[t][0], yv[t][0][1] + acc[t][1]), pk2(yv[t][1][0] + acc[t][2], yv[t][1][1] + acc[t][3])};
    bst128<16>(pair_tiles(xp[0], xp[1]), rws, act_off + WS_X1);
  }
  STAMP(8);
  if (j == 7) { ST_FLUSH(1, p.ws, b, w); ST_FLUSHQ(p.ws, b, w); return; }
  WAIT_VM(0);

  LAS float* st6 = (LAS float*)(lds + L_ST6);
  const int q4 = l15 >> 2, pp = lane & 3;
  const LAS char* a3[2];
#pragma unroll
  for (int f = 0; f < 2; ++f) a3[f] = lds + (8 * g + q4) * 512 + (((2 * w + f) ^ (q4 | ((g & 1) << 2))) * 32) + pp * 8;
  auto w3frag = [&](unsigned off, int f) { const u32x2 lo = tr_read(a3[f] + off), hi = tr_read(a3[f] + off + 2048); return u32x4{lo[0], lo[1], hi[0], hi[1]}; };
  u32x4 areg[4][2];
  { u32x4 v[4][2] = {};
    auto w3_regs = [&] { BARS();
#pragma unroll
      for (int n = 0; n < 4; ++n)
#pragma unroll
        for (int f = 0; f < 2; ++f) areg[n][f] = w3frag(n * 16 * KB, f);
      BARS(); };
    if (act) fetch_payload<4, 14>(v, rws, pv0, pv1, WS_X1 + 4 * i * 14 * KB, 14 * KB, lds, lds0, L_X1 + 2 * w * KB, lane, w3_regs, p.ws + (40u << 20) + 12); else w3_regs();
    row_stats<4>(v, ones, st6, w, l15, g); }
  STAMP(9);
  BARS();
  {
    const LAS char* xp = lds + L_X1 + l16;
    f32x4 pt[4];
#pragma unroll
    for (int t = 0; t < 4; ++t) pt[t] = *(const LAS f32x4*)(st6 + ((t * 16 + l15) * 8 + 2 * g) * 2);
    f32x4 acc[2][4] = {};
    u32x4 bfr[2][4], afr[2][2];
    auto load_k = [&](int n) { const unsigned slot = ((n >> 1) % 3 == 2 ? L_RC : (n >> 1) % 3 == 0 ? L_RA : L_RB) + (n & 1) * 16 * KB;
#pragma unroll
      for (int f = 0; f < 2; ++f) afr[n & 1][f] = n < 4 ? areg[n][f] : w3frag(slot, f);
#pragma unroll
      for (int t = 0; t < 4; ++t) bfr[n & 1][t] = lds128(xp + (t * 14 + n) * KB); };
    load_k(0);
    SB();
#pragma unroll
    for (int n = 0; n < 14; ++n) {
      if (n & 1) BARS();
      if (n < 13) load_k(n + 1);
      SB();
#pragma unroll
      for (int t = 0; t < 4; ++t) { acc[0][t] = mfma16(afr[n & 1][0], bfr[n & 1][t], acc[0][t]); acc[1][t] = mfma16(afr[n & 1][1], bfr[n & 1][t], acc[1][t]); }
      SB();
    }
    STAMP(20);
    float mean[4], rs[4];
#pragma unroll
    for (int t = 0; t < 4; ++t) {
      const float s1 = gsum(pt[t][0] + pt[t][2]), s2 = gsum(pt[t][1] + pt[t][3]);
      mean[t] = s1 * (1.f / D); rs[t] = __builtin_amdgcn_rsqf(fmaxf(s2 * (1.f / D) - mean[t] * mean[t], 0.f) + 1e-5f);
    }
#pragma unroll
    for (int t = 0; t < 4; ++t) {
      u32x4 hv;
#pragma unroll
      for (int q = 0; q < 4; ++q) { const int f = q >> 1, r = 2 * (q & 1);
        hv[q] = gelu2((f32x2{acc[f][t][r], acc[f][t][r + 1]} - f32x2{mean[t], mean[t]} * f32x2{cs3[f][r], cs3[f][r + 1]}) * f32x2{rs[t], rs[t]}); }
      *(LAS u32x4*)(lds + L_HID + (t * 8 + w) * KB + l16) = hv;
    }
  }
  STAMP(10);
  BARS();
  STAMP(11);
  const int tb = w & 1, ft = w >> 1;
  u32x4 hf[2][8];
#pragma unroll
  for (int ks = 0; ks < 8; ++ks)
#pragma unroll
    for (int t = 0; t < 2; ++t) hf[t][ks] = lds128(lds + L_HID + ((2 * tb + t) * 8 + ks) * KB + l16);
  const unsigned pbase = l16 + WS_P + ((i * 49 + j) * 8 + w) * KB;
  const int Fo = 4 * j + ft;
  u32x2 hres[2], xres[2];
  unsigned ro[2];
#pragma unroll
  for (int t = 0; t < 2; ++t) { ro[t] = (((4 * i + 2 * tb + t) * 14 + (Fo >> 1)) * 64 + (2 * (Fo & 1) + (g >> 1)) * 16 + l15) * 16 + (g & 1) * 8;
    aload64_sc1(hres[t], rws, ro[t], WS_H); aload64_sc1(xres[t], rws, ro[t], WS_X1); }
  f32x4 acc[2] = {};
  {
    constexpr int PD = 4;
    constexpr unsigned slots[7] = {L_RB, L_RC, L_RA, L_RD, L_RB, L_RC, L_RA};
    const LAS char* wp = lds + (4 * g + q4) * 128 + ((ft ^ ((2 * g + (q4 >> 1)) & 3)) * 32) + pp * 8;
    auto w4frag = [&](unsigned off) { const u32x2 lo = tr_read(wp + off), hi = tr_read(wp + off + 2048); return u32x4{lo[0], lo[1], hi[0], hi[1]}; };
    u32x4 buf[PD + 1];
#pragma unroll
    for (int m = 0; m < PD; ++m) buf[m] = w4frag(slots[0] + m * 4096);
    SB();
#pragma unroll
    for (int m = 0; m < 56; ++m) {
      const int s = m >> 3, ks = m & 7;
      if (ks == 8 - PD && s < 6) { if (s >= 3) WAIT_VM(2); BARS(); }
      if (m + PD < 56) buf[(m + PD) % (PD + 1)] = w4frag(slots[(m + PD) >> 3] + ((m + PD) & 7) * 4096);
      SB();
      acc[0] = mfma16(buf[m % (PD + 1)], hf[0][ks], acc[0]); acc[1] = mfma16(buf[m % (PD + 1)], hf[1][ks], acc[1]);
      if (ks == 7 && s < 6) {
        bst128<16>(u32x4{pk2(acc[0][0], acc[0][1]), pk2(acc[0][2], acc[0][3]), pk2(acc[1][0], acc[1][1]), pk2(acc[1][2], acc[1][3])}, rws, pbase + pcol(s) * 56 * KB);
        acc[0] = f32x4{0.f, 0.f, 0.f, 0.f}; acc[1] = acc[0];
      }
      SB();
    }
  }
  STAMP(12);
  {
    const __amdgpu_buffer_rsrc_t rout = mk_rsrc(p.out, S * D * 2);
    u32x4 pv[6];
    const unsigned pin = WS_P + ((i * 7 + j) * 7 * 8 + w) * KB;
    WAIT_VM(0); RETIRE(hres[0]); RETIRE(hres[1]); RETIRE(xres[0]); RETIRE(xres[1]);
    BARS();
    for (unsigned spin = 0; spin < SPIN_MAX; ++spin) {
      const unsigned m = max(max(max(hres[0][0], hres[0][1]), max(xres[0][0], xres[0][1])), max(max(hres[1][0], hres[1][1]), max(xres[1][0], xres[1][1])));
      if (!__any(m == SENT)) break;
#pragma unroll
      for (int t = 0; t < 2; ++t) { aload64_sc1(hres[t], rws, ro[t], WS_H); aload64_sc1(xres[t], rws, ro[t], WS_X1); }
      WAIT_VM(0); RETIRE(hres[0]); RETIRE(hres[1]); RETIRE(xres[0]); RETIRE(xres[1]);
    }
    wait_flags(p, lane < 7 && lane != j, WS_PF + ((i * 7 + j) * 8 + lane) * 128, epoch);
#pragma unroll
    for (int s = 0; s < 6; ++s) { int js = j + 1 + s; js = js >= 7 ? js - 7 : js; aload128_sc1(pv[s], rws, l16, pin + js * 8 * KB); }
    WAIT_VM(0);
#pragma unroll
    for (int s = 0; s < 6; ++s) RETIRE(pv[s]);
    const u32x4 selAB = selA | selB;
#pragma unroll
    for (int s = 0; s < 6; ++s) { acc[0] = mfma16(selA, pv[s], acc[0]); acc[1] = mfma16(selB, pv[s], acc[1]); }
#pragma unroll
    for (int t = 0; t < 2; ++t) {
      acc[t] = mfma16(selAB, u32x4{hres[t][0], hres[t][1], xres[t][0], xres[t][1]}, acc[t]);
      __builtin_amdgcn_raw_buffer_store_b64(u32x2{pk2(acc[t][0], acc[t][1]), pk2(acc[t][2], acc[t][3])}, rout, ((16 * (4 * i + 2 * tb + t) + l15) * D + 16 * Fo + 4 * g) * 2, 0, 0);
    }
  }
  STAMP(13);
  ST_FLUSH(1, p.ws, b, w); ST_FLUSHQ(p.ws, b, w);
}

extern "C" void kernel_launch(void* const* d_in, const int* in_sizes, int n_in, void* d_out, int out_size, void* d_ws, size_t ws_size, hipStream_t stream) {
  Params p;
  p.x = (const u16*)d_in[0]; p.wqkv = (const u16*)d_in[1]; p.wo = (const u16*)d_in[2];
  p.w1 = (const u16*)d_in[4]; p.w2 = (const u16*)d_in[5]; p.w3 = (const u16*)d_in[6]; p.w4 = (const u16*)d_in[7];
  p.out = (u16*)d_out; p.ws = (char*)d_ws;
  (void)hipFuncSetAttribute((const void*)block_fused, hipFuncAttributeMaxDynamicSharedMemorySize, K2_LDS);
  block_fused<<<256, 640, K2_LDS, stream>>>(p);
}
```

```cpp
#include <hip/hip_runtime.h>
#include <hip/hip_bf16.h>
#include <stdint.h>

typedef unsigned short u16;
typedef __attribute__((ext_vector_type(8))) __bf16 bf16x8;
typedef __attribute__((ext_vector_type(2))) __bf16 bf16x2;
typedef __attribute__((ext_vector_type(4))) short s16x4;
typedef __attribute__((ext_vector_type(4))) float f32x4;
typedef __attribute__((ext_vector_type(16))) float f32x16;
typedef __attribute__((ext_vector_type(4))) unsigned u32x4;
typedef __attribute__((ext_vector_type(2))) unsigned u32x2;
#define LAS __attribute__((address_space(3)))
#define GAS __attribute__((address_space(1)))
#define DI __device__ __forceinline__

constexpr int S = 2048, D = 448, NH = 16, DH = 28, F = 1792;
constexpr unsigned SENT = 0xFFFFFFFFu;

constexpr unsigned KB = 1024;
constexpr unsigned WS_Q   = 0;
constexpr unsigned WS_K   = WS_Q + 2048 * KB;
constexpr unsigned WS_V   = WS_K + 2048 * KB;
constexpr unsigned WS_WO  = WS_V + 2048 * KB;
constexpr unsigned WS_W1  = WS_WO + 448 * KB;
constexpr unsigned WS_W2  = WS_W1 + 392 * KB;
constexpr unsigned WS_O   = WS_W2 + 392 * KB;
constexpr unsigned WS_H   = WS_O + 2048 * KB;
constexpr unsigned WS_X1  = WS_H + 1792 * KB;
constexpr unsigned WS_Z   = WS_X1 + 1792 * KB;
constexpr unsigned WS_P   = WS_Z + 1820 * KB;
constexpr unsigned WS_CS  = WS_P + 32 * 49 * 8 * KB;
constexpr unsigned WS_PF  = WS_CS + 18 * KB;
constexpr unsigned WS_FLG = WS_PF + 224 * KB;
constexpr unsigned WS_EP  = WS_FLG + 32 * KB;
constexpr unsigned WS_END = WS_EP + KB;

struct Params {
  const u16* x; const u16* wqkv; const u16* wo; const u16* w1; const u16* w2; const u16* w3; const u16* w4;
  u16* out; char* ws;
};

DI __amdgpu_buffer_rsrc_t mk_rsrc(const void* p, unsigned bytes) { return __builtin_amdgcn_make_buffer_rsrc((void*)p, (short)0, (int)bytes, 0x00020000); }
DI void dma16(__amdgpu_buffer_rsrc_t rs, unsigned voff, unsigned soff, unsigned ldsaddr) {
  asm volatile("s_mov_b32 m0, %0\n\ts_nop 0\n\tbuffer_load_dwordx4 %1, %2, %3 offen lds" :: "s"(ldsaddr), "v"(voff), "s"(rs), "s"(soff) : "memory");
}
DI void dma16_sc1(__amdgpu_buffer_rsrc_t rs, unsigned voff, unsigned soff, unsigned ldsaddr) {
  asm volatile("s_mov_b32 m0, %0\n\ts_nop 0\n\tbuffer_load_dwordx4 %1, %2, %3 offen sc1 lds" :: "s"(ldsaddr), "v"(voff), "s"(rs), "s"(soff) : "memory");
}
DI void dma4(__amdgpu_buffer_rsrc_t rs, unsigned voff, unsigned soff, unsigned ldsaddr) {
  asm volatile("s_mov_b32 m0, %0\n\ts_nop 0\n\tbuffer_load_dwordx4 %1, %2, %3 offen sc1 lds\n\tbuffer_load_dwordx4 %1, %2, %3 offen offset:1024 sc1 lds\n\t"
               "buffer_load_dwordx4 %1, %2, %3 offen offset:2048 sc1 lds\n\tbuffer_load_dwordx4 %1, %2, %3 offen offset:3072 sc1 lds" :: "s"(ldsaddr), "v"(voff), "s"(rs), "s"(soff) : "memory");
}
DI void dma3(__amdgpu_buffer_rsrc_t rs, unsigned voff, unsigned soff, unsigned ldsaddr) {
  asm volatile("s_mov_b32 m0, %0\n\ts_nop 0\n\tbuffer_load_dwordx4 %1, %2, %3 offen sc1 lds\n\tbuffer_load_dwordx4 %1, %2, %3 offen offset:1024 sc1 lds\n\t"
               "buffer_load_dwordx4 %1, %2, %3 offen offset:2048 sc1 lds" :: "s"(ldsaddr), "v"(voff), "s"(rs), "s"(soff) : "memory");
}
DI void dma4v(__amdgpu_buffer_rsrc_t rs, const unsigned (&v)[4], unsigned soff, unsigned ldsaddr) {
  asm volatile("s_mov_b32 m0, %0\n\ts_nop 0\n\tbuffer_load_dwordx4 %1, %5, %6 offen lds\n\tbuffer_load_dwordx4 %2, %5, %6 offen offset:1024 lds\n\t"
               "buffer_load_dwordx4 %3, %5, %6 offen offset:2048 lds\n\tbuffer_load_dwordx4 %4, %5, %6 offen offset:3072 lds"
               :: "s"(ldsaddr), "v"(v[0]), "v"(v[1]), "v"(v[2]), "v"(v[3]), "s"(rs), "s"(soff) : "memory");
}
#define WAIT_VM(n) asm volatile("s_waitcnt vmcnt(" #n ")" ::: "memory")
#define BAR() asm volatile("s_waitcnt lgkmcnt(0)\n\ts_barrier" ::: "memory")
#define BARS() do { __builtin_amdgcn_sched_barrier(0); BAR(); __builtin_amdgcn_sched_barrier(0); } while (0)
DI unsigned rfl(unsigned v) { return __builtin_amdgcn_readfirstlane(v); }
DI float bflo(unsigned u) { return __uint_as_float(u << 16); }
DI float bfhi(unsigned u) { return __uint_as_float(u & 0xffff0000u); }
DI unsigned pk2(float a, float b) { bf16x2 v; v[0] = (__bf16)a; v[1] = (__bf16)b; return __builtin_bit_cast(unsigned, v); }
DI float dot2(unsigned a, unsigned b, float c) { return __builtin_amdgcn_fdot2_f32_bf16(__builtin_bit_cast(bf16x2, a), __builtin_bit_cast(bf16x2, b), c, false); }
DI f32x16 mfma32(u32x4 a, u32x4 b, f32x16 c) { return __builtin_amdgcn_mfma_f32_32x32x16_bf16(__builtin_bit_cast(bf16x8, a), __builtin_bit_cast(bf16x8, b), c, 0, 0, 0); }
DI f32x4 mfma16(u32x4 a, u32x4 b, f32x4 c) { return __builtin_amdgcn_mfma_f32_16x16x32_bf16(__builtin_bit_cast(bf16x8, a), __builtin_bit_cast(bf16x8, b), c, 0, 0, 0); }
DI u32x2 tr_read(const LAS char* p) { return __builtin_bit_cast(u32x2, __builtin_amdgcn_ds_read_tr16_b64_v4i16((LAS s16x4*)p)); }
DI u32x4 lds128(const LAS char* p) { return *(const LAS u32x4*)p; }
template <int AUX> DI void bst128(u32x4 v, __amdgpu_buffer_rsrc_t rs, unsigned off) { __builtin_amdgcn_raw_buffer_store_b128(v, rs, off, 0, AUX); }
#define XJ j
constexpr unsigned ONES = 0x3F803F80u;
#define AUX_SIDE 16
#define AUX_QKV 16
#define ST_DECL
#define ST_PARAM
#define ST_DECLQ
#define ST_ARGQ
#define ST_FLUSHQ(ws, b, w)
#define CSTAMP(n)
#define STAMP(n)
#define ST_FLUSH(kern, ws, b, w)

DI void aload128(u32x4& d, __amdgpu_buffer_rsrc_t rs, unsigned voff, unsigned soff) { asm volatile("buffer_load_dwordx4 %0, %1, %2, %3 offen" : "=v"(d) : "v"(voff), "s"(rs), "s"(soff) : "memory"); }
DI void aload64(u32x2& d, __amdgpu_buffer_rsrc_t rs, unsigned voff, unsigned soff) { asm volatile("buffer_load_dwordx2 %0, %1, %2, %3 offen" : "=v"(d) : "v"(voff), "s"(rs), "s"(soff) : "memory"); }
DI void aload128_sc1(u32x4& d, __amdgpu_buffer_rsrc_t rs, unsigned voff, unsigned soff) { asm volatile("buffer_load_dwordx4 %0, %1, %2, %3 offen sc1" : "=v"(d) : "v"(voff), "s"(rs), "s"(soff) : "memory"); }
DI void aload64_sc1(u32x2& d, __amdgpu_buffer_rsrc_t rs, unsigned voff, unsigned soff) { asm volatile("buffer_load_dwordx2 %0, %1, %2, %3 offen sc1" : "=v"(d) : "v"(voff), "s"(rs), "s"(soff) : "memory"); }
#define RETIRE(x) asm volatile("; RETIRE %0" : "+v"(x))
constexpr unsigned CODE_BYTES = 50 * 1024;
DI unsigned touch_code(unsigned long long base, unsigned bytes, int b, int lane, bool on) {
  const __amdgpu_buffer_rsrc_t rc = mk_rsrc((const void*)base, bytes);
  const unsigned per = (bytes / 128 + 31) / 32, line = (b >> 3) * per + lane;
  unsigned d;
  asm volatile("buffer_load_dword %0, %1, %2, 0 offen" : "=v"(d) : "v"(on && lane < (int)per ? line * 128 : 0xF0000000u), "s"(rc) : "memory");
  return d;
}
DI unsigned long long code_base() { unsigned long long pc; asm volatile("s_getpc_b64 %0" : "=s"(pc)); return pc & ~0xFFull; }

#define ROTQ 0
#define XMAP 2
DI void map_block(int b, int& i, int& j) { const int x = b & 7, s = b >> 3; constexpr int JPX = 8 / XMAP; i = (x / XMAP) * 4 * XMAP + s / JPX; j = (x % XMAP) * JPX + s % JPX; }

constexpr unsigned K1_XS = 0;
constexpr unsigned K1_W0 = 7 * 8192;
constexpr unsigned K1_SLOT = 24576;
constexpr unsigned K1_ST = K1_W0 + 4 * K1_SLOT;
constexpr unsigned K1_CS = K1_ST + 512;
constexpr unsigned K1_EP = K1_CS + 2 * 2 * 3 * 32 * 4;

DI void retile_load(const Params& p, int mat, int ks, int cb, int lane, u32x4 (&v)[4]) {
  const u16* W = mat == 0 ? p.wo : mat == 1 ? p.w1 : mat == 2 ? p.w2 : p.w3;
  const unsigned ldw = mat == 3 ? F : D, nrows = D, krow0 = (mat == 0 ? 28 : 32) * ks;
#pragma unroll
  for (int q = 0; q < 4; ++q) {
    unsigned row = krow0 + (lane >> 3) + 8 * q; row = row < nrows ? row : nrows - 1;
    v[q] = *(const u32x4*)(W + (size_t)row * ldw + cb * 64 + (lane & 7) * 8);
  }
}
template <bool SUMS>
DI void retile_store(const Params& p, LAS char* scr, int mat, int ks, int cb, int lane, const u32x4 (&v)[4], f32x4 (&cs)[4]) {
#pragma unroll
  for (int q = 0; q < 4; ++q) *(LAS u32x4*)(scr + q * 1024 + lane * 16) = v[q];
  const int g = lane >> 4, q4 = (lane & 15) >> 2, pp = lane & 3;
  const int r0 = 8 * g + q4, r1 = 8 * g + 4 + q4;
  const __amdgpu_buffer_rsrc_t rws = mk_rsrc(p.ws, WS_END);
  const u32x4 ones = {ONES, ONES, ONES, ONES};
#pragma unroll
  for (int ft = 0; ft < 4; ++ft) {
    u32x2 lo = tr_read(scr + r0 * 128 + ft * 32 + pp * 8), hi = tr_read(scr + r1 * 128 + ft * 32 + pp * 8);
    if (mat == 0 && g == 3) { hi[0] = 0u; hi[1] = 0u; }
    const u32x4 f = {lo[0], lo[1], hi[0], hi[1]};
    const int Fi = 4 * cb + ft;
    unsigned dst;
    if (mat == 0) dst = WS_WO + (((Fi / 7) * 16 + ks) * 7 + Fi % 7) * KB;
    else dst = (mat == 1 ? WS_W1 : WS_W2) + (((Fi / 7) * 14 + ks) * 7 + Fi % 7) * KB;
    if (mat != 3) bst128<AUX_SIDE>(f, rws, lane * 16 + dst);
    if (SUMS) cs[ft] = mfma16(f, ones, cs[ft]);
  }
}

DI int qk_slot(int n0, int& half, bool& last) {
  const int head = n0 / 28, hs = 28 * head, bnd = (hs / 64 + 1) * 64;
  const bool split = bnd < hs + 28, second = split && n0 >= bnd;
  const int seg0 = second ? bnd : hs, seg1 = (split && !second) ? bnd : hs + 28, off = n0 - seg0;
  half = (off >> 2) & 1;
  last = n0 + 4 == seg1 && half == 0;
  return 4 * head + (second ? (bnd - hs + 7) >> 3 : 0) + (off >> 3);
}
DI void fill_sentinels(__amdgpu_buffer_rsrc_t rws, int i, int j, int lw, int lane) {
  const u32x4 sv = {SENT, SENT, SENT, SENT};
  const unsigned l16 = lane * 16, tt0 = 4 * i + 2 * (j >> 2), F0 = 7 * (j & 3);
#pragma unroll
  for (int t = 0; t < 2; ++t) { const unsigned o = WS_O + ((4 * i + 2 * lw + t) * 16 + 2 * j) * KB + l16; bst128<16>(sv, rws, o); bst128<16>(sv, rws, o + KB); }
  const unsigned o = ((tt0 + lw) * 14 + (F0 >> 1)) * KB + (F0 & 1) * 512 + l16;
#pragma unroll
  for (int u = 0; u < 3; ++u) { const unsigned base = u == 0 ? WS_H : u == 1 ? WS_X1 : WS_Z + 14 * KB;
    bst128<16>(sv, rws, o + base); bst128<16>(sv, rws, o + base + KB); bst128<16>(sv, rws, o + base + 2 * KB); bst128<16>(sv, rws, lane < 32 ? o + base + 3 * KB : 0xF0000000u); }
}

DI void phase_qkv(const Params& p, LAS char* lds, unsigned lds0, unsigned long long cbase, int b, int i, int j, int w, int lane ST_PARAM) {
  STAMP(0);
  unsigned epv = 0, cwarm = 0, twarm = 0;
  auto errands = [&] {
    asm volatile("buffer_load_dword %0, %1, %2, 0 offen sc1" : "=v"(epv) : "v"(WS_EP), "s"(mk_rsrc(p.ws, WS_END)) : "memory");
    cwarm = touch_code(cbase, CODE_BYTES, b, lane, true);
  };
  auto errands_done = [&] { WAIT_VM(0); RETIRE(epv); RETIRE(cwarm); RETIRE(twarm); if (lane == 0) *(LAS unsigned*)(lds + K1_EP) = epv + 1u; };

  if (j == 7) {
    const __amdgpu_buffer_rsrc_t rws = mk_rsrc(p.ws, WS_END);
    if (w >= 8) {
      const int lw = w - 8;
      if (lw == 1) { errands(); errands_done(); }
      fill_sentinels(rws, i, j, lw, lane);
#pragma unroll
      for (int n = 0; n < 4; ++n) { const int pr = (lw * 4 + n) * 4 + (lane >> 4), gq = (lane >> 2) & 3, row = lane & 3; const unsigned o = row == 0 ? ONES : 0u;
        bst128<16>(u32x4{o, o, o, o}, rws, WS_V + ((((pr >> 1) * 64 + 2 * i + (pr & 1)) * 2 + 1) * 64 + gq * 16 + 12 + row) * 16); }
      if (i == 0 || i == 31) {
#pragma unroll
        for (int n = 0; n < 7; ++n) bst128<16>(u32x4{0u, 0u, 0u, 0u}, rws, WS_Z + ((i == 0 ? 0 : 129) * 14 + lw * 7 + n) * KB + lane * 16); }
    } else {
    const int colid = w < 2 ? w * 32 + i : (w == 2 && i < 6) ? 64 + i : -1;
    const int nid = w >= 3 ? i * 5 + (w - 3) : 160 + (i - 6);
    u32x4 v[7][4];
    auto other = [&](int m, int& mat, int& ks, int& cb) { if (m < 112) { mat = 0; ks = m / 7; cb = m % 7; } else { mat = 2; ks = (m - 112) / 7; cb = (m - 112) % 7; } };
    const int ccol = colid >> 1, chalf = colid & 1, cmat = ccol < 7 ? 1 : 3, ccb = ccol < 7 ? ccol : ccol - 7;
    f32x4 cs[4] = {};
    if (colid >= 0) {
#pragma unroll
      for (int q = 0; q < 7; ++q) retile_load(p, cmat, 7 * chalf + q, ccb, lane, v[q]);
#pragma unroll
      for (int q = 0; q < 7; ++q) retile_store<true>(p, lds + (2 * w + (q & 1)) * 4096, cmat, 7 * chalf + q, ccb, lane, v[q], cs);
      if ((lane & 15) == 0) {
#pragma unroll
        for (int ft = 0; ft < 4; ++ft) bst128<16>(__builtin_bit_cast(u32x4, cs[ft]), rws, WS_CS + (chalf * 2240 + (cmat == 1 ? 0 : 448) + 64 * ccb + 16 * ft + 4 * (lane >> 4)) * 4); }
    } else {
#pragma unroll
      for (int q = 0; q < 2; ++q) { const int m = nid + 186 * q; if (m < 210) { int mat, ks, cb; other(m, mat, ks, cb); retile_load(p, mat, ks, cb, lane, v[q]); } }
#pragma unroll
      for (int q = 0; q < 2; ++q) { const int m = nid + 186 * q; if (m < 210) { int mat, ks, cb; other(m, mat, ks, cb); retile_store<false>(p, lds + (2 * w + (q & 1)) * 4096, mat, ks, cb, lane, v[q], cs); } }
    }
    }
    STAMP(7);
    WAIT_VM(0); BARS();
    return;
  }
  if (w == 6 || w == 7) {
    if (w == 7) {
      errands();
#pragma unroll
      for (int c = 0; c < 3; ++c) BARS();
      errands_done();
    } else {
#pragma unroll
      for (int c = 0; c < 3; ++c) BARS();
    }
    fill_sentinels(mk_rsrc(p.ws, WS_END), i, j, w - 6, lane);
#pragma unroll
    for (int c = 3; c < 9; ++c) BARS();
    WAIT_VM(0); BARS();
    return;
  }
  if (w == 4 || w == 5) {
    const int tok = 32 * (w - 4) + (lane & 31), part = lane >> 5;
    const LAS char* xb[4];
#pragma unroll
    for (int q = 0; q < 4; ++q) xb[q] = lds + K1_XS + tok * 128 + ((4 * part + q) ^ ((tok >> 1) & 7)) * 16;
    float s1a = 0.f, s1b = 0.f, s2a = 0.f, s2b = 0.f;
    BARS();
#pragma unroll
    for (int c = 0; c < 7; ++c) {
      u32x4 v[4];
#pragma unroll
      for (int q = 0; q < 4; ++q) v[q] = lds128(xb[q] + c * 8192);
      __builtin_amdgcn_sched_barrier(0);
#pragma unroll
      for (int q = 0; q < 4; ++q)
#pragma unroll
        for (int u = 0; u < 4; ++u) { float lo = bflo(v[q][u]), hi = bfhi(v[q][u]); asm volatile("" : "+v"(lo), "+v"(hi));
          s1a += lo; s1b += hi; s2a = fmaf(lo, lo, s2a); s2b = fmaf(hi, hi, s2b); }
      asm volatile("" : "+v"(s1a), "+v"(s1b), "+v"(s2a), "+v"(s2b));
      if (c == 6) {
        float a1 = s1a + s1b, a2 = s2a + s2b;
        a1 += __shfl_xor(a1, 32); a2 += __shfl_xor(a2, 32);
        const float mean = a1 * (1.f / D), var = fmaxf(a2 * (1.f / D) - mean * mean, 0.f);
        if (lane < 32) { *(LAS float*)(lds + K1_ST + tok * 4) = mean; *(LAS float*)(lds + K1_ST + 256 + tok * 4) = __builtin_amdgcn_rsqf(var + 1e-5f); }
      }
      BARS();
    }
    BARS();
    STAMP(7);
    WAIT_VM(0); BARS();
    return;
  }
  if (w >= 8) {
    const int lw = w - 8;
    const __amdgpu_buffer_rsrc_t rx = mk_rsrc(p.x, S * D * 2), rw = mk_rsrc(p.wqkv, D * 3 * D * 2);
    const unsigned xv0 = (lane >> 3) * 896 + ((lane & 7) ^ (lane >> 4)) * 16, xv1 = (lane >> 3) * 896 + ((lane & 7) ^ (lane >> 4) ^ 4) * 16;
    const unsigned wv = (lane >> 3) * 2688 + (((lane & 7) ^ (((lane >> 4) & 1) * 4)) * 16);
    const int rotq = ROTQ;
    auto issue_chunk = [&](int c) {
      int cc = c + rotq; cc = cc >= 7 ? cc - 7 : cc;
#pragma unroll
      for (int q = 0; q < 4; ++q) { const unsigned n = lw * 4 + q; dma16(rx, (q & 1) ? xv1 : xv0, (64 * i + 8 * n) * 896 + cc * 128, lds0 + K1_XS + c * 8192 + n * 1024); }
#pragma unroll
      for (int q = 0; q < 12; ++q) { const unsigned n = lw * 12 + q, type = n >> 3, rg = n & 7;
        dma16(rw, wv, (64 * cc + 8 * rg) * 2688 + type * 896 + j * 128, lds0 + K1_W0 + (c & 3) * K1_SLOT + type * 8192 + rg * 1024); }
    };
    issue_chunk(0); issue_chunk(1);
    STAMP(1);
    WAIT_VM(16); STAMP(2); BARS();
    issue_chunk(2); issue_chunk(3);
    WAIT_VM(32); STAMP(3); BARS();
    issue_chunk(4);
    WAIT_VM(32); STAMP(8); BARS();
    issue_chunk(5);
    WAIT_VM(32); STAMP(9); BARS();
    issue_chunk(6);
    WAIT_VM(32); STAMP(10); BARS();
    WAIT_VM(16); STAMP(11); BARS();
    WAIT_VM(0); STAMP(5); BARS();
    BAR();
    BARS();
    STAMP(7);
    WAIT_VM(0); BARS();
    return;
  }
  const int tb = w & 1, e = w >> 1, r = lane & 31, h = lane >> 5;
  f32x16 aq = {}, ak = {}, av = {};
  f32x4 cq = {}, ck = {}, cv = {};
  const unsigned cs1 = ((lane & 15) == ((lane >> 4) & 1)) ? ONES : 0u;
  const u32x4 csel = {cs1, cs1, cs1, cs1};
  const int q4 = (lane & 15) >> 2, pp = lane & 3, blk = (lane >> 4) & 1;
  const LAS char* xb[4];
#pragma unroll
  for (int kl = 0; kl < 4; ++kl) xb[kl] = lds + K1_XS + (32 * tb + r) * 128 + ((2 * (kl ^ tb) + h) ^ ((r >> 1) & 7)) * 16;
  const unsigned wb = K1_W0 + (8 * h + q4) * 128 + (((8 * e + 4 * blk + pp) ^ (8 * ((q4 >> 1) & 1))) * 8);
  const LAS char* we = lds + wb + tb * 2048;
  const LAS char* wo = lds + wb + (1 - tb) * 2048;
  u32x4 xf[3], f[3][3];
  auto load_step = [&](int n) {
    xf[n % 3] = lds128(xb[n & 3] + (n >> 2) * 8192);
    const LAS char* wp = ((n & 1) ? wo : we) + ((n >> 2) & 3) * K1_SLOT + (n & 2) * 2048;
#pragma unroll
    for (int t = 0; t < 3; ++t) { const u32x2 lo = tr_read(wp + t * 8192), hi = tr_read(wp + t * 8192 + 512); f[n % 3][t] = u32x4{lo[0], lo[1], hi[0], hi[1]}; }
  };
  const __amdgpu_buffer_rsrc_t rws = mk_rsrc(p.ws, WS_END);
  constexpr unsigned K1_IMG = K1_W0 + 3 * K1_SLOT;
  int g0half; bool g0last; const int G0 = qk_slot(64 * j, g0half, g0last);
  unsigned qdst[4], zdst[4], voff;
  {
#pragma unroll
    for (int g4 = 0; g4 < 4; ++g4) { int half; bool last; const int G = qk_slot(64 * j + 32 * e + 8 * g4 + 4 * h, half, last);
      qdst[g4] = K1_IMG + (G - G0) * KB + ((2 * tb + (r >> 4)) * 16 + (r & 15)) * 16 + half * 8;
      zdst[g4] = last ? qdst[g4] + 8 : K1_IMG + 2 * 10 * KB + lane * 8;
      asm volatile("" : "+v"(qdst[g4]), "+v"(zdst[g4])); }
    const unsigned n = 64 * j + 32 * e + r, head = n / 28, d = n - 28 * head;
    voff = WS_V + (((head * 64 + 2 * i + tb) * 2 + (d >> 4)) * 64 + h * 16 + (d & 15)) * 16; asm volatile("" : "+v"(voff)); }
  STAMP(1);
  BARS();
  STAMP(2);
  load_step(0);
#pragma unroll
  for (int n = 0; n < 28; ++n) {
    if ((n & 3) == 3 && n < 27) { if (n == 7) STAMP(8); if (n == 11) STAMP(10); BARS(); if (n == 7) { STAMP(9); CSTAMP(13); } if (n == 11) { STAMP(11); CSTAMP(14); } if (n == 15) STAMP(12); }
    if (n < 27) load_step(n + 1);
    aq = mfma32(f[n % 3][0], xf[n % 3], aq);
    ak = mfma32(f[n % 3][1], xf[n % 3], ak);
    av = mfma32(xf[n % 3], f[n % 3][2], av);
    if (!(n & 1)) {
      cq = mfma16(f[n % 3][0], csel, cq); ck = mfma16(f[n % 3][1], csel, ck); cv = mfma16(f[n % 3][2], csel, cv);
    }
    __builtin_amdgcn_sched_group_barrier(0x8, 1, 0); __builtin_amdgcn_sched_group_barrier(0x100, 3, 0);
    __builtin_amdgcn_sched_group_barrier(0x8, 1, 0); __builtin_amdgcn_sched_group_barrier(0x100, 2, 0);
    __builtin_amdgcn_sched_group_barrier(0x8, 1, 0); __builtin_amdgcn_sched_group_barrier(0x100, 2, 0);
    __builtin_amdgcn_sched_group_barrier(0x8, 3, 0);
    __builtin_amdgcn_sched_barrier(0);
  }
  STAMP(4);
  LAS float* cs = (LAS float*)(lds + K1_CS) + e * 192;
  if ((lane & 15) < 2) { LAS float* o = cs + tb * 96 + 16 * (lane & 1) + 4 * (lane >> 4);
    *(LAS f32x4*)o = cq; *(LAS f32x4*)(o + 32) = ck; *(LAS f32x4*)(o + 64) = cv; }
  BAR();
  STAMP(5);
  const LAS float* st = (const LAS float*)(lds + K1_ST);
  f32x4 cqk[2][4][2], m4[4], r4[4];
#pragma unroll
  for (int t = 0; t < 2; ++t)
#pragma unroll
    for (int g4 = 0; g4 < 4; ++g4) { cqk[t][g4][0] = *(const LAS f32x4*)(cs + t * 32 + 8 * g4 + 4 * h); cqk[t][g4][1] = *(const LAS f32x4*)(cs + 96 + t * 32 + 8 * g4 + 4 * h); }
  const float mean = st[32 * tb + r], rstd = st[64 + 32 * tb + r];
  const float cs2 = cs[64 + r] + cs[96 + 64 + r];
#pragma unroll
  for (int g4 = 0; g4 < 4; ++g4) { m4[g4] = *(const LAS f32x4*)(st + 32 * tb + 8 * g4 + 4 * h); r4[g4] = *(const LAS f32x4*)(st + 64 + 32 * tb + 8 * g4 + 4 * h); }
  __builtin_amdgcn_sched_barrier(0);
#pragma unroll
  for (int g4 = 0; g4 < 4; ++g4)
#pragma unroll
    for (int t = 0; t < 2; ++t) {
      const f32x16& a = t ? ak : aq;
      float v[4];
#pragma unroll
      for (int q = 0; q < 4; ++q) v[q] = rstd * (a[4 * g4 + q] - mean * (cqk[t][g4][0][q] + cqk[t][g4][1][q]));
      *(LAS u32x2*)(lds + qdst[g4] + t * 10 * KB) = u32x2{pk2(v[0], v[1]), pk2(v[2], v[3])};
      *(LAS u32x2*)(lds + zdst[g4] + (zdst[g4] < K1_IMG + 2 * 10 * KB ? t * 10 * KB : 0)) = u32x2{0u, 0u};
    }
  {
    float v[16];
#pragma unroll
    for (int g4 = 0; g4 < 4; ++g4)
#pragma unroll
      for (int q = 0; q < 4; ++q) v[4 * g4 + q] = r4[g4][q] * (av[4 * g4 + q] - m4[g4][q] * cs2);
    const u32x4 oa = {pk2(v[0], v[1]), pk2(v[2], v[3]), pk2(v[8], v[9]), pk2(v[10], v[11])};
    const u32x4 ob = {pk2(v[4], v[5]), pk2(v[6], v[7]), pk2(v[12], v[13]), pk2(v[14], v[15])};
    bst128<AUX_QKV>(oa, rws, voff);
    bst128<AUX_QKV>(ob, rws, voff + 512);
  }
  BARS();
  { int lh; bool ll; const int NS = qk_slot(64 * j + 60, lh, ll) - G0 + 1;
#pragma unroll
    for (int q = 0; q < 3; ++q) { const int ls = w + 4 * q;
      if (ls < NS) { const int G = G0 + ls; const unsigned o = WS_Q + (((G >> 2) * 128 + 4 * i + (lane >> 4)) * 64 + (G & 3) * 16 + (lane & 15)) * 16;
        bst128<AUX_QKV>(lds128(lds + K1_IMG + ls * KB + lane * 16), rws, o);
        bst128<AUX_QKV>(lds128(lds + K1_IMG + (10 + ls) * KB + lane * 16), rws, o + (WS_K - WS_Q)); } }
  }
  STAMP(6);
  WAIT_VM(0);
  STAMP(7);
  WAIT_VM(0); BARS();
}

constexpr unsigned L_K = 0, L_WOA = 40 * KB, L_V = 120 * KB, L_WOB = 0;
constexpr unsigned L_O = 128 * KB;
constexpr unsigned L_W = 0;
constexpr unsigned L_H = 98 * KB, L_ST4 = 126 * KB;
constexpr unsigned L_Z = 128 * KB;
constexpr unsigned L_X1 = 96 * KB, L_ST6 = 152 * KB;
constexpr unsigned L_HID = 96 * KB;
constexpr unsigned L_RA = 0, L_RB = 32 * KB, L_RC = 64 * KB, L_RD = 128 * KB;
constexpr unsigned K2_LDS = 163840;
constexpr unsigned SPIN_MAX = 1u << 16;

DI u32x4 bload(__amdgpu_buffer_rsrc_t rs, unsigned voff, unsigned soff) { return __builtin_amdgcn_raw_buffer_load_b128(rs, voff, soff, 0); }
DI u32x4 bload_sc1(__amdgpu_buffer_rsrc_t rs, unsigned voff, unsigned soff) { return __builtin_amdgcn_raw_buffer_load_b128(rs, voff, soff, 16); }
DI void bstore_sc1(u32x4 v, __amdgpu_buffer_rsrc_t rs, unsigned voff, unsigned soff) { bst128<16>(v, rs, voff + soff); }
DI unsigned umax4(u32x4 v) { return max(max(v[0], v[1]), max(v[2], v[3])); }
#define FENCE() asm volatile("" ::: "memory")
typedef __attribute__((ext_vector_type(2))) float f32x2_;
DI float gelu_tanh(float x) {
  const float e = __builtin_amdgcn_exp2f(x * fmaf(x * x, -0.10294324f, -2.3022082f));
  return x * __builtin_amdgcn_rcpf(1.f + e);
}
DI u32x4 pair_tiles(u32x2 a, u32x2 b) {
  const auto s0 = __builtin_amdgcn_permlane16_swap(a[0], b[0], false, false), s1 = __builtin_amdgcn_permlane16_swap(a[1], b[1], false, false);
  return u32x4{s0[0], s1[0], s0[1], s1[1]};
}
template <int NT, int NS, class F>
DI void fetch_payload(u32x4 (&v)[NT][2], __amdgpu_buffer_rsrc_t rs, unsigned voff0, unsigned voff1, unsigned soff, unsigned tstride, LAS char* lds, unsigned lds0, unsigned dst, int lane, F fill, void* dbg = nullptr) {
  for (unsigned spin = 0;; ++spin) {
#pragma unroll
    for (int t = 0; t < NT; ++t) { dma16_sc1(rs, voff0, soff + t * tstride, lds0 + dst + t * NS * KB); dma16_sc1(rs, voff1, soff + t * tstride, lds0 + dst + (t * NS + 1) * KB); }
    if (spin == 0) { fill(); __builtin_amdgcn_sched_barrier(0); }
    WAIT_VM(0);
    unsigned m = 0;
#pragma unroll
    for (int t = 0; t < NT; ++t)
#pragma unroll
      for (int q = 0; q < 2; ++q) { v[t][q] = lds128(lds + dst + (t * NS + q) * KB + lane * 16); m = max(m, max(max(v[t][q][0], v[t][q][1]), max(v[t][q][2], v[t][q][3]))); }
    if (!__any(m == SENT) || spin > SPIN_MAX) break;
  }
}
template <int NT> DI void row_stats(const u32x4 (&v)[NT][2], u32x4 ones, LAS float* stp, int w, int l15, int g) {
#pragma unroll
  for (int t = 0; t < NT; ++t) {
    f32x4 d1 = {}, d2 = {};
#pragma unroll
    for (int q = 0; q < 2; ++q) { d1 = __builtin_amdgcn_mfma_f32_16x16x32_bf16(__builtin_bit_cast(bf16x8, ones), __builtin_bit_cast(bf16x8, v[t][q]), d1, 0, 0, 0);
                                  d2 = __builtin_amdgcn_mfma_f32_16x16x32_bf16(__builtin_bit_cast(bf16x8, v[t][q]), __builtin_bit_cast(bf16x8, v[t][q]), d2, 0, 0, 0); }
    const float lo = (l15 & 1) ? d2[1] : d2[0], hi = (l15 & 1) ? d2[3] : d2[2], s2 = (l15 & 2) ? hi : lo;
    if (g == (l15 >> 2)) *(LAS f32x2_*)(stp + ((t * 16 + l15) * 8 + w) * 2) = f32x2_{d1[0], s2};
  }
}

#define SB() __builtin_amdgcn_sched_barrier(0)
typedef __attribute__((ext_vector_type(2))) float f32x2;
DI float max3(float a, float b, float c) { float r; asm("v_max3_f32 %0, %1, %2, %3" : "=v"(r) : "v"(a), "v"(b), "v"(c)); return r; }
DI float gsum(float v) {
  const auto a = __builtin_amdgcn_permlane16_swap(__float_as_uint(v), __float_as_uint(v), false, false); const float s = __uint_as_float(a[0]) + __uint_as_float(a[1]);
  const auto c = __builtin_amdgcn_permlane32_swap(__float_as_uint(s), __float_as_uint(s), false, false); return __uint_as_float(c[0]) + __uint_as_float(c[1]);
}
DI float gmax(float v) {
  const auto a = __builtin_amdgcn_permlane16_swap(__float_as_uint(v), __float_as_uint(v), false, false); const float s = max3(__uint_as_float(a[0]), __uint_as_float(a[1]), __uint_as_float(a[1]));
  const auto c = __builtin_amdgcn_permlane32_swap(__float_as_uint(s), __float_as_uint(s), false, false); return max3(__uint_as_float(c[0]), __uint_as_float(c[1]), __uint_as_float(c[1]));
}
DI unsigned gelu2(f32x2 x) {
  const f32x2 t = x * (x * x * f32x2{-0.10294324f, -0.10294324f} + f32x2{-2.3022082f, -2.3022082f});
  const f32x2 d = f32x2{__builtin_amdgcn_exp2f(t[0]), __builtin_amdgcn_exp2f(t[1])} + f32x2{1.f, 1.f};
  const f32x2 r = x * f32x2{__builtin_amdgcn_rcpf(d[0]), __builtin_amdgcn_rcpf(d[1])};
  return pk2(r[0], r[1]);
}
template <int N, int PD, class L, class U, class M> DI void lds_pipe(L load, U use, M mid) {
  u32x4 buf[PD + 1];
#pragma unroll
  for (int n = 0; n < PD; ++n) buf[n] = load(n);
  SB();
#pragma unroll
  for (int n = 0; n < N; ++n) {
    if (n + PD < N) buf[(n + PD) % (PD + 1)] = load(n + PD);
    SB();
    use(n, buf[n % (PD + 1)]);
    mid(n);
    SB();
  }
}

DI void wait_flags(const Params& p, bool on, unsigned off, unsigned epoch) {
  const GAS unsigned* f = (const GAS unsigned*)(p.ws + (on ? off : WS_EP));
  for (unsigned spin = 0; spin < SPIN_MAX; ++spin) {
    const unsigned v = __hip_atomic_load(f, __ATOMIC_RELAXED, __HIP_MEMORY_SCOPE_AGENT);
    if (__all(!on || v == epoch)) break;
    __builtin_amdgcn_s_sleep(2);
  }
}

__global__ void __launch_bounds__(640, 3) block_fused(Params p) {
  extern __shared__ __attribute__((aligned(16))) char smem[];
  LAS char* lds = (LAS char*)smem;
  const unsigned lds0 = (unsigned)(size_t)lds;
  const int b = blockIdx.x, tid = threadIdx.x, lane = tid & 63;
  const int w = rfl(tid >> 6);
#define PROBE(n)
  const unsigned long long cbase = code_base();
  int i, j; map_block(b, i, j);
  const __amdgpu_buffer_rsrc_t rws = mk_rsrc(p.ws, WS_END);
  ST_DECLQ;
  phase_qkv(p, lds, lds0, cbase, b, i, j, w, lane ST_ARGQ);
  PROBE(1);
  const unsigned epoch = rfl(*(const LAS unsigned*)(lds + K1_EP));
  ST_DECL; STAMP(0);
  const int l15 = lane & 15, g = lane >> 4;
  const unsigned l16 = lane * 16;
  constexpr unsigned OOB = 0xF0000000u;
  const int t32 = 2 * i + (j >> 2), c4 = j & 3, tt0 = 2 * t32;
  const int sid = XMAP == 1 ? (((i & 3) << 1) | (j >> 2)) : XMAP == 2 ? (i & 7) : ((i & 15) >> 1);
  const int rot14 = (sid * 7) >> 2, rot16 = 2 * sid, rotc = XMAP == 1 ? ((i & 3) * 3) >> 1 : (i & 7) % 6;
  auto pcol = [&](int s) { int u = s + rotc; u = u >= 6 ? u - 6 : u; int c = j + 1 + u; return c >= 7 ? c - 7 : c; };
  const u32x4 sentv = {SENT, SENT, SENT, SENT};

  if (w >= 8) {
    const int lw = w - 8;
    if (lw == 0 && lane == 0) __hip_atomic_store((GAS unsigned*)(p.ws + WS_FLG + (i * 8 + j) * 128), epoch, __ATOMIC_RELAXED, __HIP_MEMORY_SCOPE_AGENT);
    { const int t = i - 4 + (lane >> 3);
      if (lw == 0) wait_flags(p, lane < 48 && t >= 0 && t < 32, WS_FLG + (t * 8 + (lane & 7)) * 128, epoch);
      else wait_flags(p, lane < 32, WS_FLG + (lane * 8 + 7) * 128, epoch); }
    STAMP(6);
    BARS();
#pragma unroll
    for (int q = 0; q < 5; ++q) { const int n = 2 * q + lw, hh = n / 5, kt = 4 * i - 16 + 4 * (n % 5);
      dma4(rws, l16, WS_K + ((2 * j + hh) * 128 + (kt < 0 ? 0 : kt)) * KB, lds0 + L_K + n * 4 * KB); }
#pragma unroll
    for (int q = 0; q < 5; ++q) { const int n = 2 * q + lw, hh = n / 5, kb = 2 * i - 8 + 2 * (n % 5);
      dma4(rws, l16, WS_V + ((2 * j + hh) * 64 + (kb < 0 ? 0 : kb)) * 2 * KB, lds0 + L_V + n * 4 * KB); }
    STAMP(1); WAIT_VM(20); STAMP(2); BARS();
    auto wo_slot = [&](int q) { const int n = 2 * q + lw; const unsigned src = WS_WO + (c4 * 16 + ((n + rot16) & 15)) * 7 * KB, dst = lds0 + (n <= 10 ? L_WOA + n * 7 * KB : L_WOB + (n - 11) * 7 * KB);
      dma4(rws, l16, src, dst); dma3(rws, l16, src + 4 * KB, dst + 4 * KB); };
    wo_slot(0); wo_slot(1);
    STAMP(3); WAIT_VM(14); STAMP(4); BARS(); STAMP(5);
#pragma unroll
    for (int q = 2; q < 8; ++q) wo_slot(q);
    WAIT_VM(0); STAMP(7); BARS(); STAMP(8);
    auto w_stage = [&](unsigned wsrc, int q0, int q1) {
#pragma unroll
      for (int q = q0; q < q1; ++q) { const int n = 2 * q + lw; int ks = n + rot14; ks = ks >= 14 ? ks - 14 : ks; const unsigned src = wsrc + (c4 * 14 + ks) * 7 * KB, dst = lds0 + L_W + n * 7 * KB;
        dma4(rws, l16, src, dst); dma3(rws, l16, src + 4 * KB, dst + 4 * KB); } };
    BARS(); STAMP(9);
    w_stage(WS_W1, 0, 2);
    BARS();
    w_stage(WS_W1, 2, 7);
    STAMP(10); WAIT_VM(0); STAMP(11); BARS(); STAMP(12);
    BARS(); STAMP(13);
    w_stage(WS_W2, 0, 2);
    BARS();
    w_stage(WS_W2, 2, 7);
    STAMP(14); WAIT_VM(0); STAMP(15); BARS(); STAMP(16);
    BARS(); STAMP(17);
    if (j == 7) { BARS(); BARS(); ST_FLUSH(1, p.ws, b, w); ST_FLUSHQ(p.ws, b, w); return; }
    const __amdgpu_buffer_rsrc_t rw3 = mk_rsrc(p.w3, D * F * 2), rw4 = mk_rsrc(p.w4, F * D * 2);
    unsigned v3[2][4], v4[4];
#pragma unroll
    for (int k = 0; k < 4; ++k) {
#pragma unroll
      for (int e = 0; e < 2; ++e) { const int rr = lane >> 5, f = (2 * (k & 1) + rr) | (e << 2); v3[e][k] = rr * 3584 + (((lane & 31) ^ (f << 1)) * 16) + k * 6144; }
      const int rr = lane >> 3; v4[k] = rr * 896 + (((lane & 7) ^ (((rr >> 1) & 3) << 1)) * 16) + k * 6144;
    }
    auto w3_round = [&](int r, unsigned slot) { int ks = 2 * r + lw + rot14; ks = ks >= 14 ? ks - 14 : ks;
#pragma unroll
      for (int u = 0; u < 4; ++u) dma4v(rw3, v3[u & 1], (ks * 32 + 8 * u) * 3584 + j * 512, lds0 + slot + (lw * 16 + u * 4) * KB); };
    auto w4_step = [&](int s, unsigned slot) { const int c = s < 6 ? pcol(s) : j;
#pragma unroll
      for (int u = 0; u < 4; ++u) dma4v(rw4, v4, (256 * j + 128 * lw + 32 * u) * 896 + c * 128, lds0 + slot + (lw * 16 + u * 4) * KB); };
    auto arrive = [&](int s) { if (lw == 0 && lane == 0) __hip_atomic_store((GAS unsigned*)(p.ws + WS_PF + ((i * 7 + pcol(s)) * 8 + j) * 128), epoch, __ATOMIC_RELAXED, __HIP_MEMORY_SCOPE_AGENT); };
    w3_round(0, L_RA);
    BARS();
    w3_round(1, L_RB); w3_round(2, L_RC);
    BARS();
    STAMP(18); WAIT_VM(0); STAMP(19); BARS();
    BARS(); STAMP(20);
    BARS();
    w3_round(3, L_RA); w3_round(4, L_RB);
    BARS();
    BARS();
    WAIT_VM(16); BARS(); w3_round(5, L_RC);
    WAIT_VM(16); BARS(); w3_round(6, L_RA);
    WAIT_VM(16); BARS(); w4_step(0, L_RB);
    WAIT_VM(16); BARS(); w4_step(1, L_RC);
    BARS(); w4_step(2, L_RA); w4_step(3, L_RD);
    STAMP(21); WAIT_VM(16); STAMP(22); BARS();
    BARS(); w4_step(4, L_RB);
    BARS(); w4_step(5, L_RC);
    WAIT_VM(32); BARS(); w4_step(6, L_RA);
    WAIT_VM(32); BARS(); arrive(0);
    WAIT_VM(16); BARS(); arrive(1);
    WAIT_VM(0); BARS(); arrive(2);
    BARS(); arrive(3); arrive(4); arrive(5);
    if (b == 0 && lw == 0) {
#pragma unroll
      for (int q = 0; q < 4; ++q) wait_flags(p, true, WS_FLG + (q * 64 + lane) * 128, epoch);
      if (lane == 0) __hip_atomic_store((GAS unsigned*)(p.ws + WS_EP), epoch, __ATOMIC_RELAXED, __HIP_MEMORY_SCOPE_AGENT);
    }
    STAMP(23); ST_FLUSH(1, p.ws, b, w); ST_FLUSHQ(p.ws, b, w);
    return;
  }

  const int hl = w >> 2, a = w & 3, head = 2 * j + hl, tt = 4 * i + a;
  const bool act = w < 7;
  const int wf = act ? w : 6, Fg = 7 * c4 + wf;
  u32x4 qf; u32x2 xr[2]; u32x4 cs1u[2], cs3u[2][2];
  const u32x4 ones = {ONES, ONES, ONES, ONES};
  unsigned act_off = act ? (((tt0 + (g & 1)) * 14 + (Fg >> 1)) * 64 + (2 * (Fg & 1) + (g >> 1)) * 16 + l15) * 16 : OOB;
  const int ka = (2 * w + rot14) % 14, kb14 = (2 * w + 1 + rot14) % 14;
  unsigned pv0 = l16 + ka * KB, pv1 = l16 + kb14 * KB, ov0 = l16 + ((2 * w + rot16) & 15) * KB, ov1 = l16 + ((2 * w + 1 + rot16) & 15) * KB;
  unsigned zv[2];
#pragma unroll
  for (int q = 0; q < 2; ++q) { const int ks = q ? kb14 : ka, f0 = 32 * ks + 8 * g, sh = f0 < 112 ? -1 : f0 < 224 ? 1 : f0 < 336 ? -2 : 2, tz = 16 * (tt0 + 1) + l15 + sh;
    zv[q] = (((tz >> 4) * 14 + ks) * 64 + g * 16 + (tz & 15)) * 16; }
  u32x4 selA, selB;
  { const unsigned v = g == (l15 >> 2) ? ((l15 & 1) ? 0x3F800000u : 0x00003F80u) : 0u, d0 = (l15 & 2) ? 0u : v, d1 = (l15 & 2) ? v : 0u;
    selA = u32x4{d0, d1, 0u, 0u}; selB = u32x4{0u, 0u, d0, d1}; }
  unsigned ooff = ((tt * 16 + head) * 64 + ((g >> 1) + 2 * (g & 1)) * 16 + l15) * 16 + WS_O;
  asm volatile("" : "+v"(act_off), "+v"(pv0), "+v"(pv1), "+v"(ov0), "+v"(ov1), "+v"(zv[0]), "+v"(zv[1]), "+v"(selA), "+v"(selB), "+v"(ooff));
  BARS();
  aload128_sc1(qf, rws, l16, WS_Q + (head * 128 + tt) * KB);
  WAIT_VM(0); RETIRE(qf);
  STAMP(1);
  BARS();
  PROBE(2);
  { const __amdgpu_buffer_rsrc_t rx = mk_rsrc(p.x, S * D * 2);
#pragma unroll
    for (int t = 0; t < 2; ++t) aload64(xr[t], rx, ((16 * (tt0 + t) + l15) * D + 16 * Fg + 4 * g) * 2, 0); }
#pragma unroll
  for (int hf = 0; hf < 2; ++hf) { aload128_sc1(cs1u[hf], rws, (16 * Fg + 4 * g) * 4, WS_CS + hf * 2240 * 4);
#pragma unroll
    for (int f = 0; f < 2; ++f) aload128_sc1(cs3u[hf][f], rws, (448 + 256 * (j < 7 ? j : 6) + 32 * w + 16 * f + 4 * g) * 4, WS_CS + hf * 2240 * 4); }

  u32x4 opiece;
  {
    const int par = a & 1, kl0 = hl * 20 + (a & ~1), vl0 = hl * 20 + (a >> 1) * 2;
    f32x4 sc[18];
    const LAS char* kp = lds + L_K + kl0 * KB + l16;
    const f32x4 zero4 = {0.f, 0.f, 0.f, 0.f};
    lds_pipe<18, 8>([&](int n) { return lds128(kp + n * KB); }, [&](int n, u32x4 kf) { sc[n] = mfma16(kf, qf, zero4); }, [](int) {});
    const int dq = l15 - 4 * g;
    const float NEG = -__builtin_inff();
    if (par == 0) {
#pragma unroll
      for (int r = 0; r < 4; ++r) { const bool ge = dq >= r; sc[0][r] = ge ? NEG : sc[0][r]; sc[16][r] = ge ? sc[16][r] : NEG; sc[17][r] = NEG; }
    } else {
#pragma unroll
      for (int r = 0; r < 4; ++r) { const bool ge = dq >= r; sc[1][r] = ge ? NEG : sc[1][r]; sc[17][r] = ge ? sc[17][r] : NEG; sc[0][r] = NEG; }
    }
    const int nneg = 16 - (tt & ~1);
    if (nneg > 0) {
#pragma unroll
      for (int n = 0; n < 16; ++n) if (n < nneg) sc[n] = f32x4{NEG, NEG, NEG, NEG};
    }
    float m = max3(sc[0][0], sc[0][1], sc[0][2]), m2 = max3(sc[0][3], sc[1][0], sc[1][1]);
    m = max3(m, sc[1][2], sc[1][3]);
#pragma unroll
    for (int n = 2; n < 18; ++n) { m = max3(m, sc[n][0], sc[n][1]); m2 = max3(m2, sc[n][2], sc[n][3]); }
    m = gmax(max3(m, m2, m2));
    const float cexp = 0.18898223650461363f * 1.4426950408889634f, mc = -m * cexp;
    STAMP(14);
    BARS();
    STAMP(15);
    f32x4 o0 = zero4, o1 = zero4;
    const LAS char* vp = lds + L_V + vl0 * KB + l16;
    u32x4 v0 = lds128(vp), v1 = lds128(vp + KB);
#pragma unroll
    for (int mm = 0; mm < 9; ++mm) {
      u32x4 n0, n1;
      if (mm < 8) { n0 = lds128(vp + (2 * mm + 2) * KB); n1 = lds128(vp + (2 * mm + 3) * KB); }
      u32x4 pf;
#pragma unroll
      for (int q = 0; q < 4; ++q) { const f32x4& sv = sc[2 * mm + (q >> 1)];
        pf[q] = pk2(__builtin_amdgcn_exp2f(fmaf(sv[2 * (q & 1)], cexp, mc)), __builtin_amdgcn_exp2f(fmaf(sv[2 * (q & 1) + 1], cexp, mc))); }
      o0 = mfma16(v0, pf, o0);
      o1 = mfma16(v1, pf, o1);
      if (mm < 8) { v0 = n0; v1 = n1; }
      SB();
    }
    const float rl = __builtin_amdgcn_rcpf(gsum(g == 3 ? o1[0] : 0.f));
    opiece = pair_tiles(u32x2{pk2(o0[0] * rl, o0[1] * rl), pk2(o0[2] * rl, o0[3] * rl)}, u32x2{pk2(o1[0] * rl, o1[1] * rl), pk2(o1[2] * rl, o1[3] * rl)});
  }
  STAMP(16);
  WAIT_VM(0); RETIRE(xr[0]); RETIRE(xr[1]); RETIRE(cs1u[0]); RETIRE(cs1u[1]); RETIRE(cs3u[0][0]); RETIRE(cs3u[0][1]); RETIRE(cs3u[1][0]); RETIRE(cs3u[1][1]);
  const f32x4 cs1 = __builtin_bit_cast(f32x4, cs1u[0]) + __builtin_bit_cast(f32x4, cs1u[1]);
  f32x4 cs3[2];
#pragma unroll
  for (int f = 0; f < 2; ++f) cs3[f] = __builtin_bit_cast(f32x4, cs3u[0][f]) + __builtin_bit_cast(f32x4, cs3u[1][f]);
  BARS();
  bst128<16>(opiece, rws, ooff);
  u32x4 wr[16];
#pragma unroll
  for (int n = 0; n < 16; ++n) wr[n] = lds128(lds + (n <= 10 ? L_WOA + n * 7 * KB : L_WOB + (n - 11) * 7 * KB) + wf * KB + l16);
  STAMP(2);
  WAIT_VM(0);

  { u32x4 v[2][2];
    fetch_payload<2, 16>(v, rws, ov0, ov1, WS_O + tt0 * 16 * KB, 16 * KB, lds, lds0, L_O + 2 * w * KB, lane, [] { BARS();   }, p.ws + (40u << 20)); }
  STAMP(3);
  BARS();
  PROBE(3);
  u32x2 hp[2];
  {
    f32x4 acc[2];
#pragma unroll
    for (int t = 0; t < 2; ++t) acc[t] = mfma16(selA, u32x4{xr[t][0], xr[t][1], 0u, 0u}, f32x4{0.f, 0.f, 0.f, 0.f});
    const LAS char* op = lds + L_O + l16;
    lds_pipe<32, 8>([&](int n) { return lds128(op + ((n & 1) * 16 + (n >> 1)) * KB); }, [&](int n, u32x4 f) { acc[n & 1] = mfma16(wr[n >> 1], f, acc[n & 1]); }, [](int) {});
#pragma unroll
    for (int t = 0; t < 2; ++t) hp[t] = u32x2{pk2(acc[t][0], acc[t][1]), pk2(acc[t][2], acc[t][3])};
    bst128<16>(pair_tiles(hp[0], hp[1]), rws, act_off + WS_H);
  }
  STAMP(4);
  WAIT_VM(0);

  LAS float* st4 = (LAS float*)(lds + L_ST4);
  auto stage_weights = [&] { BARS();
#pragma unroll
    for (int n = 0; n < 14; ++n) wr[n] = lds128(lds + L_W + (n * 7 + wf) * KB + l16);
    BARS(); };
  { u32x4 v[2][2] = {};
    if (act) fetch_payload<2, 14>(v, rws, pv0, pv1, WS_H + tt0 * 14 * KB, 14 * KB, lds, lds0, L_H + 2 * w * KB, lane, stage_weights, p.ws + (40u << 20) + 4); else stage_weights();
    row_stats<2>(v, ones, st4, w, l15, g); }
  STAMP(5);
  BARS();
  f32x2 yv[2][2];
  {
    const LAS char* hpz = lds + L_H + l16;
    f32x4 pt[2];
#pragma unroll
    for (int t = 0; t < 2; ++t) pt[t] = *(const LAS f32x4*)(st4 + ((t * 16 + l15) * 8 + 2 * g) * 2);
    f32x4 acc[2] = {};
    lds_pipe<28, 8>([&](int n) { return lds128(hpz + ((n & 1) * 14 + (n >> 1)) * KB); }, [&](int n, u32x4 f) { acc[n & 1] = mfma16(wr[n >> 1], f, acc[n & 1]); }, [](int) {});
    u32x2 zp[2];
#pragma unroll
    for (int t = 0; t < 2; ++t) {
      const float s1 = gsum(pt[t][0] + pt[t][2]), s2 = gsum(pt[t][1] + pt[t][3]);
      const float mean = s1 * (1.f / D), var = fmaxf(s2 * (1.f / D) - mean * mean, 0.f);
      const float r1 = __builtin_amdgcn_rsqf(var + 1e-5f);
      const float r12 = r1 * __builtin_amdgcn_rsqf(var * r1 * r1 + 1e-5f);
      const f32x2 m2 = {mean, mean};
#pragma unroll
      for (int q = 0; q < 2; ++q) {
        yv[t][q] = (f32x2{bflo(hp[t][q]), bfhi(hp[t][q])} - m2) * f32x2{r1, r1};
        zp[t][q] = gelu2((f32x2{acc[t][2 * q], acc[t][2 * q + 1]} - m2 * f32x2{cs1[2 * q], cs1[2 * q + 1]}) * f32x2{r12, r12});
      }
    }
    bst128<16>(pair_tiles(zp[0], zp[1]), rws, act_off + WS_Z + 14 * KB);
  }
  STAMP(6);
  WAIT_VM(0);

  { u32x4 v[2][2];
    if (act) fetch_payload<2, 14>(v, rws, zv[0], zv[1], WS_Z, 14 * KB, lds, lds0, L_Z + 2 * w * KB, lane, stage_weights, p.ws + (40u << 20) + 8); else stage_weights(); }
  STAMP(7);
  BARS();
  {
    f32x4 acc[2] = {};
    const LAS char* zb = lds + L_Z + l16;
    lds_pipe<28, 8>([&](int n) { return lds128(zb + ((n & 1) * 14 + (n >> 1)) * KB); }, [&](int n, u32x4 f) { acc[n & 1] = mfma16(wr[n >> 1], f, acc[n & 1]); }, [](int) {});
    BARS();
    u32x2 xp[2];
#pragma unroll
    for (int t = 0; t < 2; ++t) xp[t] = u32x2{pk2(yv[t][0][0] + acc[t][0], yv[t][0][1] + acc[t][1]), pk2(yv[t][1][0] + acc[t][2], yv[t][1][1] + acc[t][3])};
    bst128<16>(pair_tiles(xp[0], xp[1]), rws, act_off + WS_X1);
  }
  STAMP(8);
  if (j == 7) { ST_FLUSH(1, p.ws, b, w); ST_FLUSHQ(p.ws, b, w); return; }
  WAIT_VM(0);

  LAS float* st6 = (LAS float*)(lds + L_ST6);
  const int q4 = l15 >> 2, pp = lane & 3;
  const LAS char* a3[2];
#pragma unroll
  for (int f = 0; f < 2; ++f) a3[f] = lds + (8 * g + q4) * 512 + (((2 * w + f) ^ (q4 | ((g & 1) << 2))) * 32) + pp * 8;
  auto w3frag = [&](unsigned off, int f) { const u32x2 lo = tr_read(a3[f] + off), hi = tr_read(a3[f] + off + 2048); return u32x4{lo[0], lo[1], hi[0], hi[1]}; };
  u32x4 areg[4][2];
  { u32x4 v[4][2] = {};
    auto w3_regs = [&] { BARS();
#pragma unroll
      for (int n = 0; n < 4; ++n)
#pragma unroll
        for (int f = 0; f < 2; ++f) areg[n][f] = w3frag(n * 16 * KB, f);
      BARS(); };
    if (act) fetch_payload<4, 14>(v, rws, pv0, pv1, WS_X1 + 4 * i * 14 * KB, 14 * KB, lds, lds0, L_X1 + 2 * w * KB, lane, w3_regs, p.ws + (40u << 20) + 12); else w3_regs();
    row_stats<4>(v, ones, st6, w, l15, g); }
  STAMP(9);
  BARS();
  PROBE(4);
  {
    const LAS char* xp = lds + L_X1 + l16;
    f32x4 pt[4];
#pragma unroll
    for (int t = 0; t < 4; ++t) pt[t] = *(const LAS f32x4*)(st6 + ((t * 16 + l15) * 8 + 2 * g) * 2);
    f32x4 acc[2][4] = {};
    u32x4 bfr[2][4], afr[2][2];
    auto load_k = [&](int n) { const unsigned slot = ((n >> 1) % 3 == 2 ? L_RC : (n >> 1) % 3 == 0 ? L_RA : L_RB) + (n & 1) * 16 * KB;
#pragma unroll
      for (int f = 0; f < 2; ++f) afr[n & 1][f] = n < 4 ? areg[n][f] : w3frag(slot, f);
#pragma unroll
      for (int t = 0; t < 4; ++t) bfr[n & 1][t] = lds128(xp + (t * 14 + n) * KB); };
    load_k(0);
    SB();
#pragma unroll
    for (int n = 0; n < 14; ++n) {
      if (n & 1) BARS();
      if (n < 13) load_k(n + 1);
      SB();
#pragma unroll
      for (int t = 0; t < 4; ++t) { acc[0][t] = mfma16(afr[n & 1][0], bfr[n & 1][t], acc[0][t]); acc[1][t] = mfma16(afr[n & 1][1], bfr[n & 1][t], acc[1][t]); }
      SB();
    }
    STAMP(20);
    float mean[4], rs[4];
#pragma unroll
    for (int t = 0; t < 4; ++t) {
      const float s1 = gsum(pt[t][0] + pt[t][2]), s2 = gsum(pt[t][1] + pt[t][3]);
      mean[t] = s1 * (1.f / D); rs[t] = __builtin_amdgcn_rsqf(fmaxf(s2 * (1.f / D) - mean[t] * mean[t], 0.f) + 1e-5f);
    }
#pragma unroll
    for (int t = 0; t < 4; ++t) {
      u32x4 hv;
#pragma unroll
      for (int q = 0; q < 4; ++q) { const int f = q >> 1, r = 2 * (q & 1);
        hv[q] = gelu2((f32x2{acc[f][t][r], acc[f][t][r + 1]} - f32x2{mean[t], mean[t]} * f32x2{cs3[f][r], cs3[f][r + 1]}) * f32x2{rs[t], rs[t]}); }
      *(LAS u32x4*)(lds + L_HID + (t * 8 + w) * KB + l16) = hv;
    }
  }
  STAMP(10);
  BARS();
  STAMP(11);
  const int tb = w & 1, ft = w >> 1;
  u32x4 hf[2][8];
#pragma unroll
  for (int ks = 0; ks < 8; ++ks)
#pragma unroll
    for (int t = 0; t < 2; ++t) hf[t][ks] = lds128(lds + L_HID + ((2 * tb + t) * 8 + ks) * KB + l16);
  const unsigned pbase = l16 + WS_P + ((i * 49 + j) * 8 + w) * KB;
  const int Fo = 4 * j + ft;
  u32x2 hres[2], xres[2];
  unsigned ro[2];
#pragma unroll
  for (int t = 0; t < 2; ++t) { ro[t] = (((4 * i + 2 * tb + t) * 14 + (Fo >> 1)) * 64 + (2 * (Fo & 1) + (g >> 1)) * 16 + l15) * 16 + (g & 1) * 8;
    aload64_sc1(hres[t], rws, ro[t], WS_H); aload64_sc1(xres[t], rws, ro[t], WS_X1); }
  f32x4 acc[2] = {};
  {
    constexpr int PD = 4;
    constexpr unsigned slots[7] = {L_RB, L_RC, L_RA, L_RD, L_RB, L_RC, L_RA};
    const LAS char* wp = lds + (4 * g + q4) * 128 + ((ft ^ ((2 * g + (q4 >> 1)) & 3)) * 32) + pp * 8;
    auto w4frag = [&](unsigned off) { const u32x2 lo = tr_read(wp + off), hi = tr_read(wp + off + 2048); return u32x4{lo[0], lo[1], hi[0], hi[1]}; };
    u32x4 buf[PD + 1];
#pragma unroll
    for (int m = 0; m < PD; ++m) buf[m] = w4frag(slots[0] + m * 4096);
    SB();
#pragma unroll
    for (int m = 0; m < 56; ++m) {
      const int s = m >> 3, ks = m & 7;
      if (ks == 8 - PD && s < 6) { if (s >= 3) WAIT_VM(2); BARS(); }
      if (m + PD < 56) buf[(m + PD) % (PD + 1)] = w4frag(slots[(m + PD) >> 3] + ((m + PD) & 7) * 4096);
      SB();
      acc[0] = mfma16(buf[m % (PD + 1)], hf[0][ks], acc[0]); acc[1] = mfma16(buf[m % (PD + 1)], hf[1][ks], acc[1]);
      if (ks == 7 && s < 6) {
        bst128<16>(u32x4{pk2(acc[0][0], acc[0][1]), pk2(acc[0][2], acc[0][3]), pk2(acc[1][0], acc[1][1]), pk2(acc[1][2], acc[1][3])}, rws, pbase + pcol(s) * 56 * KB);
        acc[0] = f32x4{0.f, 0.f, 0.f, 0.f}; acc[1] = acc[0];
      }
      SB();
    }
  }
  STAMP(12);
  PROBE(5);
  {
    const __amdgpu_buffer_rsrc_t rout = mk_rsrc(p.out, S * D * 2);
    u32x4 pv[6];
    const unsigned pin = WS_P + ((i * 7 + j) * 7 * 8 + w) * KB;
    WAIT_VM(0); RETIRE(hres[0]); RETIRE(hres[1]); RETIRE(xres[0]); RETIRE(xres[1]);
    BARS();
    for (unsigned spin = 0; spin < SPIN_MAX; ++spin) {
      const unsigned m = max(max(max(hres[0][0], hres[0][1]), max(xres[0][0], xres[0][1])), max(max(hres[1][0], hres[1][1]), max(xres[1][0], xres[1][1])));
      if (!__any(m == SENT)) break;
#pragma unroll
      for (int t = 0; t < 2; ++t) { aload64_sc1(hres[t], rws, ro[t], WS_H); aload64_sc1(xres[t], rws, ro[t], WS_X1); }
      WAIT_VM(0); RETIRE(hres[0]); RETIRE(hres[1]); RETIRE(xres[0]); RETIRE(xres[1]);
    }
    wait_flags(p, lane < 7 && lane != j, WS_PF + ((i * 7 + j) * 8 + lane) * 128, epoch);
#pragma unroll
    for (int s = 0; s < 6; ++s) { int js = j + 1 + s; js = js >= 7 ? js - 7 : js; aload128_sc1(pv[s], rws, l16, pin + js * 8 * KB); }
    WAIT_VM(0);
#pragma unroll
    for (int s = 0; s < 6; ++s) RETIRE(pv[s]);
    const u32x4 selAB = selA | selB;
#pragma unroll
    for (int s = 0; s < 6; ++s) { acc[0] = mfma16(selA, pv[s], acc[0]); acc[1] = mfma16(selB, pv[s], acc[1]); }
#pragma unroll
    for (int t = 0; t < 2; ++t) {
      acc[t] = mfma16(selAB, u32x4{hres[t][0], hres[t][1], xres[t][0], xres[t][1]}, acc[t]);
      __builtin_amdgcn_raw_buffer_store_b64(u32x2{pk2(acc[t][0], acc[t][1]), pk2(acc[t][2], acc[t][3])}, rout, ((16 * (4 * i + 2 * tb + t) + l15) * D + 16 * Fo + 4 * g) * 2, 0, 0);
    }
  }
  STAMP(13);
  ST_FLUSH(1, p.ws, b, w); ST_FLUSHQ(p.ws, b, w);
}

extern "C" void kernel_launch(void* const* d_in, const int* in_sizes, int n_in, void* d_out, int out_size, void* d_ws, size_t ws_size, hipStream_t stream) {
  Params p;
  p.x = (const u16*)d_in[0]; p.wqkv = (const u16*)d_in[1]; p.wo = (const u16*)d_in[2];
  p.w1 = (const u16*)d_in[4]; p.w2 = (const u16*)d_in[5]; p.w3 = (const u16*)d_in[6]; p.w4 = (const u16*)d_in[7];
  p.out = (u16*)d_out; p.ws = (char*)d_ws;
  (void)hipFuncSetAttribute((const void*)block_fused, hipFuncAttributeMaxDynamicSharedMemorySize, K2_LDS);
  block_fused<<<256, 640, K2_LDS, stream>>>(p);
}
```

```cpp
#include <hip/hip_runtime.h>
#include <hip/hip_bf16.h>
#include <stdint.h>

typedef unsigned short u16;
typedef __attribute__((ext_vector_type(8))) __bf16 bf16x8;
typedef __attribute__((ext_vector_type(2))) __bf16 bf16x2;
typedef __attribute__((ext_vector_type(4))) short s16x4;
typedef __attribute__((ext_vector_type(4))) float f32x4;
typedef __attribute__((ext_vector_type(16))) float f32x16;
typedef __attribute__((ext_vector_type(4))) unsigned u32x4;
typedef __attribute__((ext_vector_type(2))) unsigned u32x2;
#define LAS __attribute__((address_space(3)))
#define GAS __attribute__((address_space(1)))
#define DI __device__ __forceinline__

constexpr int S = 2048, D = 448, NH = 16, DH = 28, F = 1792;
constexpr unsigned SENT = 0xFFFFFFFFu;

constexpr unsigned KB = 1024;
constexpr unsigned WS_Q   = 0;
constexpr unsigned WS_K   = WS_Q + 2048 * KB;
constexpr unsigned WS_V   = WS_K + 2048 * KB;
constexpr unsigned WS_WO  = WS_V + 2048 * KB;
constexpr unsigned WS_W1  = WS_WO + 448 * KB;
constexpr unsigned WS_W2  = WS_W1 + 392 * KB;
constexpr unsigned WS_O   = WS_W2 + 392 * KB;
constexpr unsigned WS_H   = WS_O + 2048 * KB;
constexpr unsigned WS_X1  = WS_H + 1792 * KB;
constexpr unsigned WS_Z   = WS_X1 + 1792 * KB;
constexpr unsigned WS_P   = WS_Z + 1820 * KB;
constexpr unsigned WS_CS  = WS_P + 32 * 49 * 8 * KB;
constexpr unsigned WS_PF  = WS_CS + 18 * KB;
constexpr unsigned WS_FLG = WS_PF + 224 * KB;
constexpr unsigned WS_EP  = WS_FLG + 32 * KB;
constexpr unsigned WS_END = WS_EP + KB;

struct Params {
  const u16* x; const u16* wqkv; const u16* wo; const u16* w1; const u16* w2; const u16* w3; const u16* w4;
  u16* out; char* ws;
};

DI __amdgpu_buffer_rsrc_t mk_rsrc(const void* p, unsigned bytes) { return __builtin_amdgcn_make_buffer_rsrc((void*)p, (short)0, (int)bytes, 0x00020000); }
DI void dma16(__amdgpu_buffer_rsrc_t rs, unsigned voff, unsigned soff, unsigned ldsaddr) {
  asm volatile("s_mov_b32 m0, %0\n\ts_nop 0\n\tbuffer_load_dwordx4 %1, %2, %3 offen lds" :: "s"(ldsaddr), "v"(voff), "s"(rs), "s"(soff) : "memory");
}
DI void dma16_sc1(__amdgpu_buffer_rsrc_t rs, unsigned voff, unsigned soff, unsigned ldsaddr) {
  asm volatile("s_mov_b32 m0, %0\n\ts_nop 0\n\tbuffer_load_dwordx4 %1, %2, %3 offen sc1 lds" :: "s"(ldsaddr), "v"(voff), "s"(rs), "s"(soff) : "memory");
}
DI void dma4(__amdgpu_buffer_rsrc_t rs, unsigned voff, unsigned soff, unsigned ldsaddr) {
  asm volatile("s_mov_b32 m0, %0\n\ts_nop 0\n\tbuffer_load_dwordx4 %1, %2, %3 offen sc1 lds\n\tbuffer_load_dwordx4 %1, %2, %3 offen offset:1024 sc1 lds\n\t"
               "buffer_load_dwordx4 %1, %2, %3 offen offset:2048 sc1 lds\n\tbuffer_load_dwordx4 %1, %2, %3 offen offset:3072 sc1 lds" :: "s"(ldsaddr), "v"(voff), "s"(rs), "s"(soff) : "memory");
}
DI void dma3(__amdgpu_buffer_rsrc_t rs, unsigned voff, unsigned soff, unsigned ldsaddr) {
  asm volatile("s_mov_b32 m0, %0\n\ts_nop 0\n\tbuffer_load_dwordx4 %1, %2, %3 offen sc1 lds\n\tbuffer_load_dwordx4 %1, %2, %3 offen offset:1024 sc1 lds\n\t"
               "buffer_load_dwordx4 %1, %2, %3 offen offset:2048 sc1 lds" :: "s"(ldsaddr), "v"(voff), "s"(rs), "s"(soff) : "memory");
}
DI void dma4v(__amdgpu_buffer_rsrc_t rs, const unsigned (&v)[4], unsigned soff, unsigned ldsaddr) {
  asm volatile("s_mov_b32 m0, %0\n\ts_nop 0\n\tbuffer_load_dwordx4 %1, %5, %6 offen lds\n\tbuffer_load_dwordx4 %2, %5, %6 offen offset:1024 lds\n\t"
               "buffer_load_dwordx4 %3, %5, %6 offen offset:2048 lds\n\tbuffer_load_dwordx4 %4, %5, %6 offen offset:3072 lds"
               :: "s"(ldsaddr), "v"(v[0]), "v"(v[1]), "v"(v[2]), "v"(v[3]), "s"(rs), "s"(soff) : "memory");
}
#define WAIT_VM(n) asm volatile("s_waitcnt vmcnt(" #n ")" ::: "memory")
#define BAR() asm volatile("s_waitcnt lgkmcnt(0)\n\ts_barrier" ::: "memory")
#define BARS() do { __builtin_amdgcn_sched_barrier(0); BAR(); __builtin_amdgcn_sched_barrier(0); } while (0)
DI unsigned rfl(unsigned v) { return __builtin_amdgcn_readfirstlane(v); }
DI float bflo(unsigned u) { return __uint_as_float(u << 16); }
DI float bfhi(unsigned u) { return __uint_as_float(u & 0xffff0000u); }
DI unsigned pk2(float a, float b) { bf16x2 v; v[0] = (__bf16)a; v[1] = (__bf16)b; return __builtin_bit_cast(unsigned, v); }
DI float dot2(unsigned a, unsigned b, float c) { return __builtin_amdgcn_fdot2_f32_bf16(__builtin_bit_cast(bf16x2, a), __builtin_bit_cast(bf16x2, b), c, false); }
DI f32x16 mfma32(u32x4 a, u32x4 b, f32x16 c) { return __builtin_amdgcn_mfma_f32_32x32x16_bf16(__builtin_bit_cast(bf16x8, a), __builtin_bit_cast(bf16x8, b), c, 0, 0, 0); }
DI f32x4 mfma16(u32x4 a, u32x4 b, f32x4 c) { return __builtin_amdgcn_mfma_f32_16x16x32_bf16(__builtin_bit_cast(bf16x8, a), __builtin_bit_cast(bf16x8, b), c, 0, 0, 0); }
DI u32x2 tr_read(const LAS char* p) { return __builtin_bit_cast(u32x2, __builtin_amdgcn_ds_read_tr16_b64_v4i16((LAS s16x4*)p)); }
DI u32x4 lds128(const LAS char* p) { return *(const LAS u32x4*)p; }
template <int AUX> DI void bst128(u32x4 v, __amdgpu_buffer_rsrc_t rs, unsigned off) { __builtin_amdgcn_raw_buffer_store_b128(v, rs, off, 0, AUX); }
#define XJ j
constexpr unsigned ONES = 0x3F803F80u;
#define AUX_SIDE 16
#define AUX_QKV 16
#define ST_DECL
#define ST_PARAM
#define ST_DECLQ
#define ST_ARGQ
#define ST_FLUSHQ(ws, b, w)
#define CSTAMP(n)
#define STAMP(n)
#define ST_FLUSH(kern, ws, b, w)

DI void aload128(u32x4& d, __amdgpu_buffer_rsrc_t rs, unsigned voff, unsigned soff) { asm volatile("buffer_load_dwordx4 %0, %1, %2, %3 offen" : "=v"(d) : "v"(voff), "s"(rs), "s"(soff) : "memory"); }
DI void aload64(u32x2& d, __amdgpu_buffer_rsrc_t rs, unsigned voff, unsigned soff) { asm volatile("buffer_load_dwordx2 %0, %1, %2, %3 offen" : "=v"(d) : "v"(voff), "s"(rs), "s"(soff) : "memory"); }
DI void aload128_sc1(u32x4& d, __amdgpu_buffer_rsrc_t rs, unsigned voff, unsigned soff) { asm volatile("buffer_load_dwordx4 %0, %1, %2, %3 offen sc1" : "=v"(d) : "v"(voff), "s"(rs), "s"(soff) : "memory"); }
DI void aload64_sc1(u32x2& d, __amdgpu_buffer_rsrc_t rs, unsigned voff, unsigned soff) { asm volatile("buffer_load_dwordx2 %0, %1, %2, %3 offen sc1" : "=v"(d) : "v"(voff), "s"(rs), "s"(soff) : "memory"); }
#define RETIRE(x) asm volatile("; RETIRE %0" : "+v"(x))
constexpr unsigned CODE_BYTES = 50 * 1024;
DI unsigned touch_code(unsigned long long base, unsigned bytes, int b, int lane, bool on) {
  const __amdgpu_buffer_rsrc_t rc = mk_rsrc((const void*)base, bytes);
  const unsigned per = (bytes / 128 + 31) / 32, line = (b >> 3) * per + lane;
  unsigned d;
  asm volatile("buffer_load_dword %0, %1, %2, 0 offen" : "=v"(d) : "v"(on && lane < (int)per ? line * 128 : 0xF0000000u), "s"(rc) : "memory");
  return d;
}
DI unsigned long long code_base() { unsigned long long pc; asm volatile("s_getpc_b64 %0" : "=s"(pc)); return pc & ~0xFFull; }

#define HX0 8
#define HX1 8
#define HX2 12
#define HX3 10
#define HWAIT(x) do { WAIT_VM(0); if ((x) > 0) __builtin_amdgcn_s_sleep(x); } while (0)
#define ROTQ 0
#define XMAP 2
DI void map_block(int b, int& i, int& j) { const int x = b & 7, s = b >> 3; constexpr int JPX = 8 / XMAP; i = (x / XMAP) * 4 * XMAP + s / JPX; j = (x % XMAP) * JPX + s % JPX; }

constexpr unsigned K1_XS = 0;
constexpr unsigned K1_W0 = 7 * 8192;
constexpr unsigned K1_SLOT = 24576;
constexpr unsigned K1_ST = K1_W0 + 4 * K1_SLOT;
constexpr unsigned K1_CS = K1_ST + 512;
constexpr unsigned K1_EP = K1_CS + 2 * 2 * 3 * 32 * 4;

DI void retile_load(const Params& p, int mat, int ks, int cb, int lane, u32x4 (&v)[4]) {
  const u16* W = mat == 0 ? p.wo : mat == 1 ? p.w1 : mat == 2 ? p.w2 : p.w3;
  const unsigned ldw = mat == 3 ? F : D, nrows = D, krow0 = (mat == 0 ? 28 : 32) * ks;
#pragma unroll
  for (int q = 0; q < 4; ++q) {
    unsigned row = krow0 + (lane >> 3) + 8 * q; row = row < nrows ? row : nrows - 1;
    v[q] = *(const u32x4*)(W + (size_t)row * ldw + cb * 64 + (lane & 7) * 8);
  }
}
template <bool SUMS>
DI void retile_store(const Params& p, LAS char* scr, int mat, int ks, int cb, int lane, const u32x4 (&v)[4], f32x4 (&cs)[4]) {
#pragma unroll
  for (int q = 0; q < 4; ++q) *(LAS u32x4*)(scr + q * 1024 + lane * 16) = v[q];
  const int g = lane >> 4, q4 = (lane & 15) >> 2, pp = lane & 3;
  const int r0 = 8 * g + q4, r1 = 8 * g + 4 + q4;
  const __amdgpu_buffer_rsrc_t rws = mk_rsrc(p.ws, WS_END);
  const u32x4 ones = {ONES, ONES, ONES, ONES};
#pragma unroll
  for (int ft = 0; ft < 4; ++ft) {
    u32x2 lo = tr_read(scr + r0 * 128 + ft * 32 + pp * 8), hi = tr_read(scr + r1 * 128 + ft * 32 + pp * 8);
    if (mat == 0 && g == 3) { hi[0] = 0u; hi[1] = 0u; }
    const u32x4 f = {lo[0], lo[1], hi[0], hi[1]};
    const int Fi = 4 * cb + ft;
    unsigned dst;
    if (mat == 0) dst = WS_WO + (((Fi / 7) * 16 + ks) * 7 + Fi % 7) * KB;
    else dst = (mat == 1 ? WS_W1 : WS_W2) + (((Fi / 7) * 14 + ks) * 7 + Fi % 7) * KB;
    if (mat != 3) bst128<AUX_SIDE>(f, rws, lane * 16 + dst);
    if (SUMS) cs[ft] = mfma16(f, ones, cs[ft]);
  }
}

DI int qk_slot(int n0, int& half, bool& last) {
  const int head = n0 / 28, hs = 28 * head, bnd = (hs / 64 + 1) * 64;
  const bool split = bnd < hs + 28, second = split && n0 >= bnd;
  const int seg0 = second ? bnd : hs, seg1 = (split && !second) ? bnd : hs + 28, off = n0 - seg0;
  half = (off >> 2) & 1;
  last = n0 + 4 == seg1 && half == 0;
  return 4 * head + (second ? (bnd - hs + 7) >> 3 : 0) + (off >> 3);
}
DI void fill_sentinels(__amdgpu_buffer_rsrc_t rws, int i, int j, int lw, int lane) {
  const u32x4 sv = {SENT, SENT, SENT, SENT};
  const unsigned l16 = lane * 16, tt0 = 4 * i + 2 * (j >> 2), F0 = 7 * (j & 3);
#pragma unroll
  for (int t = 0; t < 2; ++t) { const unsigned o = WS_O + ((4 * i + 2 * lw + t) * 16 + 2 * j) * KB + l16; bst128<16>(sv, rws, o); bst128<16>(sv, rws, o + KB); }
  const unsigned o = ((tt0 + lw) * 14 + (F0 >> 1)) * KB + (F0 & 1) * 512 + l16;
#pragma unroll
  for (int u = 0; u < 3; ++u) { const unsigned base = u == 0 ? WS_H : u == 1 ? WS_X1 : WS_Z + 14 * KB;
    bst128<16>(sv, rws, o + base); bst128<16>(sv, rws, o + base + KB); bst128<16>(sv, rws, o + base + 2 * KB); bst128<16>(sv, rws, lane < 32 ? o + base + 3 * KB : 0xF0000000u); }
}

DI void phase_qkv(const Params& p, LAS char* lds, unsigned lds0, unsigned long long cbase, int b, int i, int j, int w, int lane ST_PARAM) {
  STAMP(0);
  unsigned epv = 0, cwarm = 0, twarm = 0;
  auto errands = [&] {
    asm volatile("buffer_load_dword %0, %1, %2, 0 offen sc1" : "=v"(epv) : "v"(WS_EP), "s"(mk_rsrc(p.ws, WS_END)) : "memory");
    cwarm = touch_code(cbase, CODE_BYTES, b, lane, true);
  };
  auto errands_done = [&] { WAIT_VM(0); RETIRE(epv); RETIRE(cwarm); RETIRE(twarm); if (lane == 0) *(LAS unsigned*)(lds + K1_EP) = epv + 1u; };

  if (j == 7) {
    const __amdgpu_buffer_rsrc_t rws = mk_rsrc(p.ws, WS_END);
    if (w >= 8) {
      const int lw = w - 8;
      if (lw == 1) { errands(); errands_done(); }
      fill_sentinels(rws, i, j, lw, lane);
#pragma unroll
      for (int n = 0; n < 4; ++n) { const int pr = (lw * 4 + n) * 4 + (lane >> 4), gq = (lane >> 2) & 3, row = lane & 3; const unsigned o = row == 0 ? ONES : 0u;
        bst128<16>(u32x4{o, o, o, o}, rws, WS_V + ((((pr >> 1) * 64 + 2 * i + (pr & 1)) * 2 + 1) * 64 + gq * 16 + 12 + row) * 16); }
      if (i == 0 || i == 31) {
#pragma unroll
        for (int n = 0; n < 7; ++n) bst128<16>(u32x4{0u, 0u, 0u, 0u}, rws, WS_Z + ((i == 0 ? 0 : 129) * 14 + lw * 7 + n) * KB + lane * 16); }
    } else {
    const int colid = w < 2 ? w * 32 + i : (w == 2 && i < 6) ? 64 + i : -1;
    const int nid = w >= 3 ? i * 5 + (w - 3) : 160 + (i - 6);
    u32x4 v[7][4];
    auto other = [&](int m, int& mat, int& ks, int& cb) { if (m < 112) { mat = 0; ks = m / 7; cb = m % 7; } else { mat = 2; ks = (m - 112) / 7; cb = (m - 112) % 7; } };
    const int ccol = colid >> 1, chalf = colid & 1, cmat = ccol < 7 ? 1 : 3, ccb = ccol < 7 ? ccol : ccol - 7;
    f32x4 cs[4] = {};
    if (colid >= 0) {
#pragma unroll
      for (int q = 0; q < 7; ++q) retile_load(p, cmat, 7 * chalf + q, ccb, lane, v[q]);
#pragma unroll
      for (int q = 0; q < 7; ++q) retile_store<true>(p, lds + (2 * w + (q & 1)) * 4096, cmat, 7 * chalf + q, ccb, lane, v[q], cs);
      if ((lane & 15) == 0) {
#pragma unroll
        for (int ft = 0; ft < 4; ++ft) bst128<16>(__builtin_bit_cast(u32x4, cs[ft]), rws, WS_CS + (chalf * 2240 + (cmat == 1 ? 0 : 448) + 64 * ccb + 16 * ft + 4 * (lane >> 4)) * 4); }
    } else {
#pragma unroll
      for (int q = 0; q < 2; ++q) { const int m = nid + 186 * q; if (m < 210) { int mat, ks, cb; other(m, mat, ks, cb); retile_load(p, mat, ks, cb, lane, v[q]); } }
#pragma unroll
      for (int q = 0; q < 2; ++q) { const int m = nid + 186 * q; if (m < 210) { int mat, ks, cb; other(m, mat, ks, cb); retile_store<false>(p, lds + (2 * w + (q & 1)) * 4096, mat, ks, cb, lane, v[q], cs); } }
    }
    }
    STAMP(7);
    WAIT_VM(0); BARS();
    return;
  }
  if (w == 6 || w == 7) {
    if (w == 7) {
      errands();
#pragma unroll
      for (int c = 0; c < 3; ++c) BARS();
      errands_done();
    } else {
#pragma unroll
      for (int c = 0; c < 3; ++c) BARS();
    }
    fill_sentinels(mk_rsrc(p.ws, WS_END), i, j, w - 6, lane);
#pragma unroll
    for (int c = 3; c < 9; ++c) BARS();
    WAIT_VM(0); BARS();
    return;
  }
  if (w == 4 || w == 5) {
    const int tok = 32 * (w - 4) + (lane & 31), part = lane >> 5;
    const LAS char* xb[4];
#pragma unroll
    for (int q = 0; q < 4; ++q) xb[q] = lds + K1_XS + tok * 128 + ((4 * part + q) ^ ((tok >> 1) & 7)) * 16;
    float s1a = 0.f, s1b = 0.f, s2a = 0.f, s2b = 0.f;
    BARS();
#pragma unroll
    for (int c = 0; c < 7; ++c) {
      u32x4 v[4];
#pragma unroll
      for (int q = 0; q < 4; ++q) v[q] = lds128(xb[q] + c * 8192);
      __builtin_amdgcn_sched_barrier(0);
#pragma unroll
      for (int q = 0; q < 4; ++q)
#pragma unroll
        for (int u = 0; u < 4; ++u) { float lo = bflo(v[q][u]), hi = bfhi(v[q][u]); asm volatile("" : "+v"(lo), "+v"(hi));
          s1a += lo; s1b += hi; s2a = fmaf(lo, lo, s2a); s2b = fmaf(hi, hi, s2b); }
      asm volatile("" : "+v"(s1a), "+v"(s1b), "+v"(s2a), "+v"(s2b));
      if (c == 6) {
        float a1 = s1a + s1b, a2 = s2a + s2b;
        a1 += __shfl_xor(a1, 32); a2 += __shfl_xor(a2, 32);
        const float mean = a1 * (1.f / D), var = fmaxf(a2 * (1.f / D) - mean * mean, 0.f);
        if (lane < 32) { *(LAS float*)(lds + K1_ST + tok * 4) = mean; *(LAS float*)(lds + K1_ST + 256 + tok * 4) = __builtin_amdgcn_rsqf(var + 1e-5f); }
      }
      BARS();
    }
    BARS();
    STAMP(7);
    WAIT_VM(0); BARS();
    return;
  }
  if (w >= 8) {
    const int lw = w - 8;
    const __amdgpu_buffer_rsrc_t rx = mk_rsrc(p.x, S * D * 2), rw = mk_rsrc(p.wqkv, D * 3 * D * 2);
    const unsigned xv0 = (lane >> 3) * 896 + ((lane & 7) ^ (lane >> 4)) * 16, xv1 = (lane >> 3) * 896 + ((lane & 7) ^ (lane >> 4) ^ 4) * 16;
    const unsigned wv = (lane >> 3) * 2688 + (((lane & 7) ^ (((lane >> 4) & 1) * 4)) * 16);
    const int rotq = ROTQ;
    auto issue_chunk = [&](int c) {
      int cc = c + rotq; cc = cc >= 7 ? cc - 7 : cc;
#pragma unroll
      for (int q = 0; q < 4; ++q) { const unsigned n = lw * 4 + q; dma16(rx, (q & 1) ? xv1 : xv0, (64 * i + 8 * n) * 896 + cc * 128, lds0 + K1_XS + c * 8192 + n * 1024); }
#pragma unroll
      for (int q = 0; q < 12; ++q) { const unsigned n = lw * 12 + q, type = n >> 3, rg = n & 7;
        dma16(rw, wv, (64 * cc + 8 * rg) * 2688 + type * 896 + j * 128, lds0 + K1_W0 + (c & 3) * K1_SLOT + type * 8192 + rg * 1024); }
    };
    issue_chunk(0); issue_chunk(1);
    STAMP(1);
    WAIT_VM(16); STAMP(2); BARS();
    issue_chunk(2); issue_chunk(3);
    WAIT_VM(32); STAMP(3); BARS();
    issue_chunk(4);
    WAIT_VM(32); STAMP(8); BARS();
    issue_chunk(5);
    WAIT_VM(32); STAMP(9); BARS();
    issue_chunk(6);
    WAIT_VM(32); STAMP(10); BARS();
    WAIT_VM(16); STAMP(11); BARS();
    WAIT_VM(0); STAMP(5); BARS();
    BAR();
    BARS();
    STAMP(7);
    WAIT_VM(0); BARS();
    return;
  }
  const int tb = w & 1, e = w >> 1, r = lane & 31, h = lane >> 5;
  f32x16 aq = {}, ak = {}, av = {};
  f32x4 cq = {}, ck = {}, cv = {};
  const unsigned cs1 = ((lane & 15) == ((lane >> 4) & 1)) ? ONES : 0u;
  const u32x4 csel = {cs1, cs1, cs1, cs1};
  const int q4 = (lane & 15) >> 2, pp = lane & 3, blk = (lane >> 4) & 1;
  const LAS char* xb[4];
#pragma unroll
  for (int kl = 0; kl < 4; ++kl) xb[kl] = lds + K1_XS + (32 * tb + r) * 128 + ((2 * (kl ^ tb) + h) ^ ((r >> 1) & 7)) * 16;
  const unsigned wb = K1_W0 + (8 * h + q4) * 128 + (((8 * e + 4 * blk + pp) ^ (8 * ((q4 >> 1) & 1))) * 8);
  const LAS char* we = lds + wb + tb * 2048;
  const LAS char* wo = lds + wb + (1 - tb) * 2048;
  u32x4 xf[3], f[3][3];
  auto load_step = [&](int n) {
    xf[n % 3] = lds128(xb[n & 3] + (n >> 2) * 8192);
    const LAS char* wp = ((n & 1) ? wo : we) + ((n >> 2) & 3) * K1_SLOT + (n & 2) * 2048;
#pragma unroll
    for (int t = 0; t < 3; ++t) { const u32x2 lo = tr_read(wp + t * 8192), hi = tr_read(wp + t * 8192 + 512); f[n % 3][t] = u32x4{lo[0], lo[1], hi[0], hi[1]}; }
  };
  const __amdgpu_buffer_rsrc_t rws = mk_rsrc(p.ws, WS_END);
  constexpr unsigned K1_IMG = K1_W0 + 3 * K1_SLOT;
  int g0half; bool g0last; const int G0 = qk_slot(64 * j, g0half, g0last);
  unsigned qdst[4], zdst[4], voff;
  {
#pragma unroll
    for (int g4 = 0; g4 < 4; ++g4) { int half; bool last; const int G = qk_slot(64 * j + 32 * e + 8 * g4 + 4 * h, half, last);
      qdst[g4] = K1_IMG + (G - G0) * KB + ((2 * tb + (r >> 4)) * 16 + (r & 15)) * 16 + half * 8;
      zdst[g4] = last ? qdst[g4] + 8 : K1_IMG + 2 * 10 * KB + lane * 8;
      asm volatile("" : "+v"(qdst[g4]), "+v"(zdst[g4])); }
    const unsigned n = 64 * j + 32 * e + r, head = n / 28, d = n - 28 * head;
    voff = WS_V + (((head * 64 + 2 * i + tb) * 2 + (d >> 4)) * 64 + h * 16 + (d & 15)) * 16; asm volatile("" : "+v"(voff)); }
  STAMP(1);
  BARS();
  STAMP(2);
  load_step(0);
#pragma unroll
  for (int n = 0; n < 28; ++n) {
    if ((n & 3) == 3 && n < 27) { if (n == 7) STAMP(8); if (n == 11) STAMP(10); BARS(); if (n == 7) { STAMP(9); CSTAMP(13); } if (n == 11) { STAMP(11); CSTAMP(14); } if (n == 15) STAMP(12); }
    if (n < 27) load_step(n + 1);
    aq = mfma32(f[n % 3][0], xf[n % 3], aq);
    ak = mfma32(f[n % 3][1], xf[n % 3], ak);
    av = mfma32(xf[n % 3], f[n % 3][2], av);
    if (!(n & 1)) {
      cq = mfma16(f[n % 3][0], csel, cq); ck = mfma16(f[n % 3][1], csel, ck); cv = mfma16(f[n % 3][2], csel, cv);
    }
    __builtin_amdgcn_sched_group_barrier(0x8, 1, 0); __builtin_amdgcn_sched_group_barrier(0x100, 3, 0);
    __builtin_amdgcn_sched_group_barrier(0x8, 1, 0); __builtin_amdgcn_sched_group_barrier(0x100, 2, 0);
    __builtin_amdgcn_sched_group_barrier(0x8, 1, 0); __builtin_amdgcn_sched_group_barrier(0x100, 2, 0);
    __builtin_amdgcn_sched_group_barrier(0x8, 3, 0);
    __builtin_amdgcn_sched_barrier(0);
  }
  STAMP(4);
  LAS float* cs = (LAS float*)(lds + K1_CS) + e * 192;
  if ((lane & 15) < 2) { LAS float* o = cs + tb * 96 + 16 * (lane & 1) + 4 * (lane >> 4);
    *(LAS f32x4*)o = cq; *(LAS f32x4*)(o + 32) = ck; *(LAS f32x4*)(o + 64) = cv; }
  BAR();
  STAMP(5);
  const LAS float* st = (const LAS float*)(lds + K1_ST);
  f32x4 cqk[2][4][2], m4[4], r4[4];
#pragma unroll
  for (int t = 0; t < 2; ++t)
#pragma unroll
    for (int g4 = 0; g4 < 4; ++g4) { cqk[t][g4][0] = *(const LAS f32x4*)(cs + t * 32 + 8 * g4 + 4 * h); cqk[t][g4][1] = *(const LAS f32x4*)(cs + 96 + t * 32 + 8 * g4 + 4 * h); }
  const float mean = st[32 * tb + r], rstd = st[64 + 32 * tb + r];
  const float cs2 = cs[64 + r] + cs[96 + 64 + r];
#pragma unroll
  for (int g4 = 0; g4 < 4; ++g4) { m4[g4] = *(const LAS f32x4*)(st + 32 * tb + 8 * g4 + 4 * h); r4[g4] = *(const LAS f32x4*)(st + 64 + 32 * tb + 8 * g4 + 4 * h); }
  __builtin_amdgcn_sched_barrier(0);
#pragma unroll
  for (int g4 = 0; g4 < 4; ++g4)
#pragma unroll
    for (int t = 0; t < 2; ++t) {
      const f32x16& a = t ? ak : aq;
      float v[4];
#pragma unroll
      for (int q = 0; q < 4; ++q) v[q] = rstd * (a[4 * g4 + q] - mean * (cqk[t][g4][0][q] + cqk[t][g4][1][q]));
      *(LAS u32x2*)(lds + qdst[g4] + t * 10 * KB) = u32x2{pk2(v[0], v[1]), pk2(v[2], v[3])};
      *(LAS u32x2*)(lds + zdst[g4] + (zdst[g4] < K1_IMG + 2 * 10 * KB ? t * 10 * KB : 0)) = u32x2{0u, 0u};
    }
  {
    float v[16];
#pragma unroll
    for (int g4 = 0; g4 < 4; ++g4)
#pragma unroll
      for (int q = 0; q < 4; ++q) v[4 * g4 + q] = r4[g4][q] * (av[4 * g4 + q] - m4[g4][q] * cs2);
    const u32x4 oa = {pk2(v[0], v[1]), pk2(v[2], v[3]), pk2(v[8], v[9]), pk2(v[10], v[11])};
    const u32x4 ob = {pk2(v[4], v[5]), pk2(v[6], v[7]), pk2(v[12], v[13]), pk2(v[14], v[15])};
    bst128<AUX_QKV>(oa, rws, voff);
    bst128<AUX_QKV>(ob, rws, voff + 512);
  }
  BARS();
  { int lh; bool ll; const int NS = qk_slot(64 * j + 60, lh, ll) - G0 + 1;
#pragma unroll
    for (int q = 0; q < 3; ++q) { const int ls = w + 4 * q;
      if (ls < NS) { const int G = G0 + ls; const unsigned o = WS_Q + (((G >> 2) * 128 + 4 * i + (lane >> 4)) * 64 + (G & 3) * 16 + (lane & 15)) * 16;
        bst128<AUX_QKV>(lds128(lds + K1_IMG + ls * KB + lane * 16), rws, o);
        bst128<AUX_QKV>(lds128(lds + K1_IMG + (10 + ls) * KB + lane * 16), rws, o + (WS_K - WS_Q)); } }
  }
  STAMP(6);
  WAIT_VM(0);
  STAMP(7);
  WAIT_VM(0); BARS();
}

constexpr unsigned L_K = 0, L_WOA = 40 * KB, L_V = 120 * KB, L_WOB = 0;
constexpr unsigned L_O = 128 * KB;
constexpr unsigned L_W = 0;
constexpr unsigned L_H = 98 * KB, L_ST4 = 126 * KB;
constexpr unsigned L_Z = 128 * KB;
constexpr unsigned L_X1 = 96 * KB, L_ST6 = 152 * KB;
constexpr unsigned L_HID = 96 * KB;
constexpr unsigned L_RA = 0, L_RB = 32 * KB, L_RC = 64 * KB, L_RD = 128 * KB;
constexpr unsigned K2_LDS = 163840;
constexpr unsigned SPIN_MAX = 1u << 16;

DI u32x4 bload(__amdgpu_buffer_rsrc_t rs, unsigned voff, unsigned soff) { return __builtin_amdgcn_raw_buffer_load_b128(rs, voff, soff, 0); }
DI u32x4 bload_sc1(__amdgpu_buffer_rsrc_t rs, unsigned voff, unsigned soff) { return __builtin_amdgcn_raw_buffer_load_b128(rs, voff, soff, 16); }
DI void bstore_sc1(u32x4 v, __amdgpu_buffer_rsrc_t rs, unsigned voff, unsigned soff) { bst128<16>(v, rs, voff + soff); }
DI unsigned umax4(u32x4 v) { return max(max(v[0], v[1]), max(v[2], v[3])); }
#define FENCE() asm volatile("" ::: "memory")
typedef __attribute__((ext_vector_type(2))) float f32x2_;
DI float gelu_tanh(float x) {
  const float e = __builtin_amdgcn_exp2f(x * fmaf(x * x, -0.10294324f, -2.3022082f));
  return x * __builtin_amdgcn_rcpf(1.f + e);
}
DI u32x4 pair_tiles(u32x2 a, u32x2 b) {
  const auto s0 = __builtin_amdgcn_permlane16_swap(a[0], b[0], false, false), s1 = __builtin_amdgcn_permlane16_swap(a[1], b[1], false, false);
  return u32x4{s0[0], s1[0], s0[1], s1[1]};
}
template <int NT, int NS, class F>
DI void fetch_payload(u32x4 (&v)[NT][2], __amdgpu_buffer_rsrc_t rs, unsigned voff0, unsigned voff1, unsigned soff, unsigned tstride, LAS char* lds, unsigned lds0, unsigned dst, int lane, F fill, void* dbg = nullptr) {
  for (unsigned spin = 0;; ++spin) {
#pragma unroll
    for (int t = 0; t < NT; ++t) { dma16_sc1(rs, voff0, soff + t * tstride, lds0 + dst + t * NS * KB); dma16_sc1(rs, voff1, soff + t * tstride, lds0 + dst + (t * NS + 1) * KB); }
    if (spin == 0) { fill(); __builtin_amdgcn_sched_barrier(0); }
    WAIT_VM(0);
    unsigned m = 0;
#pragma unroll
    for (int t = 0; t < NT; ++t)
#pragma unroll
      for (int q = 0; q < 2; ++q) { v[t][q] = lds128(lds + dst + (t * NS + q) * KB + lane * 16); m = max(m, max(max(v[t][q][0], v[t][q][1]), max(v[t][q][2], v[t][q][3]))); }
    if (!__any(m == SENT) || spin > SPIN_MAX) break;
  }
}
template <int NT> DI void row_stats(const u32x4 (&v)[NT][2], u32x4 ones, LAS float* stp, int w, int l15, int g) {
#pragma unroll
  for (int t = 0; t < NT; ++t) {
    f32x4 d1 = {}, d2 = {};
#pragma unroll
    for (int q = 0; q < 2; ++q) { d1 = __builtin_amdgcn_mfma_f32_16x16x32_bf16(__builtin_bit_cast(bf16x8, ones), __builtin_bit_cast(bf16x8, v[t][q]), d1, 0, 0, 0);
                                  d2 = __builtin_amdgcn_mfma_f32_16x16x32_bf16(__builtin_bit_cast(bf16x8, v[t][q]), __builtin_bit_cast(bf16x8, v[t][q]), d2, 0, 0, 0); }
    const float lo = (l15 & 1) ? d2[1] : d2[0], hi = (l15 & 1) ? d2[3] : d2[2], s2 = (l15 & 2) ? hi : lo;
    if (g == (l15 >> 2)) *(LAS f32x2_*)(stp + ((t * 16 + l15) * 8 + w) * 2) = f32x2_{d1[0], s2};
  }
}

#define SB() __builtin_amdgcn_sched_barrier(0)
typedef __attribute__((ext_vector_type(2))) float f32x2;
DI float max3(float a, float b, float c) { float r; asm("v_max3_f32 %0, %1, %2, %3" : "=v"(r) : "v"(a), "v"(b), "v"(c)); return r; }
DI float gsum(float v) {
  const auto a = __builtin_amdgcn_permlane16_swap(__float_as_uint(v), __float_as_uint(v), false, false); const float s = __uint_as_float(a[0]) + __uint_as_float(a[1]);
  const auto c = __builtin_amdgcn_permlane32_swap(__float_as_uint(s), __float_as_uint(s), false, false); return __uint_as_float(c[0]) + __uint_as_float(c[1]);
}
DI float gmax(float v) {
  const auto a = __builtin_amdgcn_permlane16_swap(__float_as_uint(v), __float_as_uint(v), false, false); const float s = max3(__uint_as_float(a[0]), __uint_as_float(a[1]), __uint_as_float(a[1]));
  const auto c = __builtin_amdgcn_permlane32_swap(__float_as_uint(s), __float_as_uint(s), false, false); return max3(__uint_as_float(c[0]), __uint_as_float(c[1]), __uint_as_float(c[1]));
}
DI unsigned gelu2(f32x2 x) {
  const f32x2 t = x * (x * x * f32x2{-0.10294324f, -0.10294324f} + f32x2{-2.3022082f, -2.3022082f});
  const f32x2 d = f32x2{__builtin_amdgcn_exp2f(t[0]), __builtin_amdgcn_exp2f(t[1])} + f32x2{1.f, 1.f};
  const f32x2 r = x * f32x2{__builtin_amdgcn_rcpf(d[0]), __builtin_amdgcn_rcpf(d[1])};
  return pk2(r[0], r[1]);
}
DI unsigned gelu2s(f32x2 a, float c1, float c2, float is) {
  const f32x2 t = a * (a * a * f32x2{c2, c2} + f32x2{c1, c1});
  const f32x2 d = f32x2{__builtin_amdgcn_exp2f(t[0]), __builtin_amdgcn_exp2f(t[1])} * f32x2{is, is} + f32x2{is, is};
  const f32x2 r = a * f32x2{__builtin_amdgcn_rcpf(d[0]), __builtin_amdgcn_rcpf(d[1])};
  return pk2(r[0], r[1]);
}
template <int N, int PD, class L, class U, class M> DI void lds_pipe(L load, U use, M mid) {
  u32x4 buf[PD + 1];
#pragma unroll
  for (int n = 0; n < PD; ++n) buf[n] = load(n);
  SB();
#pragma unroll
  for (int n = 0; n < N; ++n) {
    if (n + PD < N) buf[(n + PD) % (PD + 1)] = load(n + PD);
    SB();
    use(n, buf[n % (PD + 1)]);
    mid(n);
    SB();
  }
}

DI void wait_flags(const Params& p, bool on, unsigned off, unsigned epoch) {
  const GAS unsigned* f = (const GAS unsigned*)(p.ws + (on ? off : WS_EP));
  for (unsigned spin = 0; spin < SPIN_MAX; ++spin) {
    const unsigned v = __hip_atomic_load(f, __ATOMIC_RELAXED, __HIP_MEMORY_SCOPE_AGENT);
    if (__all(!on || v == epoch)) break;
    __builtin_amdgcn_s_sleep(2);
  }
}

__global__ void __launch_bounds__(640, 3) block_fused(Params p) {
  extern __shared__ __attribute__((aligned(16))) char smem[];
  LAS char* lds = (LAS char*)smem;
  const unsigned lds0 = (unsigned)(size_t)lds;
  const int b = blockIdx.x, tid = threadIdx.x, lane = tid & 63;
  const int w = rfl(tid >> 6);
#define PROBE(n)
  PROBE(0);
  const unsigned long long cbase = code_base();
  int i, j; map_block(b, i, j);
  const __amdgpu_buffer_rsrc_t rws = mk_rsrc(p.ws, WS_END);
  ST_DECLQ;
  phase_qkv(p, lds, lds0, cbase, b, i, j, w, lane ST_ARGQ);
  PROBE(1);
  const unsigned epoch = rfl(*(const LAS unsigned*)(lds + K1_EP));
  ST_DECL; STAMP(0);
  const int l15 = lane & 15, g = lane >> 4;
  const unsigned l16 = lane * 16;
  constexpr unsigned OOB = 0xF0000000u;
  const int t32 = 2 * i + (j >> 2), c4 = j & 3, tt0 = 2 * t32;
  const int sid = XMAP == 1 ? (((i & 3) << 1) | (j >> 2)) : XMAP == 2 ? (i & 7) : ((i & 15) >> 1);
  const int rot14 = (sid * 7) >> 2, rot16 = 2 * sid, rotc = XMAP == 1 ? ((i & 3) * 3) >> 1 : (i & 7) % 6;
  auto pcol = [&](int s) { int u = s + rotc; u = u >= 6 ? u - 6 : u; int c = j + 1 + u; return c >= 7 ? c - 7 : c; };
  const u32x4 sentv = {SENT, SENT, SENT, SENT};

  if (w >= 8) {
    const int lw = w - 8;
    if (lw == 0 && lane == 0) __hip_atomic_store((GAS unsigned*)(p.ws + WS_FLG + (i * 8 + j) * 128), epoch, __ATOMIC_RELAXED, __HIP_MEMORY_SCOPE_AGENT);
    { const int t = i - 4 + (lane >> 3);
      if (lw == 0) wait_flags(p, lane < 48 && t >= 0 && t < 32, WS_FLG + (t * 8 + (lane & 7)) * 128, epoch);
      else wait_flags(p, lane < 32, WS_FLG + (lane * 8 + 7) * 128, epoch); }
    STAMP(6);
    BARS();
#pragma unroll
    for (int q = 0; q < 5; ++q) { const int n = 2 * q + lw, hh = n / 5, kt = 4 * i - 16 + 4 * (n % 5);
      dma4(rws, l16, WS_K + ((2 * j + hh) * 128 + (kt < 0 ? 0 : kt)) * KB, lds0 + L_K + n * 4 * KB); }
#pragma unroll
    for (int q = 0; q < 5; ++q) { const int n = 2 * q + lw, hh = n / 5, kb = 2 * i - 8 + 2 * (n % 5);
      dma4(rws, l16, WS_V + ((2 * j + hh) * 64 + (kb < 0 ? 0 : kb)) * 2 * KB, lds0 + L_V + n * 4 * KB); }
    STAMP(1); WAIT_VM(20); STAMP(2); BARS();
    auto wo_slot = [&](int q) { const int n = 2 * q + lw; const unsigned src = WS_WO + (c4 * 16 + ((n + rot16) & 15)) * 7 * KB, dst = lds0 + (n <= 10 ? L_WOA + n * 7 * KB : L_WOB + (n - 11) * 7 * KB);
      dma4(rws, l16, src, dst); dma3(rws, l16, src + 4 * KB, dst + 4 * KB); };
    wo_slot(0); wo_slot(1);
    STAMP(3); WAIT_VM(14); STAMP(4); BARS(); STAMP(5);
#pragma unroll
    for (int q = 2; q < 8; ++q) wo_slot(q);
    WAIT_VM(0); STAMP(7); BARS(); STAMP(8);
    auto w_stage = [&](unsigned wsrc, int q0, int q1) {
#pragma unroll
      for (int q = q0; q < q1; ++q) { const int n = 2 * q + lw; int ks = n + rot14; ks = ks >= 14 ? ks - 14 : ks; const unsigned src = wsrc + (c4 * 14 + ks) * 7 * KB, dst = lds0 + L_W + n * 7 * KB;
        dma4(rws, l16, src, dst); dma3(rws, l16, src + 4 * KB, dst + 4 * KB); } };
    BARS(); STAMP(9);
    w_stage(WS_W1, 0, 2);
    BARS();
    w_stage(WS_W1, 2, 7);
    STAMP(10); WAIT_VM(0); STAMP(11); BARS(); STAMP(12);
    BARS(); STAMP(13);
    w_stage(WS_W2, 0, 2);
    BARS();
    w_stage(WS_W2, 2, 7);
    STAMP(14); WAIT_VM(0); STAMP(15); BARS(); STAMP(16);
    BARS(); STAMP(17);
    if (j == 7) { BARS(); BARS(); ST_FLUSH(1, p.ws, b, w); ST_FLUSHQ(p.ws, b, w); return; }
    const __amdgpu_buffer_rsrc_t rw3 = mk_rsrc(p.w3, D * F * 2), rw4 = mk_rsrc(p.w4, F * D * 2);
    unsigned v3[2][4], v4[4];
#pragma unroll
    for (int k = 0; k < 4; ++k) {
#pragma unroll
      for (int e = 0; e < 2; ++e) { const int rr = lane >> 5, f = (2 * (k & 1) + rr) | (e << 2); v3[e][k] = rr * 3584 + (((lane & 31) ^ (f << 1)) * 16) + k * 6144; }
      const int rr = lane >> 3; v4[k] = rr * 896 + (((lane & 7) ^ (((rr >> 1) & 3) << 1)) * 16) + k * 6144;
    }
    auto w3_round = [&](int r, unsigned slot) { int ks = 2 * r + lw + rot14; ks = ks >= 14 ? ks - 14 : ks;
#pragma unroll
      for (int u = 0; u < 4; ++u) dma4v(rw3, v3[u & 1], (ks * 32 + 8 * u) * 3584 + j * 512, lds0 + slot + (lw * 16 + u * 4) * KB); };
    auto w4_step = [&](int s, unsigned slot) { const int c = s < 6 ? pcol(s) : j;
#pragma unroll
      for (int u = 0; u < 4; ++u) dma4v(rw4, v4, (256 * j + 128 * lw + 32 * u) * 896 + c * 128, lds0 + slot + (lw * 16 + u * 4) * KB); };
    auto arrive = [&](int s) { if (lw == 0 && lane == 0) __hip_atomic_store((GAS unsigned*)(p.ws + WS_PF + ((i * 7 + pcol(s)) * 8 + j) * 128), epoch, __ATOMIC_RELAXED, __HIP_MEMORY_SCOPE_AGENT); };
    w3_round(0, L_RA);
    BARS();
    w3_round(1, L_RB); w3_round(2, L_RC);
    BARS();
    STAMP(18); WAIT_VM(0); STAMP(19); BARS();
    BARS(); STAMP(20);
    BARS();
    w3_round(3, L_RA); w3_round(4, L_RB);
    BARS();
    BARS();
    WAIT_VM(16); BARS(); w3_round(5, L_RC);
    WAIT_VM(16); BARS(); w3_round(6, L_RA);
    WAIT_VM(16); BARS(); w4_step(0, L_RB);
    WAIT_VM(16); BARS(); w4_step(1, L_RC);
    BARS(); w4_step(2, L_RA); w4_step(3, L_RD);
    STAMP(21); WAIT_VM(16); STAMP(22); BARS();
    BARS(); w4_step(4, L_RB);
    BARS(); w4_step(5, L_RC);
    WAIT_VM(32); BARS(); w4_step(6, L_RA);
    WAIT_VM(32); BARS(); arrive(0);
    WAIT_VM(16); BARS(); arrive(1);
    WAIT_VM(0); BARS(); arrive(2);
    BARS(); arrive(3); arrive(4); arrive(5);
    if (b == 0 && lw == 0) {
#pragma unroll
      for (int q = 0; q < 4; ++q) wait_flags(p, true, WS_FLG + (q * 64 + lane) * 128, epoch);
      if (lane == 0) __hip_atomic_store((GAS unsigned*)(p.ws + WS_EP), epoch, __ATOMIC_RELAXED, __HIP_MEMORY_SCOPE_AGENT);
    }
    STAMP(23); ST_FLUSH(1, p.ws, b, w); ST_FLUSHQ(p.ws, b, w);
    return;
  }

  const int hl = w >> 2, a = w & 3, head = 2 * j + hl, tt = 4 * i + a;
  const bool act = w < 7;
  const int wf = act ? w : 6, Fg = 7 * c4 + wf;
  u32x4 qf; u32x2 xr[2]; u32x4 cs1u[2], cs3u[2][2];
  const u32x4 ones = {ONES, ONES, ONES, ONES};
  unsigned act_off = act ? (((tt0 + (g & 1)) * 14 + (Fg >> 1)) * 64 + (2 * (Fg & 1) + (g >> 1)) * 16 + l15) * 16 : OOB;
  const int ka = (2 * w + rot14) % 14, kb14 = (2 * w + 1 + rot14) % 14;
  unsigned pv0 = l16 + ka * KB, pv1 = l16 + kb14 * KB, ov0 = l16 + ((2 * w + rot16) & 15) * KB, ov1 = l16 + ((2 * w + 1 + rot16) & 15) * KB;
  unsigned zv[2];
#pragma unroll
  for (int q = 0; q < 2; ++q) { const int ks = q ? kb14 : ka, f0 = 32 * ks + 8 * g, sh = f0 < 112 ? -1 : f0 < 224 ? 1 : f0 < 336 ? -2 : 2, tz = 16 * (tt0 + 1) + l15 + sh;
    zv[q] = (((tz >> 4) * 14 + ks) * 64 + g * 16 + (tz & 15)) * 16; }
  u32x4 selA, selB;
  { const unsigned v = g == (l15 >> 2) ? ((l15 & 1) ? 0x3F800000u : 0x00003F80u) : 0u, d0 = (l15 & 2) ? 0u : v, d1 = (l15 & 2) ? v : 0u;
    selA = u32x4{d0, d1, 0u, 0u}; selB = u32x4{0u, 0u, d0, d1}; }
  unsigned ooff = ((tt * 16 + head) * 64 + ((g >> 1) + 2 * (g & 1)) * 16 + l15) * 16 + WS_O;
  asm volatile("" : "+v"(act_off), "+v"(pv0), "+v"(pv1), "+v"(ov0), "+v"(ov1), "+v"(zv[0]), "+v"(zv[1]), "+v"(selA), "+v"(selB), "+v"(ooff));
  BARS();
  aload128_sc1(qf, rws, l16, WS_Q + (head * 128 + tt) * KB);
  WAIT_VM(0); RETIRE(qf);
  STAMP(1);
  BARS();
  PROBE(2);
  { const __amdgpu_buffer_rsrc_t rx = mk_rsrc(p.x, S * D * 2);
#pragma unroll
    for (int t = 0; t < 2; ++t) aload64(xr[t], rx, ((16 * (tt0 + t) + l15) * D + 16 * Fg + 4 * g) * 2, 0); }
#pragma unroll
  for (int hf = 0; hf < 2; ++hf) { aload128_sc1(cs1u[hf], rws, (16 * Fg + 4 * g) * 4, WS_CS + hf * 2240 * 4);
#pragma unroll
    for (int f = 0; f < 2; ++f) aload128_sc1(cs3u[hf][f], rws, (448 + 256 * (j < 7 ? j : 6) + 32 * w + 16 * f + 4 * g) * 4, WS_CS + hf * 2240 * 4); }

  u32x4 opiece;
  {
    const int par = a & 1, kl0 = hl * 20 + (a & ~1), vl0 = hl * 20 + (a >> 1) * 2;
    f32x4 sc[18];
    const LAS char* kp = lds + L_K + kl0 * KB + l16;
    const f32x4 zero4 = {0.f, 0.f, 0.f, 0.f};
    lds_pipe<18, 8>([&](int n) { return lds128(kp + n * KB); }, [&](int n, u32x4 kf) { sc[n] = mfma16(kf, qf, zero4); }, [](int) {});
    const int dq = l15 - 4 * g;
    const float NEG = -__builtin_inff();
    if (par == 0) {
#pragma unroll
      for (int r = 0; r < 4; ++r) { const bool ge = dq >= r; sc[0][r] = ge ? NEG : sc[0][r]; sc[16][r] = ge ? sc[16][r] : NEG; sc[17][r] = NEG; }
    } else {
#pragma unroll
      for (int r = 0; r < 4; ++r) { const bool ge = dq >= r; sc[1][r] = ge ? NEG : sc[1][r]; sc[17][r] = ge ? sc[17][r] : NEG; sc[0][r] = NEG; }
    }
    const int nneg = 16 - (tt & ~1);
    if (nneg > 0) {
#pragma unroll
      for (int n = 0; n < 16; ++n) if (n < nneg) sc[n] = f32x4{NEG, NEG, NEG, NEG};
    }
    float m = max3(sc[0][0], sc[0][1], sc[0][2]), m2 = max3(sc[0][3], sc[1][0], sc[1][1]);
    m = max3(m, sc[1][2], sc[1][3]);
#pragma unroll
    for (int n = 2; n < 18; ++n) { m = max3(m, sc[n][0], sc[n][1]); m2 = max3(m2, sc[n][2], sc[n][3]); }
    m = gmax(max3(m, m2, m2));
    const float cexp = 0.18898223650461363f * 1.4426950408889634f, mc = -m * cexp;
    STAMP(14);
    BARS();
    STAMP(15);
    f32x4 o0 = zero4, o1 = zero4;
    const LAS char* vp = lds + L_V + vl0 * KB + l16;
    u32x4 v0 = lds128(vp), v1 = lds128(vp + KB);
#pragma unroll
    for (int mm = 0; mm < 9; ++mm) {
      u32x4 n0, n1;
      if (mm < 8) { n0 = lds128(vp + (2 * mm + 2) * KB); n1 = lds128(vp + (2 * mm + 3) * KB); }
      u32x4 pf;
#pragma unroll
      for (int q = 0; q < 4; ++q) { const f32x4& sv = sc[2 * mm + (q >> 1)];
        pf[q] = pk2(__builtin_amdgcn_exp2f(fmaf(sv[2 * (q & 1)], cexp, mc)), __builtin_amdgcn_exp2f(fmaf(sv[2 * (q & 1) + 1], cexp, mc))); }
      o0 = mfma16(v0, pf, o0);
      o1 = mfma16(v1, pf, o1);
      if (mm < 8) { v0 = n0; v1 = n1; }
      SB();
    }
    const float rl = __builtin_amdgcn_rcpf(gsum(g == 3 ? o1[0] : 0.f));
    opiece = pair_tiles(u32x2{pk2(o0[0] * rl, o0[1] * rl), pk2(o0[2] * rl, o0[3] * rl)}, u32x2{pk2(o1[0] * rl, o1[1] * rl), pk2(o1[2] * rl, o1[3] * rl)});
  }
  STAMP(16);
  WAIT_VM(0); RETIRE(xr[0]); RETIRE(xr[1]); RETIRE(cs1u[0]); RETIRE(cs1u[1]); RETIRE(cs3u[0][0]); RETIRE(cs3u[0][1]); RETIRE(cs3u[1][0]); RETIRE(cs3u[1][1]);
  const f32x4 cs1 = __builtin_bit_cast(f32x4, cs1u[0]) + __builtin_bit_cast(f32x4, cs1u[1]);
  f32x4 cs3[2];
#pragma unroll
  for (int f = 0; f < 2; ++f) cs3[f] = __builtin_bit_cast(f32x4, cs3u[0][f]) + __builtin_bit_cast(f32x4, cs3u[1][f]);
  BARS();
  bst128<16>(opiece, rws, ooff);
  u32x4 wr[16];
#pragma unroll
  for (int n = 0; n < 16; ++n) wr[n] = lds128(lds + (n <= 10 ? L_WOA + n * 7 * KB : L_WOB + (n - 11) * 7 * KB) + wf * KB + l16);
  STAMP(2);
  HWAIT(HX0);

  { u32x4 v[2][2];
    fetch_payload<2, 16>(v, rws, ov0, ov1, WS_O + tt0 * 16 * KB, 16 * KB, lds, lds0, L_O + 2 * w * KB, lane, [] { BARS();   }, p.ws + (40u << 20)); }
  STAMP(3);
  BARS();
  PROBE(3);
  u32x2 hp[2];
  {
    f32x4 acc[2];
#pragma unroll
    for (int t = 0; t < 2; ++t) acc[t] = mfma16(selA, u32x4{xr[t][0], xr[t][1], 0u, 0u}, f32x4{0.f, 0.f, 0.f, 0.f});
    const LAS char* op = lds + L_O + l16;
    lds_pipe<32, 8>([&](int n) { return lds128(op + ((n & 1) * 16 + (n >> 1)) * KB); }, [&](int n, u32x4 f) { acc[n & 1] = mfma16(wr[n >> 1], f, acc[n & 1]); }, [](int) {});
#pragma unroll
    for (int t = 0; t < 2; ++t) hp[t] = u32x2{pk2(acc[t][0], acc[t][1]), pk2(acc[t][2], acc[t][3])};
    bst128<16>(pair_tiles(hp[0], hp[1]), rws, act_off + WS_H);
  }
  STAMP(4);
  HWAIT(HX1);

  LAS float* st4 = (LAS float*)(lds + L_ST4);
  auto stage_weights = [&] { BARS();
#pragma unroll
    for (int n = 0; n < 14; ++n) wr[n] = lds128(lds + L_W + (n * 7 + wf) * KB + l16);
    BARS(); };
  { u32x4 v[2][2] = {};
    if (act) fetch_payload<2, 14>(v, rws, pv0, pv1, WS_H + tt0 * 14 * KB, 14 * KB, lds, lds0, L_H + 2 * w * KB, lane, stage_weights, p.ws + (40u << 20) + 4); else stage_weights();
    row_stats<2>(v, ones, st4, w, l15, g); }
  STAMP(5);
  BARS();
  f32x2 yv[2][2];
  {
    const LAS char* hpz = lds + L_H + l16;
    f32x4 pt[2];
#pragma unroll
    for (int t = 0; t < 2; ++t) pt[t] = *(const LAS f32x4*)(st4 + ((t * 16 + l15) * 8 + 2 * g) * 2);
    f32x4 acc[2] = {};
    lds_pipe<28, 8>([&](int n) { return lds128(hpz + ((n & 1) * 14 + (n >> 1)) * KB); }, [&](int n, u32x4 f) { acc[n & 1] = mfma16(wr[n >> 1], f, acc[n & 1]); }, [](int) {});
    u32x2 zp[2];
#pragma unroll
    for (int t = 0; t < 2; ++t) {
      const float s1 = gsum(pt[t][0] + pt[t][2]), s2 = gsum(pt[t][1] + pt[t][3]);
      const float mean = s1 * (1.f / D), var = fmaxf(s2 * (1.f / D) - mean * mean, 0.f);
      const float r1 = __builtin_amdgcn_rsqf(var + 1e-5f);
      const float r12 = r1 * __builtin_amdgcn_rsqf(var * r1 * r1 + 1e-5f);
      const f32x2 m2 = {mean, mean};
#pragma unroll
      for (int q = 0; q < 2; ++q) {
        yv[t][q] = (f32x2{bflo(hp[t][q]), bfhi(hp[t][q])} - m2) * f32x2{r1, r1};
        zp[t][q] = gelu2((f32x2{acc[t][2 * q], acc[t][2 * q + 1]} - m2 * f32x2{cs1[2 * q], cs1[2 * q + 1]}) * f32x2{r12, r12});
      }
    }
    bst128<16>(pair_tiles(zp[0], zp[1]), rws, act_off + WS_Z + 14 * KB);
  }
  STAMP(6);
  HWAIT(HX2);

  { u32x4 v[2][2];
    if (act) fetch_payload<2, 14>(v, rws, zv[0], zv[1], WS_Z, 14 * KB, lds, lds0, L_Z + 2 * w * KB, lane, stage_weights, p.ws + (40u << 20) + 8); else stage_weights(); }
  STAMP(7);
  BARS();
  {
    f32x4 acc[2] = {};
    const LAS char* zb = lds + L_Z + l16;
    lds_pipe<28, 8>([&](int n) { return lds128(zb + ((n & 1) * 14 + (n >> 1)) * KB); }, [&](int n, u32x4 f) { acc[n & 1] = mfma16(wr[n >> 1], f, acc[n & 1]); }, [](int) {});
    BARS();
    u32x2 xp[2];
#pragma unroll
    for (int t = 0; t < 2; ++t) xp[t] = u32x2{pk2(yv[t][0][0] + acc[t][0], yv[t][0][1] + acc[t][1]), pk2(yv[t][1][0] + acc[t][2], yv[t][1][1] + acc[t][3])};
    bst128<16>(pair_tiles(xp[0], xp[1]), rws, act_off + WS_X1);
  }
  STAMP(8);
  if (j == 7) { ST_FLUSH(1, p.ws, b, w); ST_FLUSHQ(p.ws, b, w); return; }
  HWAIT(HX3);

  LAS float* st6 = (LAS float*)(lds + L_ST6);
  const int q4 = l15 >> 2, pp = lane & 3;
  const LAS char* a3[2];
#pragma unroll
  for (int f = 0; f < 2; ++f) a3[f] = lds + (8 * g + q4) * 512 + (((2 * w + f) ^ (q4 | ((g & 1) << 2))) * 32) + pp * 8;
  auto w3frag = [&](unsigned off, int f) { const u32x2 lo = tr_read(a3[f] + off), hi = tr_read(a3[f] + off + 2048); return u32x4{lo[0], lo[1], hi[0], hi[1]}; };
  u32x4 areg[4][2];
  { u32x4 v[4][2] = {};
    auto w3_regs = [&] { BARS();
#pragma unroll
      for (int n = 0; n < 4; ++n)
#pragma unroll
        for (int f = 0; f < 2; ++f) areg[n][f] = w3frag(n * 16 * KB, f);
      BARS(); };
    if (act) fetch_payload<4, 14>(v, rws, pv0, pv1, WS_X1 + 4 * i * 14 * KB, 14 * KB, lds, lds0, L_X1 + 2 * w * KB, lane, w3_regs, p.ws + (40u << 20) + 12); else w3_regs();
    row_stats<4>(v, ones, st6, w, l15, g); }
  STAMP(9);
  BARS();
  PROBE(4);
  {
    const LAS char* xp = lds + L_X1 + l16;
    f32x4 pt[4];
#pragma unroll
    for (int t = 0; t < 4; ++t) pt[t] = *(const LAS f32x4*)(st6 + ((t * 16 + l15) * 8 + 2 * g) * 2);
    f32x4 acc[2][4] = {};
    u32x4 bfr[2][4], afr[2][2];
    auto load_k = [&](int n) { const unsigned slot = ((n >> 1) % 3 == 2 ? L_RC : (n >> 1) % 3 == 0 ? L_RA : L_RB) + (n & 1) * 16 * KB;
#pragma unroll
      for (int f = 0; f < 2; ++f) afr[n & 1][f] = n < 4 ? areg[n][f] : w3frag(slot, f);
#pragma unroll
      for (int t = 0; t < 4; ++t) bfr[n & 1][t] = lds128(xp + (t * 14 + n) * KB); };
    load_k(0);
    SB();
#pragma unroll
    for (int n = 0; n < 14; ++n) {
      if (n & 1) BARS();
      if (n < 13) load_k(n + 1);
#pragma unroll
      for (int t = 0; t < 4; ++t) { acc[0][t] = mfma16(afr[n & 1][0], bfr[n & 1][t], acc[0][t]); acc[1][t] = mfma16(afr[n & 1][1], bfr[n & 1][t], acc[1][t]); }
#pragma unroll
      for (int q = 0; q < 4; ++q) { __builtin_amdgcn_sched_group_barrier(0x8, 1, 0); __builtin_amdgcn_sched_group_barrier(0x100, 2, 0); }
      __builtin_amdgcn_sched_group_barrier(0x8, 4, 0);
      SB();
    }
    STAMP(20);
    float mean[4], gis[4], gc1[4], gc2[4];
#pragma unroll
    for (int t = 0; t < 4; ++t) {
      const float s1 = gsum(pt[t][0] + pt[t][2]), s2 = gsum(pt[t][1] + pt[t][3]);
      mean[t] = s1 * (1.f / D);
      const float ve = fmaxf(s2 * (1.f / D) - mean[t] * mean[t], 0.f) + 1e-5f, rs = __builtin_amdgcn_rsqf(ve);
      gis[t] = ve * rs; gc1[t] = -2.3022082f * rs; gc2[t] = -0.10294324f * rs * rs * rs;
    }
#pragma unroll
    for (int t = 0; t < 4; ++t) {
      u32x4 hv;
#pragma unroll
      for (int q = 0; q < 4; ++q) { const int f = q >> 1, r = 2 * (q & 1);
        hv[q] = gelu2s(f32x2{acc[f][t][r], acc[f][t][r + 1]} - f32x2{mean[t], mean[t]} * f32x2{cs3[f][r], cs3[f][r + 1]}, gc1[t], gc2[t], gis[t]); }
      *(LAS u32x4*)(lds + L_HID + (t * 8 + w) * KB + l16) = hv;
    }
  }
  STAMP(10);
  BARS();
  STAMP(11);
  const int tb = w & 1, ft = w >> 1;
  u32x4 hf[2][8];
#pragma unroll
  for (int ks = 0; ks < 8; ++ks)
#pragma unroll
    for (int t = 0; t < 2; ++t) hf[t][ks] = lds128(lds + L_HID + ((2 * tb + t) * 8 + ks) * KB + l16);
  const unsigned pbase = l16 + WS_P + ((i * 49 + j) * 8 + w) * KB;
  const int Fo = 4 * j + ft;
  u32x2 hres[2], xres[2];
  unsigned ro[2];
#pragma unroll
  for (int t = 0; t < 2; ++t) { ro[t] = (((4 * i + 2 * tb + t) * 14 + (Fo >> 1)) * 64 + (2 * (Fo & 1) + (g >> 1)) * 16 + l15) * 16 + (g & 1) * 8;
    aload64_sc1(hres[t], rws, ro[t], WS_H); aload64_sc1(xres[t], rws, ro[t], WS_X1); }
  f32x4 acc[2] = {};
  {
    constexpr int PD = 4;
    constexpr unsigned slots[7] = {L_RB, L_RC, L_RA, L_RD, L_RB, L_RC, L_RA};
    const LAS char* wp = lds + (4 * g + q4) * 128 + ((ft ^ ((2 * g + (q4 >> 1)) & 3)) * 32) + pp * 8;
    auto w4frag = [&](unsigned off) { const u32x2 lo = tr_read(wp + off), hi = tr_read(wp + off + 2048); return u32x4{lo[0], lo[1], hi[0], hi[1]}; };
    u32x4 buf[PD + 1];
#pragma unroll
    for (int m = 0; m < PD; ++m) buf[m] = w4frag(slots[0] + m * 4096);
    SB();
#pragma unroll
    for (int m = 0; m < 56; ++m) {
      const int s = m >> 3, ks = m & 7;
      if (ks == 8 - PD && s < 6) { if (s >= 3) WAIT_VM(2); BARS(); }
      if (m + PD < 56) buf[(m + PD) % (PD + 1)] = w4frag(slots[(m + PD) >> 3] + ((m + PD) & 7) * 4096);
      SB();
      acc[0] = mfma16(buf[m % (PD + 1)], hf[0][ks], acc[0]); acc[1] = mfma16(buf[m % (PD + 1)], hf[1][ks], acc[1]);
      if (ks == 7 && s < 6) {
        bst128<16>(u32x4{pk2(acc[0][0], acc[0][1]), pk2(acc[0][2], acc[0][3]), pk2(acc[1][0], acc[1][1]), pk2(acc[1][2], acc[1][3])}, rws, pbase + pcol(s) * 56 * KB);
        acc[0] = f32x4{0.f, 0.f, 0.f, 0.f}; acc[1] = acc[0];
      }
      SB();
    }
  }
  STAMP(12);
  PROBE(5);
  {
    const __amdgpu_buffer_rsrc_t rout = mk_rsrc(p.out, S * D * 2);
    u32x4 pv[6];
    const unsigned pin = WS_P + ((i * 7 + j) * 7 * 8 + w) * KB;
    WAIT_VM(0); RETIRE(hres[0]); RETIRE(hres[1]); RETIRE(xres[0]); RETIRE(xres[1]);
    BARS();
    for (unsigned spin = 0; spin < SPIN_MAX; ++spin) {
      const unsigned m = max(max(max(hres[0][0], hres[0][1]), max(xres[0][0], xres[0][1])), max(max(hres[1][0], hres[1][1]), max(xres[1][0], xres[1][1])));
      if (!__any(m == SENT)) break;
#pragma unroll
      for (int t = 0; t < 2; ++t) { aload64_sc1(hres[t], rws, ro[t], WS_H); aload64_sc1(xres[t], rws, ro[t], WS_X1); }
      WAIT_VM(0); RETIRE(hres[0]); RETIRE(hres[1]); RETIRE(xres[0]); RETIRE(xres[1]);
    }
    wait_flags(p, lane < 7 && lane != j, WS_PF + ((i * 7 + j) * 8 + lane) * 128, epoch);
#pragma unroll
    for (int s = 0; s < 6; ++s) { int js = j + 1 + s; js = js >= 7 ? js - 7 : js; aload128_sc1(pv[s], rws, l16, pin + js * 8 * KB); }
    WAIT_VM(0);
#pragma unroll
    for (int s = 0; s < 6; ++s) RETIRE(pv[s]);
    const u32x4 selAB = selA | selB;
#pragma unroll
    for (int s = 0; s < 6; ++s) { acc[0] = mfma16(selA, pv[s], acc[0]); acc[1] = mfma16(selB, pv[s], acc[1]); }
#pragma unroll
    for (int t = 0; t < 2; ++t) {
      acc[t] = mfma16(selAB, u32x4{hres[t][0], hres[t][1], xres[t][0], xres[t][1]}, acc[t]);
      __builtin_amdgcn_raw_buffer_store_b64(u32x2{pk2(acc[t][0], acc[t][1]), pk2(acc[t][2], acc[t][3])}, rout, ((16 * (4 * i + 2 * tb + t) + l15) * D + 16 * Fo + 4 * g) * 2, 0, 0);
    }
  }
  PROBE(6);
  STAMP(13);
  ST_FLUSH(1, p.ws, b, w); ST_FLUSHQ(p.ws, b, w);
}

extern "C" void kernel_launch(void* const* d_in, const int* in_sizes, int n_in, void* d_out, int out_size, void* d_ws, size_t ws_size, hipStream_t stream) {
  Params p;
  p.x = (const u16*)d_in[0]; p.wqkv = (const u16*)d_in[1]; p.wo = (const u16*)d_in[2];
  p.w1 = (const u16*)d_in[4]; p.w2 = (const u16*)d_in[5]; p.w3 = (const u16*)d_in[6]; p.w4 = (const u16*)d_in[7];
  p.out = (u16*)d_out; p.ws = (char*)d_ws;
  (void)hipFuncSetAttribute((const void*)block_fused, hipFuncAttributeMaxDynamicSharedMemorySize, K2_LDS);
  block_fused<<<256, 640, K2_LDS, stream>>>(p);
}
```

```cpp
#include <hip/hip_runtime.h>
#include <hip/hip_bf16.h>
#include <stdint.h>

typedef unsigned short u16;
typedef __attribute__((ext_vector_type(8))) __bf16 bf16x8;
typedef __attribute__((ext_vector_type(2))) __bf16 bf16x2;
typedef __attribute__((ext_vector_type(4))) short s16x4;
typedef __attribute__((ext_vector_type(4))) float f32x4;
typedef __attribute__((ext_vector_type(16))) float f32x16;
typedef __attribute__((ext_vector_type(4))) unsigned u32x4;
typedef __attribute__((ext_vector_type(2))) unsigned u32x2;
#define LAS __attribute__((address_space(3)))
#define GAS __attribute__((address_space(1)))
#define DI __device__ __forceinline__

constexpr int S = 2048, D = 448, NH = 16, DH = 28, F = 1792;
constexpr unsigned SENT = 0xFFFFFFFFu;

constexpr unsigned KB = 1024;
constexpr unsigned WS_Q   = 0;
constexpr unsigned WS_K   = WS_Q + 2048 * KB;
constexpr unsigned WS_V   = WS_K + 2048 * KB;
constexpr unsigned WS_WO  = WS_V + 2048 * KB;
constexpr unsigned WS_W1  = WS_WO + 448 * KB;
constexpr unsigned WS_W2  = WS_W1 + 392 * KB;
constexpr unsigned WS_O   = WS_W2 + 392 * KB;
constexpr unsigned WS_H   = WS_O + 2048 * KB;
constexpr unsigned WS_X1  = WS_H + 1792 * KB;
constexpr unsigned WS_Z   = WS_X1 + 1792 * KB;
constexpr unsigned WS_P   = WS_Z + 1820 * KB;
constexpr unsigned WS_CS  = WS_P + 32 * 49 * 8 * KB;
constexpr unsigned WS_PF  = WS_CS + 18 * KB;
constexpr unsigned WS_FLG = WS_PF + 224 * KB;
constexpr unsigned WS_EP  = WS_FLG + 32 * KB;
constexpr unsigned WS_END = WS_EP + KB;

struct Params {
  const u16* x; const u16* wqkv; const u16* wo; const u16* w1; const u16* w2; const u16* w3; const u16* w4;
  u16* out; char* ws;
};

DI __amdgpu_buffer_rsrc_t mk_rsrc(const void* p, unsigned bytes) { return __builtin_amdgcn_make_buffer_rsrc((void*)p, (short)0, (int)bytes, 0x00020000); }
DI void dma16(__amdgpu_buffer_rsrc_t rs, unsigned voff, unsigned soff, unsigned ldsaddr) {
  asm volatile("s_mov_b32 m0, %0\n\ts_nop 0\n\tbuffer_load_dwordx4 %1, %2, %3 offen lds" :: "s"(ldsaddr), "v"(voff), "s"(rs), "s"(soff) : "memory");
}
DI void dma16_sc1(__amdgpu_buffer_rsrc_t rs, unsigned voff, unsigned soff, unsigned ldsaddr) {
  asm volatile("s_mov_b32 m0, %0\n\ts_nop 0\n\tbuffer_load_dwordx4 %1, %2, %3 offen sc1 lds" :: "s"(ldsaddr), "v"(voff), "s"(rs), "s"(soff) : "memory");
}
DI void dma4(__amdgpu_buffer_rsrc_t rs, unsigned voff, unsigned soff, unsigned ldsaddr) {
  asm volatile("s_mov_b32 m0, %0\n\ts_nop 0\n\tbuffer_load_dwordx4 %1, %2, %3 offen sc1 lds\n\tbuffer_load_dwordx4 %1, %2, %3 offen offset:1024 sc1 lds\n\t"
               "buffer_load_dwordx4 %1, %2, %3 offen offset:2048 sc1 lds\n\tbuffer_load_dwordx4 %1, %2, %3 offen offset:3072 sc1 lds" :: "s"(ldsaddr), "v"(voff), "s"(rs), "s"(soff) : "memory");
}
DI void dma3(__amdgpu_buffer_rsrc_t rs, unsigned voff, unsigned soff, unsigned ldsaddr) {
  asm volatile("s_mov_b32 m0, %0\n\ts_nop 0\n\tbuffer_load_dwordx4 %1, %2, %3 offen sc1 lds\n\tbuffer_load_dwordx4 %1, %2, %3 offen offset:1024 sc1 lds\n\t"
               "buffer_load_dwordx4 %1, %2, %3 offen offset:2048 sc1 lds" :: "s"(ldsaddr), "v"(voff), "s"(rs), "s"(soff) : "memory");
}
DI void dma4v(__amdgpu_buffer_rsrc_t rs, const unsigned (&v)[4], unsigned soff, unsigned ldsaddr) {
  asm volatile("s_mov_b32 m0, %0\n\ts_nop 0\n\tbuffer_load_dwordx4 %1, %5, %6 offen lds\n\tbuffer_load_dwordx4 %2, %5, %6 offen offset:1024 lds\n\t"
               "buffer_load_dwordx4 %3, %5, %6 offen offset:2048 lds\n\tbuffer_load_dwordx4 %4, %5, %6 offen offset:3072 lds"
               :: "s"(ldsaddr), "v"(v[0]), "v"(v[1]), "v"(v[2]), "v"(v[3]), "s"(rs), "s"(soff) : "memory");
}
#define WAIT_VM(n) asm volatile("s_waitcnt vmcnt(" #n ")" ::: "memory")
#define BAR() asm volatile("s_waitcnt lgkmcnt(0)\n\ts_barrier" ::: "memory")
#define BARS() do { __builtin_amdgcn_sched_barrier(0); BAR(); __builtin_amdgcn_sched_barrier(0); } while (0)
DI unsigned rfl(unsigned v) { return __builtin_amdgcn_readfirstlane(v); }
DI float bflo(unsigned u) { return __uint_as_float(u << 16); }
DI float bfhi(unsigned u) { return __uint_as_float(u & 0xffff0000u); }
DI unsigned pk2(float a, float b) { bf16x2 v; v[0] = (__bf16)a; v[1] = (__bf16)b; return __builtin_bit_cast(unsigned, v); }
DI float dot2(unsigned a, unsigned b, float c) { return __builtin_amdgcn_fdot2_f32_bf16(__builtin_bit_cast(bf16x2, a), __builtin_bit_cast(bf16x2, b), c, false); }
DI f32x16 mfma32(u32x4 a, u32x4 b, f32x16 c) { return __builtin_amdgcn_mfma_f32_32x32x16_bf16(__builtin_bit_cast(bf16x8, a), __builtin_bit_cast(bf16x8, b), c, 0, 0, 0); }
DI f32x4 mfma16(u32x4 a, u32x4 b, f32x4 c) { return __builtin_amdgcn_mfma_f32_16x16x32_bf16(__builtin_bit_cast(bf16x8, a), __builtin_bit_cast(bf16x8, b), c, 0, 0, 0); }
DI u32x2 tr_read(const LAS char* p) { return __builtin_bit_cast(u32x2, __builtin_amdgcn_ds_read_tr16_b64_v4i16((LAS s16x4*)p)); }
DI u32x4 lds128(const LAS char* p) { return *(const LAS u32x4*)p; }
template <int AUX> DI void bst128(u32x4 v, __amdgpu_buffer_rsrc_t rs, unsigned off) { __builtin_amdgcn_raw_buffer_store_b128(v, rs, off, 0, AUX); }
#define XJ j
constexpr unsigned ONES = 0x3F803F80u;
#define AUX_SIDE 16
#define AUX_QKV 16
#define ST_DECL
#define ST_PARAM
#define ST_DECLQ
#define ST_ARGQ
#define ST_FLUSHQ(ws, b, w)
#define CSTAMP(n)
#define STAMP(n)
#define ST_FLUSH(kern, ws, b, w)

DI void aload128(u32x4& d, __amdgpu_buffer_rsrc_t rs, unsigned voff, unsigned soff) { asm volatile("buffer_load_dwordx4 %0, %1, %2, %3 offen" : "=v"(d) : "v"(voff), "s"(rs), "s"(soff) : "memory"); }
DI void aload64(u32x2& d, __amdgpu_buffer_rsrc_t rs, unsigned voff, unsigned soff) { asm volatile("buffer_load_dwordx2 %0, %1, %2, %3 offen" : "=v"(d) : "v"(voff), "s"(rs), "s"(soff) : "memory"); }
DI void aload128_sc1(u32x4& d, __amdgpu_buffer_rsrc_t rs, unsigned voff, unsigned soff) { asm volatile("buffer_load_dwordx4 %0, %1, %2, %3 offen sc1" : "=v"(d) : "v"(voff), "s"(rs), "s"(soff) : "memory"); }
DI void aload64_sc1(u32x2& d, __amdgpu_buffer_rsrc_t rs, unsigned voff, unsigned soff) { asm volatile("buffer_load_dwordx2 %0, %1, %2, %3 offen sc1" : "=v"(d) : "v"(voff), "s"(rs), "s"(soff) : "memory"); }
#define RETIRE(x) asm volatile("; RETIRE %0" : "+v"(x))
constexpr unsigned CODE_BYTES = 50 * 1024;
DI unsigned touch_code(unsigned long long base, unsigned bytes, int b, int lane, bool on) {
  const __amdgpu_buffer_rsrc_t rc = mk_rsrc((const void*)base, bytes);
  const unsigned per = (bytes / 128 + 31) / 32, line = (b >> 3) * per + lane;
  unsigned d;
  asm volatile("buffer_load_dword %0, %1, %2, 0 offen" : "=v"(d) : "v"(on && lane < (int)per ? line * 128 : 0xF0000000u), "s"(rc) : "memory");
  return d;
}
DI unsigned long long code_base() { unsigned long long pc; asm volatile("s_getpc_b64 %0" : "=s"(pc)); return pc & ~0xFFull; }

#define HX0 8
#define HX1 8
#define HX2 12
#define HX3 10
#define HWAIT(x) do { WAIT_VM(0); if ((x) > 0) __builtin_amdgcn_s_sleep(x); } while (0)
#define ROTQ 0
#define XMAP 2
DI void map_block(int b, int& i, int& j) { const int x = b & 7, s = b >> 3; constexpr int JPX = 8 / XMAP; i = (x / XMAP) * 4 * XMAP + s / JPX; j = (x % XMAP) * JPX + s % JPX; }

constexpr unsigned K1_XS = 0;
constexpr unsigned K1_W0 = 7 * 8192;
constexpr unsigned K1_SLOT = 24576;
constexpr unsigned K1_ST = K1_W0 + 4 * K1_SLOT;
constexpr unsigned K1_CS = K1_ST + 512;
constexpr unsigned K1_EP = K1_CS + 2 * 2 * 3 * 32 * 4;

DI void retile_load(const Params& p, int mat, int ks, int cb, int lane, u32x4 (&v)[4]) {
  const u16* W = mat == 0 ? p.wo : mat == 1 ? p.w1 : mat == 2 ? p.w2 : p.w3;
  const unsigned ldw = mat == 3 ? F : D, nrows = D, krow0 = (mat == 0 ? 28 : 32) * ks;
#pragma unroll
  for (int q = 0; q < 4; ++q) {
    unsigned row = krow0 + (lane >> 3) + 8 * q; row = row < nrows ? row : nrows - 1;
    v[q] = *(const u32x4*)(W + (size_t)row * ldw + cb * 64 + (lane & 7) * 8);
  }
}
template <bool SUMS>
DI void retile_store(const Params& p, LAS char* scr, int mat, int ks, int cb, int lane, const u32x4 (&v)[4], f32x4 (&cs)[4]) {
#pragma unroll
  for (int q = 0; q < 4; ++q) *(LAS u32x4*)(scr + q * 1024 + lane * 16) = v[q];
  const int g = lane >> 4, q4 = (lane & 15) >> 2, pp = lane & 3;
  const int r0 = 8 * g + q4, r1 = 8 * g + 4 + q4;
  const __amdgpu_buffer_rsrc_t rws = mk_rsrc(p.ws, WS_END);
  const u32x4 ones = {ONES, ONES, ONES, ONES};
#pragma unroll
  for (int ft = 0; ft < 4; ++ft) {
    u32x2 lo = tr_read(scr + r0 * 128 + ft * 32 + pp * 8), hi = tr_read(scr + r1 * 128 + ft * 32 + pp * 8);
    if (mat == 0 && g == 3) { hi[0] = 0u; hi[1] = 0u; }
    const u32x4 f = {lo[0], lo[1], hi[0], hi[1]};
    const int Fi = 4 * cb + ft;
    unsigned dst;
    if (mat == 0) dst = WS_WO + (((Fi / 7) * 16 + ks) * 7 + Fi % 7) * KB;
    else dst = (mat == 1 ? WS_W1 : WS_W2) + (((Fi / 7) * 14 + ks) * 7 + Fi % 7) * KB;
    if (mat != 3) bst128<AUX_SIDE>(f, rws, lane * 16 + dst);
    if (SUMS) cs[ft] = mfma16(f, ones, cs[ft]);
  }
}

DI int qk_slot(int n0, int& half, bool& last) {
  const int head = n0 / 28, hs = 28 * head, bnd = (hs / 64 + 1) * 64;
  const bool split = bnd < hs + 28, second = split && n0 >= bnd;
  const int seg0 = second ? bnd : hs, seg1 = (split && !second) ? bnd : hs + 28, off = n0 - seg0;
  half = (off >> 2) & 1;
  last = n0 + 4 == seg1 && half == 0;
  return 4 * head + (second ? (bnd - hs + 7) >> 3 : 0) + (off >> 3);
}
DI void fill_early(__amdgpu_buffer_rsrc_t rws, int i, int j, int lw, int lane) {
  const u32x4 sv = {SENT, SENT, SENT, SENT};
  const unsigned l16 = lane * 16, tt0 = 4 * i + 2 * (j >> 2), F0 = 7 * (j & 3);
#pragma unroll
  for (int t = 0; t < 2; ++t) { const unsigned o = WS_O + ((4 * i + 2 * lw + t) * 16 + 2 * j) * KB + l16; bst128<16>(sv, rws, o); bst128<16>(sv, rws, o + KB); }
  bst128<16>(sv, rws, lane < 28 ? WS_Z + ((tt0 + lw + 1) * 14 + (F0 >> 1)) * KB + (F0 & 1) * 512 + (lane >> 1) * 256 + (lw * 14 + (lane & 1)) * 16 : 0xF0000000u);
}
DI void fill_late(__amdgpu_buffer_rsrc_t rws, int i, int j, int lw, int lane) {
  const u32x4 sv = {SENT, SENT, SENT, SENT};
  const unsigned l16 = lane * 16, tt0 = 4 * i + 2 * (j >> 2), F0 = 7 * (j & 3);
  const unsigned o = ((tt0 + lw) * 14 + (F0 >> 1)) * KB + (F0 & 1) * 512 + l16;
#pragma unroll
  for (int u = 0; u < 3; ++u) { const unsigned base = u == 0 ? WS_H : u == 1 ? WS_X1 : WS_Z + 14 * KB;
    bst128<16>(sv, rws, o + base); bst128<16>(sv, rws, o + base + KB); bst128<16>(sv, rws, o + base + 2 * KB); bst128<16>(sv, rws, lane < 32 ? o + base + 3 * KB : 0xF0000000u); }
}

DI void phase_qkv(const Params& p, LAS char* lds, unsigned lds0, unsigned long long cbase, int b, int i, int j, int w, int lane ST_PARAM) {
  STAMP(0);
  unsigned epv = 0, cwarm = 0, twarm = 0;
  auto errands = [&] {
    asm volatile("buffer_load_dword %0, %1, %2, 0 offen sc1" : "=v"(epv) : "v"(WS_EP), "s"(mk_rsrc(p.ws, WS_END)) : "memory");
    cwarm = touch_code(cbase, CODE_BYTES, b, lane, true);
  };
  auto errands_done = [&] { WAIT_VM(0); RETIRE(epv); RETIRE(cwarm); RETIRE(twarm); if (lane == 0) *(LAS unsigned*)(lds + K1_EP) = epv + 1u; };

  if (j == 7) {
    const __amdgpu_buffer_rsrc_t rws = mk_rsrc(p.ws, WS_END);
    if (w >= 8) {
      const int lw = w - 8;
      if (lw == 1) { errands(); errands_done(); }
      fill_early(rws, i, j, lw, lane);
#pragma unroll
      for (int n = 0; n < 4; ++n) { const int pr = (lw * 4 + n) * 4 + (lane >> 4), gq = (lane >> 2) & 3, row = lane & 3; const unsigned o = row == 0 ? ONES : 0u;
        bst128<16>(u32x4{o, o, o, o}, rws, WS_V + ((((pr >> 1) * 64 + 2 * i + (pr & 1)) * 2 + 1) * 64 + gq * 16 + 12 + row) * 16); }
      if (i == 0 || i == 31) {
#pragma unroll
        for (int n = 0; n < 7; ++n) bst128<16>(u32x4{0u, 0u, 0u, 0u}, rws, WS_Z + ((i == 0 ? 0 : 129) * 14 + lw * 7 + n) * KB + lane * 16); }
    } else {
    const int colid = w < 2 ? w * 32 + i : (w == 2 && i < 6) ? 64 + i : -1;
    const int nid = w >= 3 ? i * 5 + (w - 3) : 160 + (i - 6);
    u32x4 v[7][4];
    auto other = [&](int m, int& mat, int& ks, int& cb) { if (m < 112) { mat = 0; ks = m / 7; cb = m % 7; } else { mat = 2; ks = (m - 112) / 7; cb = (m - 112) % 7; } };
    const int ccol = colid >> 1, chalf = colid & 1, cmat = ccol < 7 ? 1 : 3, ccb = ccol < 7 ? ccol : ccol - 7;
    f32x4 cs[4] = {};
    if (colid >= 0) {
#pragma unroll
      for (int q = 0; q < 7; ++q) retile_load(p, cmat, 7 * chalf + q, ccb, lane, v[q]);
#pragma unroll
      for (int q = 0; q < 7; ++q) retile_store<true>(p, lds + (2 * w + (q & 1)) * 4096, cmat, 7 * chalf + q, ccb, lane, v[q], cs);
      if ((lane & 15) == 0) {
#pragma unroll
        for (int ft = 0; ft < 4; ++ft) bst128<16>(__builtin_bit_cast(u32x4, cs[ft]), rws, WS_CS + (chalf * 2240 + (cmat == 1 ? 0 : 448) + 64 * ccb + 16 * ft + 4 * (lane >> 4)) * 4); }
    } else {
#pragma unroll
      for (int q = 0; q < 2; ++q) { const int m = nid + 186 * q; if (m < 210) { int mat, ks, cb; other(m, mat, ks, cb); retile_load(p, mat, ks, cb, lane, v[q]); } }
#pragma unroll
      for (int q = 0; q < 2; ++q) { const int m = nid + 186 * q; if (m < 210) { int mat, ks, cb; other(m, mat, ks, cb); retile_store<false>(p, lds + (2 * w + (q & 1)) * 4096, mat, ks, cb, lane, v[q], cs); } }
    }
    }
    STAMP(7);
    WAIT_VM(0); BARS();
    return;
  }
  if (w == 6 || w == 7) {
    if (w == 7) {
      errands();
#pragma unroll
      for (int c = 0; c < 3; ++c) BARS();
      errands_done();
    } else {
#pragma unroll
      for (int c = 0; c < 3; ++c) BARS();
    }
    fill_early(mk_rsrc(p.ws, WS_END), i, j, w - 6, lane);
#pragma unroll
    for (int c = 3; c < 9; ++c) BARS();
    WAIT_VM(0); BARS();
    return;
  }
  if (w == 4 || w == 5) {
    const int tok = 32 * (w - 4) + (lane & 31), part = lane >> 5;
    const LAS char* xb[4];
#pragma unroll
    for (int q = 0; q < 4; ++q) xb[q] = lds + K1_XS + tok * 128 + ((4 * part + q) ^ ((tok >> 1) & 7)) * 16;
    float s1a = 0.f, s1b = 0.f, s2a = 0.f, s2b = 0.f;
    BARS();
#pragma unroll
    for (int c = 0; c < 7; ++c) {
      u32x4 v[4];
#pragma unroll
      for (int q = 0; q < 4; ++q) v[q] = lds128(xb[q] + c * 8192);
      __builtin_amdgcn_sched_barrier(0);
#pragma unroll
      for (int q = 0; q < 4; ++q)
#pragma unroll
        for (int u = 0; u < 4; ++u) { float lo = bflo(v[q][u]), hi = bfhi(v[q][u]); asm volatile("" : "+v"(lo), "+v"(hi));
          s1a += lo; s1b += hi; s2a = fmaf(lo, lo, s2a); s2b = fmaf(hi, hi, s2b); }
      asm volatile("" : "+v"(s1a), "+v"(s1b), "+v"(s2a), "+v"(s2b));
      if (c == 6) {
        float a1 = s1a + s1b, a2 = s2a + s2b;
        a1 += __shfl_xor(a1, 32); a2 += __shfl_xor(a2, 32);
        const float mean = a1 * (1.f / D), var = fmaxf(a2 * (1.f / D) - mean * mean, 0.f);
        if (lane < 32) { *(LAS float*)(lds + K1_ST + tok * 4) = mean; *(LAS float*)(lds + K1_ST + 256 + tok * 4) = __builtin_amdgcn_rsqf(var + 1e-5f); }
      }
      BARS();
    }
    BARS();
    STAMP(7);
    WAIT_VM(0); BARS();
    return;
  }
  if (w >= 8) {
    const int lw = w - 8;
    const __amdgpu_buffer_rsrc_t rx = mk_rsrc(p.x, S * D * 2), rw = mk_rsrc(p.wqkv, D * 3 * D * 2);
    const unsigned xv0 = (lane >> 3) * 896 + ((lane & 7) ^ (lane >> 4)) * 16, xv1 = (lane >> 3) * 896 + ((lane & 7) ^ (lane >> 4) ^ 4) * 16;
    const unsigned wv = (lane >> 3) * 2688 + (((lane & 7) ^ (((lane >> 4) & 1) * 4)) * 16);
    const int rotq = ROTQ;
    auto issue_chunk = [&](int c) {
      int cc = c + rotq; cc = cc >= 7 ? cc - 7 : cc;
#pragma unroll
      for (int q = 0; q < 4; ++q) { const unsigned n = lw * 4 + q; dma16(rx, (q & 1) ? xv1 : xv0, (64 * i + 8 * n) * 896 + cc * 128, lds0 + K1_XS + c * 8192 + n * 1024); }
#pragma unroll
      for (int q = 0; q < 12; ++q) { const unsigned n = lw * 12 + q, type = n >> 3, rg = n & 7;
        dma16(rw, wv, (64 * cc + 8 * rg) * 2688 + type * 896 + j * 128, lds0 + K1_W0 + (c & 3) * K1_SLOT + type * 8192 + rg * 1024); }
    };
    issue_chunk(0); issue_chunk(1);
    STAMP(1);
    WAIT_VM(16); STAMP(2); BARS();
    issue_chunk(2); issue_chunk(3);
    WAIT_VM(32); STAMP(3); BARS();
    issue_chunk(4);
    WAIT_VM(32); STAMP(8); BARS();
    issue_chunk(5);
    WAIT_VM(32); STAMP(9); BARS();
    issue_chunk(6);
    WAIT_VM(32); STAMP(10); BARS();
    WAIT_VM(16); STAMP(11); BARS();
    WAIT_VM(0); STAMP(5); BARS();
    BAR();
    BARS();
    STAMP(7);
    WAIT_VM(0); BARS();
    return;
  }
  const int tb = w & 1, e = w >> 1, r = lane & 31, h = lane >> 5;
  f32x16 aq = {}, ak = {}, av = {};
  f32x4 cq = {}, ck = {}, cv = {};
  const unsigned cs1 = ((lane & 15) == ((lane >> 4) & 1)) ? ONES : 0u;
  const u32x4 csel = {cs1, cs1, cs1, cs1};
  const int q4 = (lane & 15) >> 2, pp = lane & 3, blk = (lane >> 4) & 1;
  const LAS char* xb[4];
#pragma unroll
  for (int kl = 0; kl < 4; ++kl) xb[kl] = lds + K1_XS + (32 * tb + r) * 128 + ((2 * (kl ^ tb) + h) ^ ((r >> 1) & 7)) * 16;
  const unsigned wb = K1_W0 + (8 * h + q4) * 128 + (((8 * e + 4 * blk + pp) ^ (8 * ((q4 >> 1) & 1))) * 8);
  const LAS char* we = lds + wb + tb * 2048;
  const LAS char* wo = lds + wb + (1 - tb) * 2048;
  u32x4 xf[3], f[3][3];
  auto load_step = [&](int n) {
    xf[n % 3] = lds128(xb[n & 3] + (n >> 2) * 8192);
    const LAS char* wp = ((n & 1) ? wo : we) + ((n >> 2) & 3) * K1_SLOT + (n & 2) * 2048;
#pragma unroll
    for (int t = 0; t < 3; ++t) { const u32x2 lo = tr_read(wp + t * 8192), hi = tr_read(wp + t * 8192 + 512); f[n % 3][t] = u32x4{lo[0], lo[1], hi[0], hi[1]}; }
  };
  const __amdgpu_buffer_rsrc_t rws = mk_rsrc(p.ws, WS_END);
  constexpr unsigned K1_IMG = K1_W0 + 3 * K1_SLOT;
  int g0half; bool g0last; const int G0 = qk_slot(64 * j, g0half, g0last);
  unsigned qdst[4], zdst[4], voff;
  {
#pragma unroll
    for (int g4 = 0; g4 < 4; ++g4) { int half; bool last; const int G = qk_slot(64 * j + 32 * e + 8 * g4 + 4 * h, half, last);
      qdst[g4] = K1_IMG + (G - G0) * KB + ((2 * tb + (r >> 4)) * 16 + (r & 15)) * 16 + half * 8;
      zdst[g4] = last ? qdst[g4] + 8 : K1_IMG + 2 * 10 * KB + lane * 8;
      asm volatile("" : "+v"(qdst[g4]), "+v"(zdst[g4])); }
    const unsigned n = 64 * j + 32 * e + r, head = n / 28, d = n - 28 * head;
    voff = WS_V + (((head * 64 + 2 * i + tb) * 2 + (d >> 4)) * 64 + h * 16 + (d & 15)) * 16; asm volatile("" : "+v"(voff)); }
  STAMP(1);
  BARS();
  STAMP(2);
  load_step(0);
#pragma unroll
  for (int n = 0; n < 28; ++n) {
    if ((n & 3) == 3 && n < 27) { if (n == 7) STAMP(8); if (n == 11) STAMP(10); BARS(); if (n == 7) { STAMP(9); CSTAMP(13); } if (n == 11) { STAMP(11); CSTAMP(14); } if (n == 15) STAMP(12); }
    if (n < 27) load_step(n + 1);
    aq = mfma32(f[n % 3][0], xf[n % 3], aq);
    ak = mfma32(f[n % 3][1], xf[n % 3], ak);
    av = mfma32(xf[n % 3], f[n % 3][2], av);
    if (!(n & 1)) {
      cq = mfma16(f[n % 3][0], csel, cq); ck = mfma16(f[n % 3][1], csel, ck); cv = mfma16(f[n % 3][2], csel, cv);
    }
    __builtin_amdgcn_sched_group_barrier(0x8, 1, 0); __builtin_amdgcn_sched_group_barrier(0x100, 3, 0);
    __builtin_amdgcn_sched_group_barrier(0x8, 1, 0); __builtin_amdgcn_sched_group_barrier(0x100, 2, 0);
    __builtin_amdgcn_sched_group_barrier(0x8, 1, 0); __builtin_amdgcn_sched_group_barrier(0x100, 2, 0);
    __builtin_amdgcn_sched_group_barrier(0x8, 3, 0);
    __builtin_amdgcn_sched_barrier(0);
  }
  STAMP(4);
  LAS float* cs = (LAS float*)(lds + K1_CS) + e * 192;
  if ((lane & 15) < 2) { LAS float* o = cs + tb * 96 + 16 * (lane & 1) + 4 * (lane >> 4);
    *(LAS f32x4*)o = cq; *(LAS f32x4*)(o + 32) = ck; *(LAS f32x4*)(o + 64) = cv; }
  BAR();
  STAMP(5);
  const LAS float* st = (const LAS float*)(lds + K1_ST);
  f32x4 cqk[2][4][2], m4[4], r4[4];
#pragma unroll
  for (int t = 0; t < 2; ++t)
#pragma unroll
    for (int g4 = 0; g4 < 4; ++g4) { cqk[t][g4][0] = *(const LAS f32x4*)(cs + t * 32 + 8 * g4 + 4 * h); cqk[t][g4][1] = *(const LAS f32x4*)(cs + 96 + t * 32 + 8 * g4 + 4 * h); }
  const float mean = st[32 * tb + r], rstd = st[64 + 32 * tb + r];
  const float cs2 = cs[64 + r] + cs[96 + 64 + r];
#pragma unroll
  for (int g4 = 0; g4 < 4; ++g4) { m4[g4] = *(const LAS f32x4*)(st + 32 * tb + 8 * g4 + 4 * h); r4[g4] = *(const LAS f32x4*)(st + 64 + 32 * tb + 8 * g4 + 4 * h); }
  __builtin_amdgcn_sched_barrier(0);
#pragma unroll
  for (int g4 = 0; g4 < 4; ++g4)
#pragma unroll
    for (int t = 0; t < 2; ++t) {
      const f32x16& a = t ? ak : aq;
      float v[4];
#pragma unroll
      for (int q = 0; q < 4; ++q) v[q] = rstd * (a[4 * g4 + q] - mean * (cqk[t][g4][0][q] + cqk[t][g4][1][q]));
      *(LAS u32x2*)(lds + qdst[g4] + t * 10 * KB) = u32x2{pk2(v[0], v[1]), pk2(v[2], v[3])};
      *(LAS u32x2*)(lds + zdst[g4] + (zdst[g4] < K1_IMG + 2 * 10 * KB ? t * 10 * KB : 0)) = u32x2{0u, 0u};
    }
  {
    float v[16];
#pragma unroll
    for (int g4 = 0; g4 < 4; ++g4)
#pragma unroll
      for (int q = 0; q < 4; ++q) v[4 * g4 + q] = r4[g4][q] * (av[4 * g4 + q] - m4[g4][q] * cs2);
    const u32x4 oa = {pk2(v[0], v[1]), pk2(v[2], v[3]), pk2(v[8], v[9]), pk2(v[10], v[11])};
    const u32x4 ob = {pk2(v[4], v[5]), pk2(v[6], v[7]), pk2(v[12], v[13]), pk2(v[14], v[15])};
    bst128<AUX_QKV>(oa, rws, voff);
    bst128<AUX_QKV>(ob, rws, voff + 512);
  }
  BARS();
  { int lh; bool ll; const int NS = qk_slot(64 * j + 60, lh, ll) - G0 + 1;
#pragma unroll
    for (int q = 0; q < 3; ++q) { const int ls = w + 4 * q;
      if (ls < NS) { const int G = G0 + ls; const unsigned o = WS_Q + (((G >> 2) * 128 + 4 * i + (lane >> 4)) * 64 + (G & 3) * 16 + (lane & 15)) * 16;
        bst128<AUX_QKV>(lds128(lds + K1_IMG + ls * KB + lane * 16), rws, o);
        bst128<AUX_QKV>(lds128(lds + K1_IMG + (10 + ls) * KB + lane * 16), rws, o + (WS_K - WS_Q)); } }
  }
  STAMP(6);
  WAIT_VM(0);
  STAMP(7);
  WAIT_VM(0); BARS();
}

constexpr unsigned L_K = 0, L_WOA = 40 * KB, L_V = 120 * KB, L_WOB = 0;
constexpr unsigned L_O = 128 * KB;
constexpr unsigned L_W = 0;
constexpr unsigned L_H = 98 * KB, L_ST4 = 126 * KB;
constexpr unsigned L_Z = 128 * KB;
constexpr unsigned L_X1 = 96 * KB, L_ST6 = 152 * KB;
constexpr unsigned L_HID = 96 * KB;
constexpr unsigned L_RA = 0, L_RB = 32 * KB, L_RC = 64 * KB, L_RD = 128 * KB;
constexpr unsigned K2_LDS = 163840;
constexpr unsigned SPIN_MAX = 1u << 16;

DI u32x4 bload(__amdgpu_buffer_rsrc_t rs, unsigned voff, unsigned soff) { return __builtin_amdgcn_raw_buffer_load_b128(rs, voff, soff, 0); }
DI u32x4 bload_sc1(__amdgpu_buffer_rsrc_t rs, unsigned voff, unsigned soff) { return __builtin_amdgcn_raw_buffer_load_b128(rs, voff, soff, 16); }
DI void bstore_sc1(u32x4 v, __amdgpu_buffer_rsrc_t rs, unsigned voff, unsigned soff) { bst128<16>(v, rs, voff + soff); }
DI unsigned umax4(u32x4 v) { return max(max(v[0], v[1]), max(v[2], v[3])); }
#define FENCE() asm volatile("" ::: "memory")
typedef __attribute__((ext_vector_type(2))) float f32x2_;
DI float gelu_tanh(float x) {
  const float e = __builtin_amdgcn_exp2f(x * fmaf(x * x, -0.10294324f, -2.3022082f));
  return x * __builtin_amdgcn_rcpf(1.f + e);
}
DI u32x4 pair_tiles(u32x2 a, u32x2 b) {
  const auto s0 = __builtin_amdgcn_permlane16_swap(a[0], b[0], false, false), s1 = __builtin_amdgcn_permlane16_swap(a[1], b[1], false, false);
  return u32x4{s0[0], s1[0], s0[1], s1[1]};
}
template <int NT, int NS, class F>
DI void fetch_payload(u32x4 (&v)[NT][2], __amdgpu_buffer_rsrc_t rs, unsigned voff0, unsigned voff1, unsigned soff, unsigned tstride, LAS char* lds, unsigned lds0, unsigned dst, int lane, F fill, void* dbg = nullptr) {
  for (unsigned spin = 0;; ++spin) {
#pragma unroll
    for (int t = 0; t < NT; ++t) { dma16_sc1(rs, voff0, soff + t * tstride, lds0 + dst + t * NS * KB); dma16_sc1(rs, voff1, soff + t * tstride, lds0 + dst + (t * NS + 1) * KB); }
    if (spin == 0) { fill(); __builtin_amdgcn_sched_barrier(0); }
    WAIT_VM(0);
    unsigned m = 0;
#pragma unroll
    for (int t = 0; t < NT; ++t)
#pragma unroll
      for (int q = 0; q < 2; ++q) { v[t][q] = lds128(lds + dst + (t * NS + q) * KB + lane * 16); m = max(m, max(max(v[t][q][0], v[t][q][1]), max(v[t][q][2], v[t][q][3]))); }
    if (!__any(m == SENT) || spin > SPIN_MAX) break;
  }
}
template <int NT> DI void row_stats(const u32x4 (&v)[NT][2], u32x4 ones, LAS float* stp, int w, int l15, int g) {
#pragma unroll
  for (int t = 0; t < NT; ++t) {
    f32x4 d1 = {}, d2 = {};
#pragma unroll
    for (int q = 0; q < 2; ++q) { d1 = __builtin_amdgcn_mfma_f32_16x16x32_bf16(__builtin_bit_cast(bf16x8, ones), __builtin_bit_cast(bf16x8, v[t][q]), d1, 0, 0, 0);
                                  d2 = __builtin_amdgcn_mfma_f32_16x16x32_bf16(__builtin_bit_cast(bf16x8, v[t][q]), __builtin_bit_cast(bf16x8, v[t][q]), d2, 0, 0, 0); }
    const float lo = (l15 & 1) ? d2[1] : d2[0], hi = (l15 & 1) ? d2[3] : d2[2], s2 = (l15 & 2) ? hi : lo;
    if (g == (l15 >> 2)) *(LAS f32x2_*)(stp + ((t * 16 + l15) * 8 + w) * 2) = f32x2_{d1[0], s2};
  }
}

#define SB() __builtin_amdgcn_sched_barrier(0)
typedef __attribute__((ext_vector_type(2))) float f32x2;
DI float max3(float a, float b, float c) { float r; asm("v_max3_f32 %0, %1, %2, %3" : "=v"(r) : "v"(a), "v"(b), "v"(c)); return r; }
DI float gsum(float v) {
  const auto a = __builtin_amdgcn_permlane16_swap(__float_as_uint(v), __float_as_uint(v), false, false); const float s = __uint_as_float(a[0]) + __uint_as_float(a[1]);
  const auto c = __builtin_amdgcn_permlane32_swap(__float_as_uint(s), __float_as_uint(s), false, false); return __uint_as_float(c[0]) + __uint_as_float(c[1]);
}
DI float gmax(float v) {
  const auto a = __builtin_amdgcn_permlane16_swap(__float_as_uint(v), __float_as_uint(v), false, false); const float s = max3(__uint_as_float(a[0]), __uint_as_float(a[1]), __uint_as_float(a[1]));
  const auto c = __builtin_amdgcn_permlane32_swap(__float_as_uint(s), __float_as_uint(s), false, false); return max3(__uint_as_float(c[0]), __uint_as_float(c[1]), __uint_as_float(c[1]));
}
DI unsigned gelu2(f32x2 x) {
  const f32x2 t = x * (x * x * f32x2{-0.10294324f, -0.10294324f} + f32x2{-2.3022082f, -2.3022082f});
  const f32x2 d = f32x2{__builtin_amdgcn_exp2f(t[0]), __builtin_amdgcn_exp2f(t[1])} + f32x2{1.f, 1.f};
  const f32x2 r = x * f32x2{__builtin_amdgcn_rcpf(d[0]), __builtin_amdgcn_rcpf(d[1])};
  return pk2(r[0], r[1]);
}
DI unsigned gelu2s(f32x2 a, float c1, float c2, float is) {
  const f32x2 t = a * (a * a * f32x2{c2, c2} + f32x2{c1, c1});
  const f32x2 d = f32x2{__builtin_amdgcn_exp2f(t[0]), __builtin_amdgcn_exp2f(t[1])} * f32x2{is, is} + f32x2{is, is};
  const f32x2 r = a * f32x2{__builtin_amdgcn_rcpf(d[0]), __builtin_amdgcn_rcpf(d[1])};
  return pk2(r[0], r[1]);
}
template <int N, int PD, class L, class U, class M> DI void lds_pipe(L load, U use, M mid) {
  u32x4 buf[PD + 1];
#pragma unroll
  for (int n = 0; n < PD; ++n) buf[n] = load(n);
  SB();
#pragma unroll
  for (int n = 0; n < N; ++n) {
    if (n + PD < N) buf[(n + PD) % (PD + 1)] = load(n + PD);
    SB();
    use(n, buf[n % (PD + 1)]);
    mid(n);
    SB();
  }
}

DI void wait_flags(const Params& p, bool on, unsigned off, unsigned epoch) {
  const GAS unsigned* f = (const GAS unsigned*)(p.ws + (on ? off : WS_EP));
  for (unsigned spin = 0; spin < SPIN_MAX; ++spin) {
    const unsigned v = __hip_atomic_load(f, __ATOMIC_RELAXED, __HIP_MEMORY_SCOPE_AGENT);
    if (__all(!on || v == epoch)) break;
    __builtin_amdgcn_s_sleep(2);
  }
}

__global__ void __launch_bounds__(640, 3) block_fused(Params p) {
  extern __shared__ __attribute__((aligned(16))) char smem[];
  LAS char* lds = (LAS char*)smem;
  const unsigned lds0 = (unsigned)(size_t)lds;
  const int b = blockIdx.x, tid = threadIdx.x, lane = tid & 63;
  const int w = rfl(tid >> 6);
#define PROBE(n)
  PROBE(0);
  const unsigned long long cbase = code_base();
  int i, j; map_block(b, i, j);
  const __amdgpu_buffer_rsrc_t rws = mk_rsrc(p.ws, WS_END);
  ST_DECLQ;
  phase_qkv(p, lds, lds0, cbase, b, i, j, w, lane ST_ARGQ);
  PROBE(1);
  const unsigned epoch = rfl(*(const LAS unsigned*)(lds + K1_EP));
  ST_DECL; STAMP(0);
  const int l15 = lane & 15, g = lane >> 4;
  const unsigned l16 = lane * 16;
  constexpr unsigned OOB = 0xF0000000u;
  const int t32 = 2 * i + (j >> 2), c4 = j & 3, tt0 = 2 * t32;
  const int sid = XMAP == 1 ? (((i & 3) << 1) | (j >> 2)) : XMAP == 2 ? (i & 7) : ((i & 15) >> 1);
  const int rot14 = (sid * 7) >> 2, rot16 = 2 * sid, rotc = XMAP == 1 ? ((i & 3) * 3) >> 1 : (i & 7) % 6;
  auto pcol = [&](int s) { int u = s + rotc; u = u >= 6 ? u - 6 : u; int c = j + 1 + u; return c >= 7 ? c - 7 : c; };
  const u32x4 sentv = {SENT, SENT, SENT, SENT};

  if (w >= 8) {
    const int lw = w - 8;
    if (lw == 0 && lane == 0) __hip_atomic_store((GAS unsigned*)(p.ws + WS_FLG + (i * 8 + j) * 128), epoch, __ATOMIC_RELAXED, __HIP_MEMORY_SCOPE_AGENT);
    { const int t = i - 4 + (lane >> 3);
      if (lw == 0) wait_flags(p, lane < 48 && t >= 0 && t < 32, WS_FLG + (t * 8 + (lane & 7)) * 128, epoch);
      else wait_flags(p, lane < 32, WS_FLG + (lane * 8 + 7) * 128, epoch); }
    STAMP(6);
    BARS();
#pragma unroll
    for (int q = 0; q < 5; ++q) { const int n = 2 * q + lw, hh = n / 5, kt = 4 * i - 16 + 4 * (n % 5);
      dma4(rws, l16, WS_K + ((2 * j + hh) * 128 + (kt < 0 ? 0 : kt)) * KB, lds0 + L_K + n * 4 * KB); }
#pragma unroll
    for (int q = 0; q < 5; ++q) { const int n = 2 * q + lw, hh = n / 5, kb = 2 * i - 8 + 2 * (n % 5);
      dma4(rws, l16, WS_V + ((2 * j + hh) * 64 + (kb < 0 ? 0 : kb)) * 2 * KB, lds0 + L_V + n * 4 * KB); }
    STAMP(1); WAIT_VM(20); STAMP(2); BARS();
    auto wo_slot = [&](int q) { const int n = 2 * q + lw; const unsigned src = WS_WO + (c4 * 16 + ((n + rot16) & 15)) * 7 * KB, dst = lds0 + (n <= 10 ? L_WOA + n * 7 * KB : L_WOB + (n - 11) * 7 * KB);
      dma4(rws, l16, src, dst); dma3(rws, l16, src + 4 * KB, dst + 4 * KB); };
    wo_slot(0); wo_slot(1);
    STAMP(3); WAIT_VM(14); STAMP(4); BARS(); STAMP(5);
    fill_late(rws, i, j, lw, lane);
    FENCE();
#pragma unroll
    for (int q = 2; q < 8; ++q) wo_slot(q);
    WAIT_VM(0); STAMP(7); BARS(); STAMP(8);
    auto w_stage = [&](unsigned wsrc, int q0, int q1) {
#pragma unroll
      for (int q = q0; q < q1; ++q) { const int n = 2 * q + lw; int ks = n + rot14; ks = ks >= 14 ? ks - 14 : ks; const unsigned src = wsrc + (c4 * 14 + ks) * 7 * KB, dst = lds0 + L_W + n * 7 * KB;
        dma4(rws, l16, src, dst); dma3(rws, l16, src + 4 * KB, dst + 4 * KB); } };
    BARS(); STAMP(9);
    w_stage(WS_W1, 0, 2);
    BARS();
    w_stage(WS_W1, 2, 7);
    STAMP(10); WAIT_VM(0); STAMP(11); BARS(); STAMP(12);
    BARS(); STAMP(13);
    w_stage(WS_W2, 0, 2);
    BARS();
    w_stage(WS_W2, 2, 7);
    STAMP(14); WAIT_VM(0); STAMP(15); BARS(); STAMP(16);
    BARS(); STAMP(17);
    if (j == 7) { BARS(); BARS(); ST_FLUSH(1, p.ws, b, w); ST_FLUSHQ(p.ws, b, w); return; }
    const __amdgpu_buffer_rsrc_t rw3 = mk_rsrc(p.w3, D * F * 2), rw4 = mk_rsrc(p.w4, F * D * 2);
    unsigned v3[2][4], v4[4];
#pragma unroll
    for (int k = 0; k < 4; ++k) {
#pragma unroll
      for (int e = 0; e < 2; ++e) { const int rr = lane >> 5, f = (2 * (k & 1) + rr) | (e << 2); v3[e][k] = rr * 3584 + (((lane & 31) ^ (f << 1)) * 16) + k * 6144; }
      const int rr = lane >> 3; v4[k] = rr * 896 + (((lane & 7) ^ (((rr >> 1) & 3) << 1)) * 16) + k * 6144;
    }
    auto w3_round = [&](int r, unsigned slot) { int ks = 2 * r + lw + rot14; ks = ks >= 14 ? ks - 14 : ks;
#pragma unroll
      for (int u = 0; u < 4; ++u) dma4v(rw3, v3[u & 1], (ks * 32 + 8 * u) * 3584 + j * 512, lds0 + slot + (lw * 16 + u * 4) * KB); };
    auto w4_step = [&](int s, unsigned slot) { const int c = s < 6 ? pcol(s) : j;
#pragma unroll
      for (int u = 0; u < 4; ++u) dma4v(rw4, v4, (256 * j + 128 * lw + 32 * u) * 896 + c * 128, lds0 + slot + (lw * 16 + u * 4) * KB); };
    auto arrive = [&](int s) { if (lw == 0 && lane == 0) __hip_atomic_store((GAS unsigned*)(p.ws + WS_PF + ((i * 7 + pcol(s)) * 8 + j) * 128), epoch, __ATOMIC_RELAXED, __HIP_MEMORY_SCOPE_AGENT); };
    w3_round(0, L_RA);
    BARS();
    w3_round(1, L_RB); w3_round(2, L_RC);
    BARS();
    STAMP(18); WAIT_VM(0); STAMP(19); BARS();
    BARS(); STAMP(20);
    BARS();
    w3_round(3, L_RA); w3_round(4, L_RB);
    BARS();
    BARS();
    WAIT_VM(16); BARS(); w3_round(5, L_RC);
    WAIT_VM(16); BARS(); w3_round(6, L_RA);
    WAIT_VM(16); BARS(); w4_step(0, L_RB);
    WAIT_VM(16); BARS(); w4_step(1, L_RC);
    BARS(); w4_step(2, L_RA); w4_step(3, L_RD);
    STAMP(21); WAIT_VM(16); STAMP(22); BARS();
    BARS(); w4_step(4, L_RB);
    BARS(); w4_step(5, L_RC);
    WAIT_VM(32); BARS(); w4_step(6, L_RA);
    WAIT_VM(32); BARS(); arrive(0);
    WAIT_VM(16); BARS(); arrive(1);
    WAIT_VM(0); BARS(); arrive(2);
    BARS(); arrive(3); arrive(4); arrive(5);
    if (b == 0 && lw == 0) {
#pragma unroll
      for (int q = 0; q < 4; ++q) wait_flags(p, true, WS_FLG + (q * 64 + lane) * 128, epoch);
      if (lane == 0) __hip_atomic_store((GAS unsigned*)(p.ws + WS_EP), epoch, __ATOMIC_RELAXED, __HIP_MEMORY_SCOPE_AGENT);
    }
    STAMP(23); ST_FLUSH(1, p.ws, b, w); ST_FLUSHQ(p.ws, b, w);
    return;
  }

  const int hl = w >> 2, a = w & 3, head = 2 * j + hl, tt = 4 * i + a;
  const bool act = w < 7;
  const int wf = act ? w : 6, Fg = 7 * c4 + wf;
  u32x4 qf; u32x2 xr[2]; u32x4 cs1u[2], cs3u[2][2];
  const u32x4 ones = {ONES, ONES, ONES, ONES};
  unsigned act_off = act ? (((tt0 + (g & 1)) * 14 + (Fg >> 1)) * 64 + (2 * (Fg & 1) + (g >> 1)) * 16 + l15) * 16 : OOB;
  const int ka = (2 * w + rot14) % 14, kb14 = (2 * w + 1 + rot14) % 14;
  unsigned pv0 = l16 + ka * KB, pv1 = l16 + kb14 * KB, ov0 = l16 + ((2 * w + rot16) & 15) * KB, ov1 = l16 + ((2 * w + 1 + rot16) & 15) * KB;
  unsigned zv[2];
#pragma unroll
  for (int q = 0; q < 2; ++q) { const int ks = q ? kb14 : ka, f0 = 32 * ks + 8 * g, sh = f0 < 112 ? -1 : f0 < 224 ? 1 : f0 < 336 ? -2 : 2, tz = 16 * (tt0 + 1) + l15 + sh;
    zv[q] = (((tz >> 4) * 14 + ks) * 64 + g * 16 + (tz & 15)) * 16; }
  u32x4 selA, selB;
  { const unsigned v = g == (l15 >> 2) ? ((l15 & 1) ? 0x3F800000u : 0x00003F80u) : 0u, d0 = (l15 & 2) ? 0u : v, d1 = (l15 & 2) ? v : 0u;
    selA = u32x4{d0, d1, 0u, 0u}; selB = u32x4{0u, 0u, d0, d1}; }
  unsigned ooff = ((tt * 16 + head) * 64 + ((g >> 1) + 2 * (g & 1)) * 16 + l15) * 16 + WS_O;
  asm volatile("" : "+v"(act_off), "+v"(pv0), "+v"(pv1), "+v"(ov0), "+v"(ov1), "+v"(zv[0]), "+v"(zv[1]), "+v"(selA), "+v"(selB), "+v"(ooff));
  { const __amdgpu_buffer_rsrc_t rx = mk_rsrc(p.x, S * D * 2);
#pragma unroll
    for (int t = 0; t < 2; ++t) aload64(xr[t], rx, ((16 * (tt0 + t) + l15) * D + 16 * Fg + 4 * g) * 2, 0); }
  BARS();
  aload128_sc1(qf, rws, l16, WS_Q + (head * 128 + tt) * KB);
#pragma unroll
  for (int hf = 0; hf < 2; ++hf) { aload128_sc1(cs1u[hf], rws, (16 * Fg + 4 * g) * 4, WS_CS + hf * 2240 * 4);
#pragma unroll
    for (int f = 0; f < 2; ++f) aload128_sc1(cs3u[hf][f], rws, (448 + 256 * (j < 7 ? j : 6) + 32 * w + 16 * f + 4 * g) * 4, WS_CS + hf * 2240 * 4); }
  WAIT_VM(0); RETIRE(qf); RETIRE(xr[0]); RETIRE(xr[1]); RETIRE(cs1u[0]); RETIRE(cs1u[1]); RETIRE(cs3u[0][0]); RETIRE(cs3u[0][1]); RETIRE(cs3u[1][0]); RETIRE(cs3u[1][1]);
  const f32x4 cs1 = __builtin_bit_cast(f32x4, cs1u[0]) + __builtin_bit_cast(f32x4, cs1u[1]);
  f32x4 cs3[2];
#pragma unroll
  for (int f = 0; f < 2; ++f) cs3[f] = __builtin_bit_cast(f32x4, cs3u[0][f]) + __builtin_bit_cast(f32x4, cs3u[1][f]);
  STAMP(1);
  BARS();
  PROBE(2);

  u32x4 opiece;
  {
    const int par = a & 1, kl0 = hl * 20 + (a & ~1), vl0 = hl * 20 + (a >> 1) * 2;
    f32x4 sc[18];
    const LAS char* kp = lds + L_K + kl0 * KB + l16;
    const f32x4 zero4 = {0.f, 0.f, 0.f, 0.f};
    lds_pipe<18, 8>([&](int n) { return lds128(kp + n * KB); }, [&](int n, u32x4 kf) { sc[n] = mfma16(kf, qf, zero4); }, [](int) {});
    const int dq = l15 - 4 * g;
    const float NEG = -__builtin_inff();
    if (par == 0) {
#pragma unroll
      for (int r = 0; r < 4; ++r) { const bool ge = dq >= r; sc[0][r] = ge ? NEG : sc[0][r]; sc[16][r] = ge ? sc[16][r] : NEG; sc[17][r] = NEG; }
    } else {
#pragma unroll
      for (int r = 0; r < 4; ++r) { const bool ge = dq >= r; sc[1][r] = ge ? NEG : sc[1][r]; sc[17][r] = ge ? sc[17][r] : NEG; sc[0][r] = NEG; }
    }
    const int nneg = 16 - (tt & ~1);
    if (nneg > 0) {
#pragma unroll
      for (int n = 0; n < 16; ++n) if (n < nneg) sc[n] = f32x4{NEG, NEG, NEG, NEG};
    }
    float m = max3(sc[0][0], sc[0][1], sc[0][2]), m2 = max3(sc[0][3], sc[1][0], sc[1][1]);
    m = max3(m, sc[1][2], sc[1][3]);
#pragma unroll
    for (int n = 2; n < 18; ++n) { m = max3(m, sc[n][0], sc[n][1]); m2 = max3(m2, sc[n][2], sc[n][3]); }
    m = gmax(max3(m, m2, m2));
    const float cexp = 0.18898223650461363f * 1.4426950408889634f, mc = -m * cexp;
    STAMP(14);
    BARS();
    STAMP(15);
    f32x4 o0 = zero4, o1 = zero4;
    const LAS char* vp = lds + L_V + vl0 * KB + l16;
    u32x4 v0 = lds128(vp), v1 = lds128(vp + KB);
#pragma unroll
    for (int mm = 0; mm < 9; ++mm) {
      u32x4 n0, n1;
      if (mm < 8) { n0 = lds128(vp + (2 * mm + 2) * KB); n1 = lds128(vp + (2 * mm + 3) * KB); }
      u32x4 pf;
#pragma unroll
      for (int q = 0; q < 4; ++q) { const f32x4& sv = sc[2 * mm + (q >> 1)];
        pf[q] = pk2(__builtin_amdgcn_exp2f(fmaf(sv[2 * (q & 1)], cexp, mc)), __builtin_amdgcn_exp2f(fmaf(sv[2 * (q & 1) + 1], cexp, mc))); }
      o0 = mfma16(v0, pf, o0);
      o1 = mfma16(v1, pf, o1);
      if (mm < 8) { v0 = n0; v1 = n1; }
      SB();
    }
    const float rl = __builtin_amdgcn_rcpf(gsum(g == 3 ? o1[0] : 0.f));
    opiece = pair_tiles(u32x2{pk2(o0[0] * rl, o0[1] * rl), pk2(o0[2] * rl, o0[3] * rl)}, u32x2{pk2(o1[0] * rl, o1[1] * rl), pk2(o1[2] * rl, o1[3] * rl)});
  }
  STAMP(16);
  BARS();
  bst128<16>(opiece, rws, ooff);
  u32x4 wr[16];
#pragma unroll
  for (int n = 0; n < 16; ++n) wr[n] = lds128(lds + (n <= 10 ? L_WOA + n * 7 * KB : L_WOB + (n - 11) * 7 * KB) + wf * KB + l16);
  STAMP(2);
  HWAIT(HX0);

  { u32x4 v[2][2];
    fetch_payload<2, 16>(v, rws, ov0, ov1, WS_O + tt0 * 16 * KB, 16 * KB, lds, lds0, L_O + 2 * w * KB, lane, [] { BARS();   }, p.ws + (40u << 20)); }
  STAMP(3);
  BARS();
  PROBE(3);
  u32x2 hp[2];
  {
    f32x4 acc[2];
#pragma unroll
    for (int t = 0; t < 2; ++t) acc[t] = mfma16(selA, u32x4{xr[t][0], xr[t][1], 0u, 0u}, f32x4{0.f, 0.f, 0.f, 0.f});
    const LAS char* op = lds + L_O + l16;
    lds_pipe<32, 8>([&](int n) { return lds128(op + ((n & 1) * 16 + (n >> 1)) * KB); }, [&](int n, u32x4 f) { acc[n & 1] = mfma16(wr[n >> 1], f, acc[n & 1]); }, [](int) {});
#pragma unroll
    for (int t = 0; t < 2; ++t) hp[t] = u32x2{pk2(acc[t][0], acc[t][1]), pk2(acc[t][2], acc[t][3])};
    bst128<16>(pair_tiles(hp[0], hp[1]), rws, act_off + WS_H);
  }
  STAMP(4);
  HWAIT(HX1);

  LAS float* st4 = (LAS float*)(lds + L_ST4);
  auto stage_weights = [&] { BARS();
#pragma unroll
    for (int n = 0; n < 14; ++n) wr[n] = lds128(lds + L_W + (n * 7 + wf) * KB + l16);
    BARS(); };
  { u32x4 v[2][2] = {};
    if (act) fetch_payload<2, 14>(v, rws, pv0, pv1, WS_H + tt0 * 14 * KB, 14 * KB, lds, lds0, L_H + 2 * w * KB, lane, stage_weights, p.ws + (40u << 20) + 4); else stage_weights();
    row_stats<2>(v, ones, st4, w, l15, g); }
  STAMP(5);
  BARS();
  f32x2 yv[2][2];
  {
    const LAS char* hpz = lds + L_H + l16;
    f32x4 pt[2];
#pragma unroll
    for (int t = 0; t < 2; ++t) pt[t] = *(const LAS f32x4*)(st4 + ((t * 16 + l15) * 8 + 2 * g) * 2);
    f32x4 acc[2] = {};
    lds_pipe<28, 8>([&](int n) { return lds128(hpz + ((n & 1) * 14 + (n >> 1)) * KB); }, [&](int n, u32x4 f) { acc[n & 1] = mfma16(wr[n >> 1], f, acc[n & 1]); }, [](int) {});
    u32x2 zp[2];
#pragma unroll
    for (int t = 0; t < 2; ++t) {
      const float s1 = gsum(pt[t][0] + pt[t][2]), s2 = gsum(pt[t][1] + pt[t][3]);
      const float mean = s1 * (1.f / D), var = fmaxf(s2 * (1.f / D) - mean * mean, 0.f);
      const float r1 = __builtin_amdgcn_rsqf(var + 1e-5f);
      const float r12 = r1 * __builtin_amdgcn_rsqf(var * r1 * r1 + 1e-5f);
      const f32x2 m2 = {mean, mean};
#pragma unroll
      for (int q = 0; q < 2; ++q) {
        yv[t][q] = (f32x2{bflo(hp[t][q]), bfhi(hp[t][q])} - m2) * f32x2{r1, r1};
        zp[t][q] = gelu2((f32x2{acc[t][2 * q], acc[t][2 * q + 1]} - m2 * f32x2{cs1[2 * q], cs1[2 * q + 1]}) * f32x2{r12, r12});
      }
    }
    bst128<16>(pair_tiles(zp[0], zp[1]), rws, act_off + WS_Z + 14 * KB);
  }
  STAMP(6);
  HWAIT(HX2);

  { u32x4 v[2][2];
    if (act) fetch_payload<2, 14>(v, rws, zv[0], zv[1], WS_Z, 14 * KB, lds, lds0, L_Z + 2 * w * KB, lane, stage_weights, p.ws + (40u << 20) + 8); else stage_weights(); }
  STAMP(7);
  BARS();
  {
    f32x4 acc[2] = {};
    const LAS char* zb = lds + L_Z + l16;
    lds_pipe<28, 8>([&](int n) { return lds128(zb + ((n & 1) * 14 + (n >> 1)) * KB); }, [&](int n, u32x4 f) { acc[n & 1] = mfma16(wr[n >> 1], f, acc[n & 1]); }, [](int) {});
    BARS();
    u32x2 xp[2];
#pragma unroll
    for (int t = 0; t < 2; ++t) xp[t] = u32x2{pk2(yv[t][0][0] + acc[t][0], yv[t][0][1] + acc[t][1]), pk2(yv[t][1][0] + acc[t][2], yv[t][1][1] + acc[t][3])};
    bst128<16>(pair_tiles(xp[0], xp[1]), rws, act_off + WS_X1);
  }
  STAMP(8);
  if (j == 7) { ST_FLUSH(1, p.ws, b, w); ST_FLUSHQ(p.ws, b, w); return; }
  HWAIT(HX3);

  LAS float* st6 = (LAS float*)(lds + L_ST6);
  const int q4 = l15 >> 2, pp = lane & 3;
  const LAS char* a3[2];
#pragma unroll
  for (int f = 0; f < 2; ++f) a3[f] = lds + (8 * g + q4) * 512 + (((2 * w + f) ^ (q4 | ((g & 1) << 2))) * 32) + pp * 8;
  auto w3frag = [&](unsigned off, int f) { const u32x2 lo = tr_read(a3[f] + off), hi = tr_read(a3[f] + off + 2048); return u32x4{lo[0], lo[1], hi[0], hi[1]}; };
  u32x4 areg[4][2];
  { u32x4 v[4][2] = {};
    auto w3_regs = [&] { BARS();
#pragma unroll
      for (int n = 0; n < 4; ++n)
#pragma unroll
        for (int f = 0; f < 2; ++f) areg[n][f] = w3frag(n * 16 * KB, f);
      BARS(); };
    if (act) fetch_payload<4, 14>(v, rws, pv0, pv1, WS_X1 + 4 * i * 14 * KB, 14 * KB, lds, lds0, L_X1 + 2 * w * KB, lane, w3_regs, p.ws + (40u << 20) + 12); else w3_regs();
    row_stats<4>(v, ones, st6, w, l15, g); }
  STAMP(9);
  BARS();
  PROBE(4);
  {
    const LAS char* xp = lds + L_X1 + l16;
    f32x4 pt[4];
#pragma unroll
    for (int t = 0; t < 4; ++t) pt[t] = *(const LAS f32x4*)(st6 + ((t * 16 + l15) * 8 + 2 * g) * 2);
    f32x4 acc[2][4] = {};
    u32x4 bfr[2][4], afr[2][2];
    auto load_k = [&](int n) { const unsigned slot = ((n >> 1) % 3 == 2 ? L_RC : (n >> 1) % 3 == 0 ? L_RA : L_RB) + (n & 1) * 16 * KB;
#pragma unroll
      for (int f = 0; f < 2; ++f) afr[n & 1][f] = n < 4 ? areg[n][f] : w3frag(slot, f);
#pragma unroll
      for (int t = 0; t < 4; ++t) bfr[n & 1][t] = lds128(xp + (t * 14 + n) * KB); };
    load_k(0);
    SB();
#pragma unroll
    for (int n = 0; n < 14; ++n) {
      if (n & 1) BARS();
      if (n < 13) load_k(n + 1);
#pragma unroll
      for (int t = 0; t < 4; ++t) { acc[0][t] = mfma16(afr[n & 1][0], bfr[n & 1][t], acc[0][t]); acc[1][t] = mfma16(afr[n & 1][1], bfr[n & 1][t], acc[1][t]); }
#pragma unroll
      for (int q = 0; q < 4; ++q) { __builtin_amdgcn_sched_group_barrier(0x8, 1, 0); __builtin_amdgcn_sched_group_barrier(0x100, 2, 0); }
      __builtin_amdgcn_sched_group_barrier(0x8, 4, 0);
      SB();
    }
    STAMP(20);
    float mean[4], gis[4], gc1[4], gc2[4];
#pragma unroll
    for (int t = 0; t < 4; ++t) {
      const float s1 = gsum(pt[t][0] + pt[t][2]), s2 = gsum(pt[t][1] + pt[t][3]);
      mean[t] = s1 * (1.f / D);
      const float ve = fmaxf(s2 * (1.f / D) - mean[t] * mean[t], 0.f) + 1e-5f, rs = __builtin_amdgcn_rsqf(ve);
      gis[t] = ve * rs; gc1[t] = -2.3022082f * rs; gc2[t] = -0.10294324f * rs * rs * rs;
    }
#pragma unroll
    for (int t = 0; t < 4; ++t) {
      u32x4 hv;
#pragma unroll
      for (int q = 0; q < 4; ++q) { const int f = q >> 1, r = 2 * (q & 1);
        hv[q] = gelu2s(f32x2{acc[f][t][r], acc[f][t][r + 1]} - f32x2{mean[t], mean[t]} * f32x2{cs3[f][r], cs3[f][r + 1]}, gc1[t], gc2[t], gis[t]); }
      *(LAS u32x4*)(lds + L_HID + (t * 8 + w) * KB + l16) = hv;
    }
  }
  STAMP(10);
  BARS();
  STAMP(11);
  const int tb = w & 1, ft = w >> 1;
  u32x4 hf[2][8];
#pragma unroll
  for (int ks = 0; ks < 8; ++ks)
#pragma unroll
    for (int t = 0; t < 2; ++t) hf[t][ks] = lds128(lds + L_HID + ((2 * tb + t) * 8 + ks) * KB + l16);
  const unsigned pbase = l16 + WS_P + ((i * 49 + j) * 8 + w) * KB;
  const int Fo = 4 * j + ft;
  u32x2 hres[2], xres[2];
  unsigned ro[2];
#pragma unroll
  for (int t = 0; t < 2; ++t) { ro[t] = (((4 * i + 2 * tb + t) * 14 + (Fo >> 1)) * 64 + (2 * (Fo & 1) + (g >> 1)) * 16 + l15) * 16 + (g & 1) * 8;
    aload64_sc1(hres[t], rws, ro[t], WS_H); aload64_sc1(xres[t], rws, ro[t], WS_X1); }
  f32x4 acc[2] = {};
  {
    constexpr int PD = 4;
    constexpr unsigned slots[7] = {L_RB, L_RC, L_RA, L_RD, L_RB, L_RC, L_RA};
    const LAS char* wp = lds + (4 * g + q4) * 128 + ((ft ^ ((2 * g + (q4 >> 1)) & 3)) * 32) + pp * 8;
    auto w4frag = [&](unsigned off) { const u32x2 lo = tr_read(wp + off), hi = tr_read(wp + off + 2048); return u32x4{lo[0], lo[1], hi[0], hi[1]}; };
    u32x4 buf[PD + 1];
#pragma unroll
    for (int m = 0; m < PD; ++m) buf[m] = w4frag(slots[0] + m * 4096);
    SB();
#pragma unroll
    for (int m = 0; m < 56; ++m) {
      const int s = m >> 3, ks = m & 7;
      if (ks == 8 - PD && s < 6) { if (s >= 3) WAIT_VM(2); BARS(); }
      if (m + PD < 56) buf[(m + PD) % (PD + 1)] = w4frag(slots[(m + PD) >> 3] + ((m + PD) & 7) * 4096);
      SB();
      acc[0] = mfma16(buf[m % (PD + 1)], hf[0][ks], acc[0]); acc[1] = mfma16(buf[m % (PD + 1)], hf[1][ks], acc[1]);
      if (ks == 7 && s < 6) {
        bst128<16>(u32x4{pk2(acc[0][0], acc[0][1]), pk2(acc[0][2], acc[0][3]), pk2(acc[1][0], acc[1][1]), pk2(acc[1][2], acc[1][3])}, rws, pbase + pcol(s) * 56 * KB);
        acc[0] = f32x4{0.f, 0.f, 0.f, 0.f}; acc[1] = acc[0];
      }
      SB();
    }
  }
  STAMP(12);
  PROBE(5);
  {
    const __amdgpu_buffer_rsrc_t rout = mk_rsrc(p.out, S * D * 2);
    u32x4 pv[6];
    const unsigned pin = WS_P + ((i * 7 + j) * 7 * 8 + w) * KB;
    WAIT_VM(0); RETIRE(hres[0]); RETIRE(hres[1]); RETIRE(xres[0]); RETIRE(xres[1]);
    BARS();
    for (unsigned spin = 0; spin < SPIN_MAX; ++spin) {
      const unsigned m = max(max(max(hres[0][0], hres[0][1]), max(xres[0][0], xres[0][1])), max(max(hres[1][0], hres[1][1]), max(xres[1][0], xres[1][1])));
      if (!__any(m == SENT)) break;
#pragma unroll
      for (int t = 0; t < 2; ++t) { aload64_sc1(hres[t], rws, ro[t], WS_H); aload64_sc1(xres[t], rws, ro[t], WS_X1); }
      WAIT_VM(0); RETIRE(hres[0]); RETIRE(hres[1]); RETIRE(xres[0]); RETIRE(xres[1]);
    }
    wait_flags(p, lane < 7 && lane != j, WS_PF + ((i * 7 + j) * 8 + lane) * 128, epoch);
#pragma unroll
    for (int s = 0; s < 6; ++s) { int js = j + 1 + s; js = js >= 7 ? js - 7 : js; aload128_sc1(pv[s], rws, l16, pin + js * 8 * KB); }
    WAIT_VM(0);
#pragma unroll
    for (int s = 0; s < 6; ++s) RETIRE(pv[s]);
    const u32x4 selAB = selA | selB;
#pragma unroll
    for (int s = 0; s < 6; ++s) { acc[0] = mfma16(selA, pv[s], acc[0]); acc[1] = mfma16(selB, pv[s], acc[1]); }
#pragma unroll
    for (int t = 0; t < 2; ++t) {
      acc[t] = mfma16(selAB, u32x4{hres[t][0], hres[t][1], xres[t][0], xres[t][1]}, acc[t]);
      __builtin_amdgcn_raw_buffer_store_b64(u32x2{pk2(acc[t][0], acc[t][1]), pk2(acc[t][2], acc[t][3])}, rout, ((16 * (4 * i + 2 * tb + t) + l15) * D + 16 * Fo + 4 * g) * 2, 0, 0);
    }
  }
  PROBE(6);
  STAMP(13);
  ST_FLUSH(1, p.ws, b, w); ST_FLUSHQ(p.ws, b, w);
}

extern "C" void kernel_launch(void* const* d_in, const int* in_sizes, int n_in, void* d_out, int out_size, void* d_ws, size_t ws_size, hipStream_t stream) {
  Params p;
  p.x = (const u16*)d_in[0]; p.wqkv = (const u16*)d_in[1]; p.wo = (const u16*)d_in[2];
  p.w1 = (const u16*)d_in[4]; p.w2 = (const u16*)d_in[5]; p.w3 = (const u16*)d_in[6]; p.w4 = (const u16*)d_in[7];
  p.out = (u16*)d_out; p.ws = (char*)d_ws;
  (void)hipFuncSetAttribute((const void*)block_fused, hipFuncAttributeMaxDynamicSharedMemorySize, K2_LDS);
  block_fused<<<256, 640, K2_LDS, stream>>>(p);
}
```

```cpp
#include <hip/hip_runtime.h>
#include <hip/hip_bf16.h>
#include <stdint.h>

typedef unsigned short u16;
typedef __attribute__((ext_vector_type(8))) __bf16 bf16x8;
typedef __attribute__((ext_vector_type(2))) __bf16 bf16x2;
typedef __attribute__((ext_vector_type(4))) short s16x4;
typedef __attribute__((ext_vector_type(4))) float f32x4;
typedef __attribute__((ext_vector_type(16))) float f32x16;
typedef __attribute__((ext_vector_type(4))) unsigned u32x4;
typedef __attribute__((ext_vector_type(2))) unsigned u32x2;
#define LAS __attribute__((address_space(3)))
#define GAS __attribute__((address_space(1)))
#define DI __device__ __forceinline__

constexpr int S = 2048, D = 448, NH = 16, DH = 28, F = 1792;
constexpr unsigned SENT = 0xFFFFFFFFu;

constexpr unsigned KB = 1024;
constexpr unsigned WS_Q   = 0;
constexpr unsigned WS_K   = WS_Q + 2048 * KB;
constexpr unsigned WS_V   = WS_K + 2048 * KB;
constexpr unsigned WS_WO  = WS_V + 2048 * KB;
constexpr unsigned WS_W1  = WS_WO + 448 * KB;
constexpr unsigned WS_W2  = WS_W1 + 392 * KB;
constexpr unsigned WS_O   = WS_W2 + 392 * KB;
constexpr unsigned WS_H   = WS_O + 2048 * KB;
constexpr unsigned WS_X1  = WS_H + 1792 * KB;
constexpr unsigned WS_Z   = WS_X1 + 1792 * KB;
constexpr unsigned WS_P   = WS_Z + 1820 * KB;
constexpr unsigned WS_CS  = WS_P + 32 * 49 * 8 * KB;
constexpr unsigned WS_PF  = WS_CS + 18 * KB;
constexpr unsigned WS_FLG = WS_PF + 224 * KB;
constexpr unsigned WS_EP  = WS_FLG + 32 * KB;
constexpr unsigned WS_END = WS_EP + KB;

struct Params {
  const u16* x; const u16* wqkv; const u16* wo; const u16* w1; const u16* w2; const u16* w3; const u16* w4;
  u16* out; char* ws;
};

DI __amdgpu_buffer_rsrc_t mk_rsrc(const void* p, unsigned bytes) { return __builtin_amdgcn_make_buffer_rsrc((void*)p, (short)0, (int)bytes, 0x00020000); }
DI void dma16(__amdgpu_buffer_rsrc_t rs, unsigned voff, unsigned soff, unsigned ldsaddr) {
  asm volatile("s_mov_b32 m0, %0\n\ts_nop 0\n\tbuffer_load_dwordx4 %1, %2, %3 offen lds" :: "s"(ldsaddr), "v"(voff), "s"(rs), "s"(soff) : "memory");
}
DI void dma16_sc1(__amdgpu_buffer_rsrc_t rs, unsigned voff, unsigned soff, unsigned ldsaddr) {
  asm volatile("s_mov_b32 m0, %0\n\ts_nop 0\n\tbuffer_load_dwordx4 %1, %2, %3 offen sc1 lds" :: "s"(ldsaddr), "v"(voff), "s"(rs), "s"(soff) : "memory");
}
DI void dma4(__amdgpu_buffer_rsrc_t rs, unsigned voff, unsigned soff, unsigned ldsaddr) {
  asm volatile("s_mov_b32 m0, %0\n\ts_nop 0\n\tbuffer_load_dwordx4 %1, %2, %3 offen sc1 lds\n\tbuffer_load_dwordx4 %1, %2, %3 offen offset:1024 sc1 lds\n\t"
               "buffer_load_dwordx4 %1, %2, %3 offen offset:2048 sc1 lds\n\tbuffer_load_dwordx4 %1, %2, %3 offen offset:3072 sc1 lds" :: "s"(ldsaddr), "v"(voff), "s"(rs), "s"(soff) : "memory");
}
DI void dma3(__amdgpu_buffer_rsrc_t rs, unsigned voff, unsigned soff, unsigned ldsaddr) {
  asm volatile("s_mov_b32 m0, %0\n\ts_nop 0\n\tbuffer_load_dwordx4 %1, %2, %3 offen sc1 lds\n\tbuffer_load_dwordx4 %1, %2, %3 offen offset:1024 sc1 lds\n\t"
               "buffer_load_dwordx4 %1, %2, %3 offen offset:2048 sc1 lds" :: "s"(ldsaddr), "v"(voff), "s"(rs), "s"(soff) : "memory");
}
DI void dma4v(__amdgpu_buffer_rsrc_t rs, const unsigned (&v)[4], unsigned soff, unsigned ldsaddr) {
  asm volatile("s_mov_b32 m0, %0\n\ts_nop 0\n\tbuffer_load_dwordx4 %1, %5, %6 offen lds\n\tbuffer_load_dwordx4 %2, %5, %6 offen offset:1024 lds\n\t"
               "buffer_load_dwordx4 %3, %5, %6 offen offset:2048 lds\n\tbuffer_load_dwordx4 %4, %5, %6 offen offset:3072 lds"
               :: "s"(ldsaddr), "v"(v[0]), "v"(v[1]), "v"(v[2]), "v"(v[3]), "s"(rs), "s"(soff) : "memory");
}
#define WAIT_VM(n) asm volatile("s_waitcnt vmcnt(" #n ")" ::: "memory")
#define BAR() asm volatile("s_waitcnt lgkmcnt(0)\n\ts_barrier" ::: "memory")
#define BARS() do { __builtin_amdgcn_sched_barrier(0); BAR(); __builtin_amdgcn_sched_barrier(0); } while (0)
DI unsigned rfl(unsigned v) { return __builtin_amdgcn_readfirstlane(v); }
DI float bflo(unsigned u) { return __uint_as_float(u << 16); }
DI float bfhi(unsigned u) { return __uint_as_float(u & 0xffff0000u); }
DI unsigned pk2(float a, float b) { bf16x2 v; v[0] = (__bf16)a; v[1] = (__bf16)b; return __builtin_bit_cast(unsigned, v); }
DI float dot2(unsigned a, unsigned b, float c) { return __builtin_amdgcn_fdot2_f32_bf16(__builtin_bit_cast(bf16x2, a), __builtin_bit_cast(bf16x2, b), c, false); }
DI f32x16 mfma32(u32x4 a, u32x4 b, f32x16 c) { return __builtin_amdgcn_mfma_f32_32x32x16_bf16(__builtin_bit_cast(bf16x8, a), __builtin_bit_cast(bf16x8, b), c, 0, 0, 0); }
DI f32x4 mfma16(u32x4 a, u32x4 b, f32x4 c) { return __builtin_amdgcn_mfma_f32_16x16x32_bf16(__builtin_bit_cast(bf16x8, a), __builtin_bit_cast(bf16x8, b), c, 0, 0, 0); }
DI u32x2 tr_read(const LAS char* p) { return __builtin_bit_cast(u32x2, __builtin_amdgcn_ds_read_tr16_b64_v4i16((LAS s16x4*)p)); }
DI u32x4 lds128(const LAS char* p) { return *(const LAS u32x4*)p; }
template <int AUX> DI void bst128(u32x4 v, __amdgpu_buffer_rsrc_t rs, unsigned off) { __builtin_amdgcn_raw_buffer_store_b128(v, rs, off, 0, AUX); }
#define XJ j
constexpr unsigned ONES = 0x3F803F80u;
#define AUX_SIDE 16
#define AUX_QKV 16
#define ST_DECL
#define ST_PARAM
#define ST_DECLQ
#define ST_ARGQ
#define ST_FLUSHQ(ws, b, w)
#define CSTAMP(n)
#define STAMP(n)
#define ST_FLUSH(kern, ws, b, w)

DI void aload128(u32x4& d, __amdgpu_buffer_rsrc_t rs, unsigned voff, unsigned soff) { asm volatile("buffer_load_dwordx4 %0, %1, %2, %3 offen" : "=v"(d) : "v"(voff), "s"(rs), "s"(soff) : "memory"); }
DI void aload64(u32x2& d, __amdgpu_buffer_rsrc_t rs, unsigned voff, unsigned soff) { asm volatile("buffer_load_dwordx2 %0, %1, %2, %3 offen" : "=v"(d) : "v"(voff), "s"(rs), "s"(soff) : "memory"); }
DI void aload128_sc1(u32x4& d, __amdgpu_buffer_rsrc_t rs, unsigned voff, unsigned soff) { asm volatile("buffer_load_dwordx4 %0, %1, %2, %3 offen sc1" : "=v"(d) : "v"(voff), "s"(rs), "s"(soff) : "memory"); }
DI void aload64_sc1(u32x2& d, __amdgpu_buffer_rsrc_t rs, unsigned voff, unsigned soff) { asm volatile("buffer_load_dwordx2 %0, %1, %2, %3 offen sc1" : "=v"(d) : "v"(voff), "s"(rs), "s"(soff) : "memory"); }
#define RETIRE(x) asm volatile("; RETIRE %0" : "+v"(x))
constexpr unsigned CODE_BYTES = 50 * 1024;
DI unsigned touch_code(unsigned long long base, unsigned bytes, int b, int lane, bool on) {
  const __amdgpu_buffer_rsrc_t rc = mk_rsrc((const void*)base, bytes);
  const unsigned per = (bytes / 128 + 31) / 32, line = (b >> 3) * per + lane;
  unsigned d;
  asm volatile("buffer_load_dword %0, %1, %2, 0 offen" : "=v"(d) : "v"(on && lane < (int)per ? line * 128 : 0xF0000000u), "s"(rc) : "memory");
  return d;
}
#define PFS 128
template <unsigned BYTES> DI void touch_input(unsigned& d, const void* base, int b, int lane) {
  constexpr unsigned PART = (BYTES / 256 + PFS - 1) / PFS * PFS;
  const __amdgpu_buffer_rsrc_t r = mk_rsrc(base, BYTES);
#pragma unroll
  for (unsigned o = 0; o < PART; o += 64 * PFS) asm volatile("buffer_load_dword %0, %1, %2, %3 offen" : "+v"(d) : "v"(o + lane * PFS < PART ? lane * PFS : 0xF0000000u), "s"(r), "s"(b * PART + o) : "memory");
}
DI unsigned long long code_base() { unsigned long long pc; asm volatile("s_getpc_b64 %0" : "=s"(pc)); return pc & ~0xFFull; }

#define HX0 8
#define HX1 8
#define HX2 12
#define HX3 10
#define HWAIT(x) do { WAIT_VM(0); if ((x) > 0) __builtin_amdgcn_s_sleep(x); } while (0)
#define ROTQ 0
#define XMAP 2
DI void map_block(int b, int& i, int& j) { const int x = b & 7, s = b >> 3; constexpr int JPX = 8 / XMAP; i = (x / XMAP) * 4 * XMAP + s / JPX; j = (x % XMAP) * JPX + s % JPX; }

constexpr unsigned K1_XS = 0;
constexpr unsigned K1_W0 = 7 * 8192;
constexpr unsigned K1_SLOT = 24576;
constexpr unsigned K1_ST = K1_W0 + 4 * K1_SLOT;
constexpr unsigned K1_CS = K1_ST + 512;
constexpr unsigned K1_EP = K1_CS + 2 * 2 * 3 * 32 * 4;

DI void retile_load(const Params& p, int mat, int ks, int cb, int lane, u32x4 (&v)[4]) {
  const u16* W = mat == 0 ? p.wo : mat == 1 ? p.w1 : mat == 2 ? p.w2 : p.w3;
  const unsigned ldw = mat == 3 ? F : D, nrows = D, krow0 = (mat == 0 ? 28 : 32) * ks;
#pragma unroll
  for (int q = 0; q < 4; ++q) {
    unsigned row = krow0 + (lane >> 3) + 8 * q; row = row < nrows ? row : nrows - 1;
    v[q] = *(const u32x4*)(W + (size_t)row * ldw + cb * 64 + (lane & 7) * 8);
  }
}
template <bool SUMS>
DI void retile_store(const Params& p, LAS char* scr, int mat, int ks, int cb, int lane, const u32x4 (&v)[4], f32x4 (&cs)[4]) {
#pragma unroll
  for (int q = 0; q < 4; ++q) *(LAS u32x4*)(scr + q * 1024 + lane * 16) = v[q];
  const int g = lane >> 4, q4 = (lane & 15) >> 2, pp = lane & 3;
  const int r0 = 8 * g + q4, r1 = 8 * g + 4 + q4;
  const __amdgpu_buffer_rsrc_t rws = mk_rsrc(p.ws, WS_END);
  const u32x4 ones = {ONES, ONES, ONES, ONES};
#pragma unroll
  for (int ft = 0; ft < 4; ++ft) {
    u32x2 lo = tr_read(scr + r0 * 128 + ft * 32 + pp * 8), hi = tr_read(scr + r1 * 128 + ft * 32 + pp * 8);
    if (mat == 0 && g == 3) { hi[0] = 0u; hi[1] = 0u; }
    const u32x4 f = {lo[0], lo[1], hi[0], hi[1]};
    const int Fi = 4 * cb + ft;
    unsigned dst;
    if (mat == 0) dst = WS_WO + (((Fi / 7) * 16 + ks) * 7 + Fi % 7) * KB;
    else dst = (mat == 1 ? WS_W1 : WS_W2) + (((Fi / 7) * 14 + ks) * 7 + Fi % 7) * KB;
    if (mat != 3) bst128<AUX_SIDE>(f, rws, lane * 16 + dst);
    if (SUMS) cs[ft] = mfma16(f, ones, cs[ft]);
  }
}

DI int qk_slot(int n0, int& half, bool& last) {
  const int head = n0 / 28, hs = 28 * head, bnd = (hs / 64 + 1) * 64;
  const bool split = bnd < hs + 28, second = split && n0 >= bnd;
  const int seg0 = second ? bnd : hs, seg1 = (split && !second) ? bnd : hs + 28, off = n0 - seg0;
  half = (off >> 2) & 1;
  last = n0 + 4 == seg1 && half == 0;
  return 4 * head + (second ? (bnd - hs + 7) >> 3 : 0) + (off >> 3);
}
DI void fill_early(__amdgpu_buffer_rsrc_t rws, int i, int j, int lw, int lane) {
  const u32x4 sv = {SENT, SENT, SENT, SENT};
  const unsigned l16 = lane * 16, tt0 = 4 * i + 2 * (j >> 2), F0 = 7 * (j & 3);
#pragma unroll
  for (int t = 0; t < 2; ++t) { const unsigned o = WS_O + ((4 * i + 2 * lw + t) * 16 + 2 * j) * KB + l16; bst128<16>(sv, rws, o); bst128<16>(sv, rws, o + KB); }
  bst128<16>(sv, rws, lane < 28 ? WS_Z + ((tt0 + lw + 1) * 14 + (F0 >> 1)) * KB + (F0 & 1) * 512 + (lane >> 1) * 256 + (lw * 14 + (lane & 1)) * 16 : 0xF0000000u);
}
DI void fill_late(__amdgpu_buffer_rsrc_t rws, int i, int j, int lw, int lane) {
  const u32x4 sv = {SENT, SENT, SENT, SENT};
  const unsigned l16 = lane * 16, tt0 = 4 * i + 2 * (j >> 2), F0 = 7 * (j & 3);
  const unsigned o = ((tt0 + lw) * 14 + (F0 >> 1)) * KB + (F0 & 1) * 512 + l16;
#pragma unroll
  for (int u = 0; u < 3; ++u) { const unsigned base = u == 0 ? WS_H : u == 1 ? WS_X1 : WS_Z + 14 * KB;
    bst128<16>(sv, rws, o + base); bst128<16>(sv, rws, o + base + KB); bst128<16>(sv, rws, o + base + 2 * KB); bst128<16>(sv, rws, lane < 32 ? o + base + 3 * KB : 0xF0000000u); }
}

DI void phase_qkv(const Params& p, LAS char* lds, unsigned lds0, unsigned long long cbase, int b, int i, int j, int w, int lane ST_PARAM) {
  STAMP(0);
  unsigned epv = 0, cwarm = 0, twarm = 0;
  auto errands = [&] {
    asm volatile("buffer_load_dword %0, %1, %2, 0 offen sc1" : "=v"(epv) : "v"(WS_EP), "s"(mk_rsrc(p.ws, WS_END)) : "memory");
    cwarm = touch_code(cbase, CODE_BYTES, b, lane, true);
  };
  auto errands_done = [&] { WAIT_VM(0); RETIRE(epv); RETIRE(cwarm); RETIRE(twarm); if (lane == 0) *(LAS unsigned*)(lds + K1_EP) = epv + 1u; };

  if (j == 7) {
    const __amdgpu_buffer_rsrc_t rws = mk_rsrc(p.ws, WS_END);
    if (w >= 8) {
      const int lw = w - 8;
      if (lw == 1) { errands(); errands_done(); }
      else { unsigned d = 0; touch_input<D * 3 * D * 2>(d, p.wqkv, b, lane); touch_input<S * D * 2>(d, p.x, b, lane); touch_input<D * F * 2>(d, p.w4, b, lane); WAIT_VM(0); RETIRE(d); }
      fill_early(rws, i, j, lw, lane);
#pragma unroll
      for (int n = 0; n < 4; ++n) { const int pr = (lw * 4 + n) * 4 + (lane >> 4), gq = (lane >> 2) & 3, row = lane & 3; const unsigned o = row == 0 ? ONES : 0u;
        bst128<16>(u32x4{o, o, o, o}, rws, WS_V + ((((pr >> 1) * 64 + 2 * i + (pr & 1)) * 2 + 1) * 64 + gq * 16 + 12 + row) * 16); }
      if (i == 0 || i == 31) {
#pragma unroll
        for (int n = 0; n < 7; ++n) bst128<16>(u32x4{0u, 0u, 0u, 0u}, rws, WS_Z + ((i == 0 ? 0 : 129) * 14 + lw * 7 + n) * KB + lane * 16); }
    } else {
    const int colid = w < 2 ? w * 32 + i : (w == 2 && i < 6) ? 64 + i : -1;
    const int nid = w >= 3 ? i * 5 + (w - 3) : 160 + (i - 6);
    u32x4 v[7][4];
    auto other = [&](int m, int& mat, int& ks, int& cb) { if (m < 112) { mat = 0; ks = m / 7; cb = m % 7; } else { mat = 2; ks = (m - 112) / 7; cb = (m - 112) % 7; } };
    const int ccol = colid >> 1, chalf = colid & 1, cmat = ccol < 7 ? 1 : 3, ccb = ccol < 7 ? ccol : ccol - 7;
    f32x4 cs[4] = {};
    if (colid >= 0) {
#pragma unroll
      for (int q = 0; q < 7; ++q) retile_load(p, cmat, 7 * chalf + q, ccb, lane, v[q]);
#pragma unroll
      for (int q = 0; q < 7; ++q) retile_store<true>(p, lds + (2 * w + (q & 1)) * 4096, cmat, 7 * chalf + q, ccb, lane, v[q], cs);
      if ((lane & 15) == 0) {
#pragma unroll
        for (int ft = 0; ft < 4; ++ft) bst128<16>(__builtin_bit_cast(u32x4, cs[ft]), rws, WS_CS + (chalf * 2240 + (cmat == 1 ? 0 : 448) + 64 * ccb + 16 * ft + 4 * (lane >> 4)) * 4); }
    } else {
#pragma unroll
      for (int q = 0; q < 2; ++q) { const int m = nid + 186 * q; if (m < 210) { int mat, ks, cb; other(m, mat, ks, cb); retile_load(p, mat, ks, cb, lane, v[q]); } }
#pragma unroll
      for (int q = 0; q < 2; ++q) { const int m = nid + 186 * q; if (m < 210) { int mat, ks, cb; other(m, mat, ks, cb); retile_store<false>(p, lds + (2 * w + (q & 1)) * 4096, mat, ks, cb, lane, v[q], cs); } }
    }
    }
    STAMP(7);
    WAIT_VM(0); BARS();
    return;
  }
  if (w == 6 || w == 7) {
    if (w == 6) { unsigned d = 0; touch_input<D * 3 * D * 2>(d, p.wqkv, b, lane); touch_input<S * D * 2>(d, p.x, b, lane); touch_input<D * F * 2>(d, p.w4, b, lane);
#pragma unroll
      for (int c = 0; c < 3; ++c) BARS();
      WAIT_VM(0); RETIRE(d);
    } else
    if (w == 7) {
      errands();
#pragma unroll
      for (int c = 0; c < 3; ++c) BARS();
      errands_done();
    } else {
#pragma unroll
      for (int c = 0; c < 3; ++c) BARS();
    }
    fill_early(mk_rsrc(p.ws, WS_END), i, j, w - 6, lane);
#pragma unroll
    for (int c = 3; c < 9; ++c) BARS();
    WAIT_VM(0); BARS();
    return;
  }
  if (w == 4 || w == 5) {
    const int tok = 32 * (w - 4) + (lane & 31), part = lane >> 5;
    const LAS char* xb[4];
#pragma unroll
    for (int q = 0; q < 4; ++q) xb[q] = lds + K1_XS + tok * 128 + ((4 * part + q) ^ ((tok >> 1) & 7)) * 16;
    float s1a = 0.f, s1b = 0.f, s2a = 0.f, s2b = 0.f;
    BARS();
#pragma unroll
    for (int c = 0; c < 7; ++c) {
      u32x4 v[4];
#pragma unroll
      for (int q = 0; q < 4; ++q) v[q] = lds128(xb[q] + c * 8192);
      __builtin_amdgcn_sched_barrier(0);
#pragma unroll
      for (int q = 0; q < 4; ++q)
#pragma unroll
        for (int u = 0; u < 4; ++u) { float lo = bflo(v[q][u]), hi = bfhi(v[q][u]); asm volatile("" : "+v"(lo), "+v"(hi));
          s1a += lo; s1b += hi; s2a = fmaf(lo, lo, s2a); s2b = fmaf(hi, hi, s2b); }
      asm volatile("" : "+v"(s1a), "+v"(s1b), "+v"(s2a), "+v"(s2b));
      if (c == 6) {
        float a1 = s1a + s1b, a2 = s2a + s2b;
        a1 += __shfl_xor(a1, 32); a2 += __shfl_xor(a2, 32);
        const float mean = a1 * (1.f / D), var = fmaxf(a2 * (1.f / D) - mean * mean, 0.f);
        if (lane < 32) { *(LAS float*)(lds + K1_ST + tok * 4) = mean; *(LAS float*)(lds + K1_ST + 256 + tok * 4) = __builtin_amdgcn_rsqf(var + 1e-5f); }
      }
      BARS();
    }
    BARS();
    STAMP(7);
    WAIT_VM(0); BARS();
    return;
  }
  if (w >= 8) {
    const int lw = w - 8;
    const __amdgpu_buffer_rsrc_t rx = mk_rsrc(p.x, S * D * 2), rw = mk_rsrc(p.wqkv, D * 3 * D * 2);
    const unsigned xv0 = (lane >> 3) * 896 + ((lane & 7) ^ (lane >> 4)) * 16, xv1 = (lane >> 3) * 896 + ((lane & 7) ^ (lane >> 4) ^ 4) * 16;
    const unsigned wv = (lane >> 3) * 2688 + (((lane & 7) ^ (((lane >> 4) & 1) * 4)) * 16);
    const int rotq = ROTQ;
    auto issue_chunk = [&](int c) {
      int cc = c + rotq; cc = cc >= 7 ? cc - 7 : cc;
#pragma unroll
      for (int q = 0; q < 4; ++q) { const unsigned n = lw * 4 + q; dma16(rx, (q & 1) ? xv1 : xv0, (64 * i + 8 * n) * 896 + cc * 128, lds0 + K1_XS + c * 8192 + n * 1024); }
#pragma unroll
      for (int q = 0; q < 12; ++q) { const unsigned n = lw * 12 + q, type = n >> 3, rg = n & 7;
        dma16(rw, wv, (64 * cc + 8 * rg) * 2688 + type * 896 + j * 128, lds0 + K1_W0 + (c & 3) * K1_SLOT + type * 8192 + rg * 1024); }
    };
    issue_chunk(0); issue_chunk(1);
    STAMP(1);
    WAIT_VM(16); STAMP(2); BARS();
    issue_chunk(2); issue_chunk(3);
    WAIT_VM(32); STAMP(3); BARS();
    issue_chunk(4);
    WAIT_VM(32); STAMP(8); BARS();
    issue_chunk(5);
    WAIT_VM(32); STAMP(9); BARS();
    issue_chunk(6);
    WAIT_VM(32); STAMP(10); BARS();
    WAIT_VM(16); STAMP(11); BARS();
    WAIT_VM(0); STAMP(5); BARS();
    BAR();
    BARS();
    STAMP(7);
    WAIT_VM(0); BARS();
    return;
  }
  const int tb = w & 1, e = w >> 1, r = lane & 31, h = lane >> 5;
  f32x16 aq = {}, ak = {}, av = {};
  f32x4 cq = {}, ck = {}, cv = {};
  const unsigned cs1 = ((lane & 15) == ((lane >> 4) & 1)) ? ONES : 0u;
  const u32x4 csel = {cs1, cs1, cs1, cs1};
  const int q4 = (lane & 15) >> 2, pp = lane & 3, blk = (lane >> 4) & 1;
  const LAS char* xb[4];
#pragma unroll
  for (int kl = 0; kl < 4; ++kl) xb[kl] = lds + K1_XS + (32 * tb + r) * 128 + ((2 * (kl ^ tb) + h) ^ ((r >> 1) & 7)) * 16;
  const unsigned wb = K1_W0 + (8 * h + q4) * 128 + (((8 * e + 4 * blk + pp) ^ (8 * ((q4 >> 1) & 1))) * 8);
  const LAS char* we = lds + wb + tb * 2048;
  const LAS char* wo = lds + wb + (1 - tb) * 2048;
  u32x4 xf[3], f[3][3];
  auto load_step = [&](int n) {
    xf[n % 3] = lds128(xb[n & 3] + (n >> 2) * 8192);
    const LAS char* wp = ((n & 1) ? wo : we) + ((n >> 2) & 3) * K1_SLOT + (n & 2) * 2048;
#pragma unroll
    for (int t = 0; t < 3; ++t) { const u32x2 lo = tr_read(wp + t * 8192), hi = tr_read(wp + t * 8192 + 512); f[n % 3][t] = u32x4{lo[0], lo[1], hi[0], hi[1]}; }
  };
  const __amdgpu_buffer_rsrc_t rws = mk_rsrc(p.ws, WS_END);
  constexpr unsigned K1_IMG = K1_W0 + 3 * K1_SLOT;
  int g0half; bool g0last; const int G0 = qk_slot(64 * j, g0half, g0last);
  unsigned qdst[4], zdst[4], voff;
  {
#pragma unroll
    for (int g4 = 0; g4 < 4; ++g4) { int half; bool last; const int G = qk_slot(64 * j + 32 * e + 8 * g4 + 4 * h, half, last);
      qdst[g4] = K1_IMG + (G - G0) * KB + ((2 * tb + (r >> 4)) * 16 + (r & 15)) * 16 + half * 8;
      zdst[g4] = last ? qdst[g4] + 8 : K1_IMG + 2 * 10 * KB + lane * 8;
      asm volatile("" : "+v"(qdst[g4]), "+v"(zdst[g4])); }
    const unsigned n = 64 * j + 32 * e + r, head = n / 28, d = n - 28 * head;
    voff = WS_V + (((head * 64 + 2 * i + tb) * 2 + (d >> 4)) * 64 + h * 16 + (d & 15)) * 16; asm volatile("" : "+v"(voff)); }
  STAMP(1);
  BARS();
  STAMP(2);
  load_step(0);
#pragma unroll
  for (int n = 0; n < 28; ++n) {
    if ((n & 3) == 3 && n < 27) { if (n == 7) STAMP(8); if (n == 11) STAMP(10); BARS(); if (n == 7) { STAMP(9); CSTAMP(13); } if (n == 11) { STAMP(11); CSTAMP(14); } if (n == 15) STAMP(12); }
    if (n < 27) load_step(n + 1);
    aq = mfma32(f[n % 3][0], xf[n % 3], aq);
    ak = mfma32(f[n % 3][1], xf[n % 3], ak);
    av = mfma32(xf[n % 3], f[n % 3][2], av);
    if (!(n & 1)) {
      cq = mfma16(f[n % 3][0], csel, cq); ck = mfma16(f[n % 3][1], csel, ck); cv = mfma16(f[n % 3][2], csel, cv);
    }
    __builtin_amdgcn_sched_group_barrier(0x8, 1, 0); __builtin_amdgcn_sched_group_barrier(0x100, 3, 0);
    __builtin_amdgcn_sched_group_barrier(0x8, 1, 0); __builtin_amdgcn_sched_group_barrier(0x100, 2, 0);
    __builtin_amdgcn_sched_group_barrier(0x8, 1, 0); __builtin_amdgcn_sched_group_barrier(0x100, 2, 0);
    __builtin_amdgcn_sched_group_barrier(0x8, 3, 0);
    __builtin_amdgcn_sched_barrier(0);
  }
  STAMP(4);
  LAS float* cs = (LAS float*)(lds + K1_CS) + e * 192;
  if ((lane & 15) < 2) { LAS float* o = cs + tb * 96 + 16 * (lane & 1) + 4 * (lane >> 4);
    *(LAS f32x4*)o = cq; *(LAS f32x4*)(o + 32) = ck; *(LAS f32x4*)(o + 64) = cv; }
  BAR();
  STAMP(5);
  const LAS float* st = (const LAS float*)(lds + K1_ST);
  f32x4 cqk[2][4][2], m4[4], r4[4];
#pragma unroll
  for (int t = 0; t < 2; ++t)
#pragma unroll
    for (int g4 = 0; g4 < 4; ++g4) { cqk[t][g4][0] = *(const LAS f32x4*)(cs + t * 32 + 8 * g4 + 4 * h); cqk[t][g4][1] = *(const LAS f32x4*)(cs + 96 + t * 32 + 8 * g4 + 4 * h); }
  const float mean = st[32 * tb + r], rstd = st[64 + 32 * tb + r];
  const float cs2 = cs[64 + r] + cs[96 + 64 + r];
#pragma unroll
  for (int g4 = 0; g4 < 4; ++g4) { m4[g4] = *(const LAS f32x4*)(st + 32 * tb + 8 * g4 + 4 * h); r4[g4] = *(const LAS f32x4*)(st + 64 + 32 * tb + 8 * g4 + 4 * h); }
  __builtin_amdgcn_sched_barrier(0);
#pragma unroll
  for (int g4 = 0; g4 < 4; ++g4)
#pragma unroll
    for (int t = 0; t < 2; ++t) {
      const f32x16& a = t ? ak : aq;
      float v[4];
#pragma unroll
      for (int q = 0; q < 4; ++q) v[q] = rstd * (a[4 * g4 + q] - mean * (cqk[t][g4][0][q] + cqk[t][g4][1][q]));
      *(LAS u32x2*)(lds + qdst[g4] + t * 10 * KB) = u32x2{pk2(v[0], v[1]), pk2(v[2], v[3])};
      *(LAS u32x2*)(lds + zdst[g4] + (zdst[g4] < K1_IMG + 2 * 10 * KB ? t * 10 * KB : 0)) = u32x2{0u, 0u};
    }
  {
    float v[16];
#pragma unroll
    for (int g4 = 0; g4 < 4; ++g4)
#pragma unroll
      for (int q = 0; q < 4; ++q) v[4 * g4 + q] = r4[g4][q] * (av[4 * g4 + q] - m4[g4][q] * cs2);
    const u32x4 oa = {pk2(v[0], v[1]), pk2(v[2], v[3]), pk2(v[8], v[9]), pk2(v[10], v[11])};
    const u32x4 ob = {pk2(v[4], v[5]), pk2(v[6], v[7]), pk2(v[12], v[13]), pk2(v[14], v[15])};
    bst128<AUX_QKV>(oa, rws, voff);
    bst128<AUX_QKV>(ob, rws, voff + 512);
  }
  BARS();
  { int lh; bool ll; const int NS = qk_slot(64 * j + 60, lh, ll) - G0 + 1;
#pragma unroll
    for (int q = 0; q < 3; ++q) { const int ls = w + 4 * q;
      if (ls < NS) { const int G = G0 + ls; const unsigned o = WS_Q + (((G >> 2) * 128 + 4 * i + (lane >> 4)) * 64 + (G & 3) * 16 + (lane & 15)) * 16;
        bst128<AUX_QKV>(lds128(lds + K1_IMG + ls * KB + lane * 16), rws, o);
        bst128<AUX_QKV>(lds128(lds + K1_IMG + (10 + ls) * KB + lane * 16), rws, o + (WS_K - WS_Q)); } }
  }
  STAMP(6);
  WAIT_VM(0);
  STAMP(7);
  WAIT_VM(0); BARS();
}

constexpr unsigned L_K = 0, L_WOA = 40 * KB, L_V = 120 * KB, L_WOB = 0;
constexpr unsigned L_O = 128 * KB;
constexpr unsigned L_W = 0;
constexpr unsigned L_H = 98 * KB, L_ST4 = 126 * KB;
constexpr unsigned L_Z = 128 * KB;
constexpr unsigned L_X1 = 96 * KB, L_ST6 = 152 * KB;
constexpr unsigned L_HID = 96 * KB;
constexpr unsigned L_RA = 0, L_RB = 32 * KB, L_RC = 64 * KB, L_RD = 128 * KB;
constexpr unsigned K2_LDS = 163840;
constexpr unsigned SPIN_MAX = 1u << 16;

DI u32x4 bload(__amdgpu_buffer_rsrc_t rs, unsigned voff, unsigned soff) { return __builtin_amdgcn_raw_buffer_load_b128(rs, voff, soff, 0); }
DI u32x4 bload_sc1(__amdgpu_buffer_rsrc_t rs, unsigned voff, unsigned soff) { return __builtin_amdgcn_raw_buffer_load_b128(rs, voff, soff, 16); }
DI void bstore_sc1(u32x4 v, __amdgpu_buffer_rsrc_t rs, unsigned voff, unsigned soff) { bst128<16>(v, rs, voff + soff); }
DI unsigned umax4(u32x4 v) { return max(max(v[0], v[1]), max(v[2], v[3])); }
#define FENCE() asm volatile("" ::: "memory")
typedef __attribute__((ext_vector_type(2))) float f32x2_;
DI float gelu_tanh(float x) {
  const float e = __builtin_amdgcn_exp2f(x * fmaf(x * x, -0.10294324f, -2.3022082f));
  return x * __builtin_amdgcn_rcpf(1.f + e);
}
DI u32x4 pair_tiles(u32x2 a, u32x2 b) {
  const auto s0 = __builtin_amdgcn_permlane16_swap(a[0], b[0], false, false), s1 = __builtin_amdgcn_permlane16_swap(a[1], b[1], false, false);
  return u32x4{s0[0], s1[0], s0[1], s1[1]};
}
template <int NT, int NS, class F>
DI void fetch_payload(u32x4 (&v)[NT][2], __amdgpu_buffer_rsrc_t rs, unsigned voff0, unsigned voff1, unsigned soff, unsigned tstride, LAS char* lds, unsigned lds0, unsigned dst, int lane, F fill, void* dbg = nullptr) {
  for (unsigned spin = 0;; ++spin) {
#pragma unroll
    for (int t = 0; t < NT; ++t) { dma16_sc1(rs, voff0, soff + t * tstride, lds0 + dst + t * NS * KB); dma16_sc1(rs, voff1, soff + t * tstride, lds0 + dst + (t * NS + 1) * KB); }
    if (spin == 0) { fill(); __builtin_amdgcn_sched_barrier(0); }
    WAIT_VM(0);
    unsigned m = 0;
#pragma unroll
    for (int t = 0; t < NT; ++t)
#pragma unroll
      for (int q = 0; q < 2; ++q) { v[t][q] = lds128(lds + dst + (t * NS + q) * KB + lane * 16); m = max(m, max(max(v[t][q][0], v[t][q][1]), max(v[t][q][2], v[t][q][3]))); }
    if (!__any(m == SENT) || spin > SPIN_MAX) break;
  }
}
template <int NT> DI void row_stats(const u32x4 (&v)[NT][2], u32x4 ones, LAS float* stp, int w, int l15, int g) {
#pragma unroll
  for (int t = 0; t < NT; ++t) {
    f32x4 d1 = {}, d2 = {};
#pragma unroll
    for (int q = 0; q < 2; ++q) { d1 = __builtin_amdgcn_mfma_f32_16x16x32_bf16(__builtin_bit_cast(bf16x8, ones), __builtin_bit_cast(bf16x8, v[t][q]), d1, 0, 0, 0);
                                  d2 = __builtin_amdgcn_mfma_f32_16x16x32_bf16(__builtin_bit_cast(bf16x8, v[t][q]), __builtin_bit_cast(bf16x8, v[t][q]), d2, 0, 0, 0); }
    const float lo = (l15 & 1) ? d2[1] : d2[0], hi = (l15 & 1) ? d2[3] : d2[2], s2 = (l15 & 2) ? hi : lo;
    if (g == (l15 >> 2)) *(LAS f32x2_*)(stp + ((t * 16 + l15) * 8 + w) * 2) = f32x2_{d1[0], s2};
  }
}

#define SB() __builtin_amdgcn_sched_barrier(0)
typedef __attribute__((ext_vector_type(2))) float f32x2;
DI float max3(float a, float b, float c) { float r; asm("v_max3_f32 %0, %1, %2, %3" : "=v"(r) : "v"(a), "v"(b), "v"(c)); return r; }
DI float gsum(float v) {
  const auto a = __builtin_amdgcn_permlane16_swap(__float_as_uint(v), __float_as_uint(v), false, false); const float s = __uint_as_float(a[0]) + __uint_as_float(a[1]);
  const auto c = __builtin_amdgcn_permlane32_swap(__float_as_uint(s), __float_as_uint(s), false, false); return __uint_as_float(c[0]) + __uint_as_float(c[1]);
}
DI float gmax(float v) {
  const auto a = __builtin_amdgcn_permlane16_swap(__float_as_uint(v), __float_as_uint(v), false, false); const float s = max3(__uint_as_float(a[0]), __uint_as_float(a[1]), __uint_as_float(a[1]));
  const auto c = __builtin_amdgcn_permlane32_swap(__float_as_uint(s), __float_as_uint(s), false, false); return max3(__uint_as_float(c[0]), __uint_as_float(c[1]), __uint_as_float(c[1]));
}
DI unsigned gelu2(f32x2 x) {
  const f32x2 t = x * (x * x * f32x2{-0.10294324f, -0.10294324f} + f32x2{-2.3022082f, -2.3022082f});
  const f32x2 d = f32x2{__builtin_amdgcn_exp2f(t[0]), __builtin_amdgcn_exp2f(t[1])} + f32x2{1.f, 1.f};
  const f32x2 r = x * f32x2{__builtin_amdgcn_rcpf(d[0]), __builtin_amdgcn_rcpf(d[1])};
  return pk2(r[0], r[1]);
}
DI unsigned gelu2s(f32x2 a, float c1, float c2, float is) {
  const f32x2 t = a * (a * a * f32x2{c2, c2} + f32x2{c1, c1});
  const f32x2 d = f32x2{__builtin_amdgcn_exp2f(t[0]), __builtin_amdgcn_exp2f(t[1])} * f32x2{is, is} + f32x2{is, is};
  const f32x2 r = a * f32x2{__builtin_amdgcn_rcpf(d[0]), __builtin_amdgcn_rcpf(d[1])};
  return pk2(r[0], r[1]);
}
template <int N, int PD, class L, class U, class M> DI void lds_pipe(L load, U use, M mid) {
  u32x4 buf[PD + 1];
#pragma unroll
  for (int n = 0; n < PD; ++n) buf[n] = load(n);
  SB();
#pragma unroll
  for (int n = 0; n < N; ++n) {
    if (n + PD < N) buf[(n + PD) % (PD + 1)] = load(n + PD);
    SB();
    use(n, buf[n % (PD + 1)]);
    mid(n);
    SB();
  }
}

DI void wait_flags(const Params& p, bool on, unsigned off, unsigned epoch) {
  const GAS unsigned* f = (const GAS unsigned*)(p.ws + (on ? off : WS_EP));
  for (unsigned spin = 0; spin < SPIN_MAX; ++spin) {
    const unsigned v = __hip_atomic_load(f, __ATOMIC_RELAXED, __HIP_MEMORY_SCOPE_AGENT);
    if (__all(!on || v == epoch)) break;
    __builtin_amdgcn_s_sleep(2);
  }
}

__global__ void __launch_bounds__(640, 3) block_fused(Params p) {
  extern __shared__ __attribute__((aligned(16))) char smem[];
  LAS char* lds = (LAS char*)smem;
  const unsigned lds0 = (unsigned)(size_t)lds;
  const int b = blockIdx.x, tid = threadIdx.x, lane = tid & 63;
  const int w = rfl(tid >> 6);
#define PROBE(n)
  PROBE(0);
  const unsigned long long cbase = code_base();
  int i, j; map_block(b, i, j);
  const __amdgpu_buffer_rsrc_t rws = mk_rsrc(p.ws, WS_END);
  ST_DECLQ;
  phase_qkv(p, lds, lds0, cbase, b, i, j, w, lane ST_ARGQ);
  PROBE(1);
  const unsigned epoch = rfl(*(const LAS unsigned*)(lds + K1_EP));
  ST_DECL; STAMP(0);
  const int l15 = lane & 15, g = lane >> 4;
  const unsigned l16 = lane * 16;
  constexpr unsigned OOB = 0xF0000000u;
  const int t32 = 2 * i + (j >> 2), c4 = j & 3, tt0 = 2 * t32;
  const int sid = XMAP == 1 ? (((i & 3) << 1) | (j >> 2)) : XMAP == 2 ? (i & 7) : ((i & 15) >> 1);
  const int rot14 = (sid * 7) >> 2, rot16 = 2 * sid, rotc = XMAP == 1 ? ((i & 3) * 3) >> 1 : (i & 7) % 6;
  auto pcol = [&](int s) { int u = s + rotc; u = u >= 6 ? u - 6 : u; int c = j + 1 + u; return c >= 7 ? c - 7 : c; };
  const u32x4 sentv = {SENT, SENT, SENT, SENT};

  if (w >= 8) {
    const int lw = w - 8;
    if (lw == 0 && lane == 0) __hip_atomic_store((GAS unsigned*)(p.ws + WS_FLG + (i * 8 + j) * 128), epoch, __ATOMIC_RELAXED, __HIP_MEMORY_SCOPE_AGENT);
    { const int t = i - 4 + (lane >> 3);
      if (lw == 0) wait_flags(p, lane < 48 && t >= 0 && t < 32, WS_FLG + (t * 8 + (lane & 7)) * 128, epoch);
      else wait_flags(p, lane < 32, WS_FLG + (lane * 8 + 7) * 128, epoch); }
    STAMP(6);
    BARS();
#pragma unroll
    for (int q = 0; q < 5; ++q) { const int n = 2 * q + lw, hh = n / 5, kt = 4 * i - 16 + 4 * (n % 5);
      dma4(rws, l16, WS_K + ((2 * j + hh) * 128 + (kt < 0 ? 0 : kt)) * KB, lds0 + L_K + n * 4 * KB); }
#pragma unroll
    for (int q = 0; q < 5; ++q) { const int n = 2 * q + lw, hh = n / 5, kb = 2 * i - 8 + 2 * (n % 5);
      dma4(rws, l16, WS_V + ((2 * j + hh) * 64 + (kb < 0 ? 0 : kb)) * 2 * KB, lds0 + L_V + n * 4 * KB); }
    STAMP(1); WAIT_VM(20); STAMP(2); BARS();
    auto wo_slot = [&](int q) { const int n = 2 * q + lw; const unsigned src = WS_WO + (c4 * 16 + ((n + rot16) & 15)) * 7 * KB, dst = lds0 + (n <= 10 ? L_WOA + n * 7 * KB : L_WOB + (n - 11) * 7 * KB);
      dma4(rws, l16, src, dst); dma3(rws, l16, src + 4 * KB, dst + 4 * KB); };
    wo_slot(0); wo_slot(1);
    STAMP(3); WAIT_VM(14); STAMP(4); BARS(); STAMP(5);
    fill_late(rws, i, j, lw, lane);
    FENCE();
#pragma unroll
    for (int q = 2; q < 8; ++q) wo_slot(q);
    WAIT_VM(0); STAMP(7); BARS(); STAMP(8);
    auto w_stage = [&](unsigned wsrc, int q0, int q1) {
#pragma unroll
      for (int q = q0; q < q1; ++q) { const int n = 2 * q + lw; int ks = n + rot14; ks = ks >= 14 ? ks - 14 : ks; const unsigned src = wsrc + (c4 * 14 + ks) * 7 * KB, dst = lds0 + L_W + n * 7 * KB;
        dma4(rws, l16, src, dst); dma3(rws, l16, src + 4 * KB, dst + 4 * KB); } };
    BARS(); STAMP(9);
    w_stage(WS_W1, 0, 2);
    BARS();
    w_stage(WS_W1, 2, 7);
    STAMP(10); WAIT_VM(0); STAMP(11); BARS(); STAMP(12);
    BARS(); STAMP(13);
    w_stage(WS_W2, 0, 2);
    BARS();
    w_stage(WS_W2, 2, 7);
    STAMP(14); WAIT_VM(0); STAMP(15); BARS(); STAMP(16);
    BARS(); STAMP(17);
    if (j == 7) { BARS(); BARS(); ST_FLUSH(1, p.ws, b, w); ST_FLUSHQ(p.ws, b, w); return; }
    const __amdgpu_buffer_rsrc_t rw3 = mk_rsrc(p.w3, D * F * 2), rw4 = mk_rsrc(p.w4, F * D * 2);
    unsigned v3[2][4], v4[4];
#pragma unroll
    for (int k = 0; k < 4; ++k) {
#pragma unroll
      for (int e = 0; e < 2; ++e) { const int rr = lane >> 5, f = (2 * (k & 1) + rr) | (e << 2); v3[e][k] = rr * 3584 + (((lane & 31) ^ (f << 1)) * 16) + k * 6144; }
      const int rr = lane >> 3; v4[k] = rr * 896 + (((lane & 7) ^ (((rr >> 1) & 3) << 1)) * 16) + k * 6144;
    }
    auto w3_round = [&](int r, unsigned slot) { int ks = 2 * r + lw + rot14; ks = ks >= 14 ? ks - 14 : ks;
#pragma unroll
      for (int u = 0; u < 4; ++u) dma4v(rw3, v3[u & 1], (ks * 32 + 8 * u) * 3584 + j * 512, lds0 + slot + (lw * 16 + u * 4) * KB); };
    auto w4_step = [&](int s, unsigned slot) { const int c = s < 6 ? pcol(s) : j;
#pragma unroll
      for (int u = 0; u < 4; ++u) dma4v(rw4, v4, (256 * j + 128 * lw + 32 * u) * 896 + c * 128, lds0 + slot + (lw * 16 + u * 4) * KB); };
    auto arrive = [&](int s) { if (lw == 0 && lane == 0) __hip_atomic_store((GAS unsigned*)(p.ws + WS_PF + ((i * 7 + pcol(s)) * 8 + j) * 128), epoch, __ATOMIC_RELAXED, __HIP_MEMORY_SCOPE_AGENT); };
    w3_round(0, L_RA);
    BARS();
    w3_round(1, L_RB); w3_round(2, L_RC);
    BARS();
    STAMP(18); WAIT_VM(0); STAMP(19); BARS();
    BARS(); STAMP(20);
    BARS();
    w3_round(3, L_RA); w3_round(4, L_RB);
    BARS();
    BARS();
    WAIT_VM(16); BARS(); w3_round(5, L_RC);
    WAIT_VM(16); BARS(); w3_round(6, L_RA);
    WAIT_VM(16); BARS(); w4_step(0, L_RB);
    WAIT_VM(16); BARS(); w4_step(1, L_RC);
    BARS(); w4_step(2, L_RA); w4_step(3, L_RD);
    STAMP(21); WAIT_VM(16); STAMP(22); BARS();
    BARS(); w4_step(4, L_RB);
    BARS(); w4_step(5, L_RC);
    WAIT_VM(32); BARS(); w4_step(6, L_RA);
    WAIT_VM(32); BARS(); arrive(0);
    WAIT_VM(16); BARS(); arrive(1);
    WAIT_VM(0); BARS(); arrive(2);
    BARS(); arrive(3); arrive(4); arrive(5);
    if (b == 0 && lw == 0) {
#pragma unroll
      for (int q = 0; q < 4; ++q) wait_flags(p, true, WS_FLG + (q * 64 + lane) * 128, epoch);
      if (lane == 0) __hip_atomic_store((GAS unsigned*)(p.ws + WS_EP), epoch, __ATOMIC_RELAXED, __HIP_MEMORY_SCOPE_AGENT);
    }
    STAMP(23); ST_FLUSH(1, p.ws, b, w); ST_FLUSHQ(p.ws, b, w);
    return;
  }

  const int hl = w >> 2, a = w & 3, head = 2 * j + hl, tt = 4 * i + a;
  const bool act = w < 7;
  const int wf = act ? w : 6, Fg = 7 * c4 + wf;
  u32x4 qf; u32x2 xr[2]; u32x4 cs1u[2], cs3u[2][2];
  const u32x4 ones = {ONES, ONES, ONES, ONES};
  unsigned act_off = act ? (((tt0 + (g & 1)) * 14 + (Fg >> 1)) * 64 + (2 * (Fg & 1) + (g >> 1)) * 16 + l15) * 16 : OOB;
  const int ka = (2 * w + rot14) % 14, kb14 = (2 * w + 1 + rot14) % 14;
  unsigned pv0 = l16 + ka * KB, pv1 = l16 + kb14 * KB, ov0 = l16 + ((2 * w + rot16) & 15) * KB, ov1 = l16 + ((2 * w + 1 + rot16) & 15) * KB;
  unsigned zv[2];
#pragma unroll
  for (int q = 0; q < 2; ++q) { const int ks = q ? kb14 : ka, f0 = 32 * ks + 8 * g, sh = f0 < 112 ? -1 : f0 < 224 ? 1 : f0 < 336 ? -2 : 2, tz = 16 * (tt0 + 1) + l15 + sh;
    zv[q] = (((tz >> 4) * 14 + ks) * 64 + g * 16 + (tz & 15)) * 16; }
  u32x4 selA, selB;
  { const unsigned v = g == (l15 >> 2) ? ((l15 & 1) ? 0x3F800000u : 0x00003F80u) : 0u, d0 = (l15 & 2) ? 0u : v, d1 = (l15 & 2) ? v : 0u;
    selA = u32x4{d0, d1, 0u, 0u}; selB = u32x4{0u, 0u, d0, d1}; }
  unsigned ooff = ((tt * 16 + head) * 64 + ((g >> 1) + 2 * (g & 1)) * 16 + l15) * 16 + WS_O;
  asm volatile("" : "+v"(act_off), "+v"(pv0), "+v"(pv1), "+v"(ov0), "+v"(ov1), "+v"(zv[0]), "+v"(zv[1]), "+v"(selA), "+v"(selB), "+v"(ooff));
  { const __amdgpu_buffer_rsrc_t rx = mk_rsrc(p.x, S * D * 2);
#pragma unroll
    for (int t = 0; t < 2; ++t) aload64(xr[t], rx, ((16 * (tt0 + t) + l15) * D + 16 * Fg + 4 * g) * 2, 0); }
  BARS();
  aload128_sc1(qf, rws, l16, WS_Q + (head * 128 + tt) * KB);
#pragma unroll
  for (int hf = 0; hf < 2; ++hf) { aload128_sc1(cs1u[hf], rws, (16 * Fg + 4 * g) * 4, WS_CS + hf * 2240 * 4);
#pragma unroll
    for (int f = 0; f < 2; ++f) aload128_sc1(cs3u[hf][f], rws, (448 + 256 * (j < 7 ? j : 6) + 32 * w + 16 * f + 4 * g) * 4, WS_CS + hf * 2240 * 4); }
  WAIT_VM(0); RETIRE(qf); RETIRE(xr[0]); RETIRE(xr[1]); RETIRE(cs1u[0]); RETIRE(cs1u[1]); RETIRE(cs3u[0][0]); RETIRE(cs3u[0][1]); RETIRE(cs3u[1][0]); RETIRE(cs3u[1][1]);
  const f32x4 cs1 = __builtin_bit_cast(f32x4, cs1u[0]) + __builtin_bit_cast(f32x4, cs1u[1]);
  f32x4 cs3[2];
#pragma unroll
  for (int f = 0; f < 2; ++f) cs3[f] = __builtin_bit_cast(f32x4, cs3u[0][f]) + __builtin_bit_cast(f32x4, cs3u[1][f]);
  STAMP(1);
  BARS();
  PROBE(2);

  u32x4 opiece;
  {
    const int par = a & 1, kl0 = hl * 20 + (a & ~1), vl0 = hl * 20 + (a >> 1) * 2;
    f32x4 sc[18];
    const LAS char* kp = lds + L_K + kl0 * KB + l16;
    const f32x4 zero4 = {0.f, 0.f, 0.f, 0.f};
    lds_pipe<18, 8>([&](int n) { return lds128(kp + n * KB); }, [&](int n, u32x4 kf) { sc[n] = mfma16(kf, qf, zero4); }, [](int) {});
    const int dq = l15 - 4 * g;
    const float NEG = -__builtin_inff();
    if (par == 0) {
#pragma unroll
      for (int r = 0; r < 4; ++r) { const bool ge = dq >= r; sc[0][r] = ge ? NEG : sc[0][r]; sc[16][r] = ge ? sc[16][r] : NEG; sc[17][r] = NEG; }
    } else {
#pragma unroll
      for (int r = 0; r < 4; ++r) { const bool ge = dq >= r; sc[1][r] = ge ? NEG : sc[1][r]; sc[17][r] = ge ? sc[17][r] : NEG; sc[0][r] = NEG; }
    }
    const int nneg = 16 - (tt & ~1);
    if (nneg > 0) {
#pragma unroll
      for (int n = 0; n < 16; ++n) if (n < nneg) sc[n] = f32x4{NEG, NEG, NEG, NEG};
    }
    float m = max3(sc[0][0], sc[0][1], sc[0][2]), m2 = max3(sc[0][3], sc[1][0], sc[1][1]);
    m = max3(m, sc[1][2], sc[1][3]);
#pragma unroll
    for (int n = 2; n < 18; ++n) { m = max3(m, sc[n][0], sc[n][1]); m2 = max3(m2, sc[n][2], sc[n][3]); }
    m = gmax(max3(m, m2, m2));
    const float cexp = 0.18898223650461363f * 1.4426950408889634f, mc = -m * cexp;
    STAMP(14);
    BARS();
    STAMP(15);
    f32x4 o0 = zero4, o1 = zero4;
    const LAS char* vp = lds + L_V + vl0 * KB + l16;
    u32x4 v0 = lds128(vp), v1 = lds128(vp + KB);
#pragma unroll
    for (int mm = 0; mm < 9; ++mm) {
      u32x4 n0, n1;
      if (mm < 8) { n0 = lds128(vp + (2 * mm + 2) * KB); n1 = lds128(vp + (2 * mm + 3) * KB); }
      u32x4 pf;
#pragma unroll
      for (int q = 0; q < 4; ++q) { const f32x4& sv = sc[2 * mm + (q >> 1)];
        pf[q] = pk2(__builtin_amdgcn_exp2f(fmaf(sv[2 * (q & 1)], cexp, mc)), __builtin_amdgcn_exp2f(fmaf(sv[2 * (q & 1) + 1], cexp, mc))); }
      o0 = mfma16(v0, pf, o0);
      o1 = mfma16(v1, pf, o1);
      if (mm < 8) { v0 = n0; v1 = n1; }
      SB();
    }
    const float rl = __builtin_amdgcn_rcpf(gsum(g == 3 ? o1[0] : 0.f));
    opiece = pair_tiles(u32x2{pk2(o0[0] * rl, o0[1] * rl), pk2(o0[2] * rl, o0[3] * rl)}, u32x2{pk2(o1[0] * rl, o1[1] * rl), pk2(o1[2] * rl, o1[3] * rl)});
  }
  STAMP(16);
  BARS();
  bst128<16>(opiece, rws, ooff);
  u32x4 wr[16];
#pragma unroll
  for (int n = 0; n < 16; ++n) wr[n] = lds128(lds + (n <= 10 ? L_WOA + n * 7 * KB : L_WOB + (n - 11) * 7 * KB) + wf * KB + l16);
  STAMP(2);
  HWAIT(HX0);

  { u32x4 v[2][2];
    fetch_payload<2, 16>(v, rws, ov0, ov1, WS_O + tt0 * 16 * KB, 16 * KB, lds, lds0, L_O + 2 * w * KB, lane, [] { BARS();   }, p.ws + (40u << 20)); }
  STAMP(3);
  BARS();
  PROBE(3);
  u32x2 hp[2];
  {
    f32x4 acc[2];
#pragma unroll
    for (int t = 0; t < 2; ++t) acc[t] = mfma16(selA, u32x4{xr[t][0], xr[t][1], 0u, 0u}, f32x4{0.f, 0.f, 0.f, 0.f});
    const LAS char* op = lds + L_O + l16;
    lds_pipe<32, 8>([&](int n) { return lds128(op + ((n & 1) * 16 + (n >> 1)) * KB); }, [&](int n, u32x4 f) { acc[n & 1] = mfma16(wr[n >> 1], f, acc[n & 1]); }, [](int) {});
#pragma unroll
    for (int t = 0; t < 2; ++t) hp[t] = u32x2{pk2(acc[t][0], acc[t][1]), pk2(acc[t][2], acc[t][3])};
    bst128<16>(pair_tiles(hp[0], hp[1]), rws, act_off + WS_H);
  }
  STAMP(4);
  HWAIT(HX1);

  LAS float* st4 = (LAS float*)(lds + L_ST4);
  auto stage_weights = [&] { BARS();
#pragma unroll
    for (int n = 0; n < 14; ++n) wr[n] = lds128(lds + L_W + (n * 7 + wf) * KB + l16);
    BARS(); };
  { u32x4 v[2][2] = {};
    if (act) fetch_payload<2, 14>(v, rws, pv0, pv1, WS_H + tt0 * 14 * KB, 14 * KB, lds, lds0, L_H + 2 * w * KB, lane, stage_weights, p.ws + (40u << 20) + 4); else stage_weights();
    row_stats<2>(v, ones, st4, w, l15, g); }
  STAMP(5);
  BARS();
  f32x2 yv[2][2];
  {
    const LAS char* hpz = lds + L_H + l16;
    f32x4 pt[2];
#pragma unroll
    for (int t = 0; t < 2; ++t) pt[t] = *(const LAS f32x4*)(st4 + ((t * 16 + l15) * 8 + 2 * g) * 2);
    f32x4 acc[2] = {};
    lds_pipe<28, 8>([&](int n) { return lds128(hpz + ((n & 1) * 14 + (n >> 1)) * KB); }, [&](int n, u32x4 f) { acc[n & 1] = mfma16(wr[n >> 1], f, acc[n & 1]); }, [](int) {});
    u32x2 zp[2];
#pragma unroll
    for (int t = 0; t < 2; ++t) {
      const float s1 = gsum(pt[t][0] + pt[t][2]), s2 = gsum(pt[t][1] + pt[t][3]);
      const float mean = s1 * (1.f / D), var = fmaxf(s2 * (1.f / D) - mean * mean, 0.f);
      const float r1 = __builtin_amdgcn_rsqf(var + 1e-5f);
      const float r12 = r1 * __builtin_amdgcn_rsqf(var * r1 * r1 + 1e-5f);
      const f32x2 m2 = {mean, mean};
#pragma unroll
      for (int q = 0; q < 2; ++q) {
        yv[t][q] = (f32x2{bflo(hp[t][q]), bfhi(hp[t][q])} - m2) * f32x2{r1, r1};
        zp[t][q] = gelu2((f32x2{acc[t][2 * q], acc[t][2 * q + 1]} - m2 * f32x2{cs1[2 * q], cs1[2 * q + 1]}) * f32x2{r12, r12});
      }
    }
    bst128<16>(pair_tiles(zp[0], zp[1]), rws, act_off + WS_Z + 14 * KB);
  }
  STAMP(6);
  HWAIT(HX2);

  { u32x4 v[2][2];
    if (act) fetch_payload<2, 14>(v, rws, zv[0], zv[1], WS_Z, 14 * KB, lds, lds0, L_Z + 2 * w * KB, lane, stage_weights, p.ws + (40u << 20) + 8); else stage_weights(); }
  STAMP(7);
  BARS();
  {
    f32x4 acc[2] = {};
    const LAS char* zb = lds + L_Z + l16;
    lds_pipe<28, 8>([&](int n) { return lds128(zb + ((n & 1) * 14 + (n >> 1)) * KB); }, [&](int n, u32x4 f) { acc[n & 1] = mfma16(wr[n >> 1], f, acc[n & 1]); }, [](int) {});
    BARS();
    u32x2 xp[2];
#pragma unroll
    for (int t = 0; t < 2; ++t) xp[t] = u32x2{pk2(yv[t][0][0] + acc[t][0], yv[t][0][1] + acc[t][1]), pk2(yv[t][1][0] + acc[t][2], yv[t][1][1] + acc[t][3])};
    bst128<16>(pair_tiles(xp[0], xp[1]), rws, act_off + WS_X1);
  }
  STAMP(8);
  if (j == 7) { ST_FLUSH(1, p.ws, b, w); ST_FLUSHQ(p.ws, b, w); return; }
  HWAIT(HX3);

  LAS float* st6 = (LAS float*)(lds + L_ST6);
  const int q4 = l15 >> 2, pp = lane & 3;
  const LAS char* a3[2];
#pragma unroll
  for (int f = 0; f < 2; ++f) a3[f] = lds + (8 * g + q4) * 512 + (((2 * w + f) ^ (q4 | ((g & 1) << 2))) * 32) + pp * 8;
  auto w3frag = [&](unsigned off, int f) { const u32x2 lo = tr_read(a3[f] + off), hi = tr_read(a3[f] + off + 2048); return u32x4{lo[0], lo[1], hi[0], hi[1]}; };
  u32x4 areg[4][2];
  { u32x4 v[4][2] = {};
    auto w3_regs = [&] { BARS();
#pragma unroll
      for (int n = 0; n < 4; ++n)
#pragma unroll
        for (int f = 0; f < 2; ++f) areg[n][f] = w3frag(n * 16 * KB, f);
      BARS(); };
    if (act) fetch_payload<4, 14>(v, rws, pv0, pv1, WS_X1 + 4 * i * 14 * KB, 14 * KB, lds, lds0, L_X1 + 2 * w * KB, lane, w3_regs, p.ws + (40u << 20) + 12); else w3_regs();
    row_stats<4>(v, ones, st6, w, l15, g); }
  STAMP(9);
  BARS();
  PROBE(4);
  {
    const LAS char* xp = lds + L_X1 + l16;
    f32x4 pt[4];
#pragma unroll
    for (int t = 0; t < 4; ++t) pt[t] = *(const LAS f32x4*)(st6 + ((t * 16 + l15) * 8 + 2 * g) * 2);
    f32x4 acc[2][4] = {};
    u32x4 bfr[2][4], afr[2][2];
    auto load_k = [&](int n) { const unsigned slot = ((n >> 1) % 3 == 2 ? L_RC : (n >> 1) % 3 == 0 ? L_RA : L_RB) + (n & 1) * 16 * KB;
#pragma unroll
      for (int f = 0; f < 2; ++f) afr[n & 1][f] = n < 4 ? areg[n][f] : w3frag(slot, f);
#pragma unroll
      for (int t = 0; t < 4; ++t) bfr[n & 1][t] = lds128(xp + (t * 14 + n) * KB); };
    load_k(0);
    SB();
#pragma unroll
    for (int n = 0; n < 14; ++n) {
      if (n & 1) BARS();
      if (n < 13) load_k(n + 1);
#pragma unroll
      for (int t = 0; t < 4; ++t) { acc[0][t] = mfma16(afr[n & 1][0], bfr[n & 1][t], acc[0][t]); acc[1][t] = mfma16(afr[n & 1][1], bfr[n & 1][t], acc[1][t]); }
#pragma unroll
      for (int q = 0; q < 4; ++q) { __builtin_amdgcn_sched_group_barrier(0x8, 1, 0); __builtin_amdgcn_sched_group_barrier(0x100, 2, 0); }
      __builtin_amdgcn_sched_group_barrier(0x8, 4, 0);
      SB();
    }
    STAMP(20);
    float mean[4], gis[4], gc1[4], gc2[4];
#pragma unroll
    for (int t = 0; t < 4; ++t) {
      const float s1 = gsum(pt[t][0] + pt[t][2]), s2 = gsum(pt[t][1] + pt[t][3]);
      mean[t] = s1 * (1.f / D);
      const float ve = fmaxf(s2 * (1.f / D) - mean[t] * mean[t], 0.f) + 1e-5f, rs = __builtin_amdgcn_rsqf(ve);
      gis[t] = ve * rs; gc1[t] = -2.3022082f * rs; gc2[t] = -0.10294324f * rs * rs * rs;
    }
#pragma unroll
    for (int t = 0; t < 4; ++t) {
      u32x4 hv;
#pragma unroll
      for (int q = 0; q < 4; ++q) { const int f = q >> 1, r = 2 * (q & 1);
        hv[q] = gelu2s(f32x2{acc[f][t][r], acc[f][t][r + 1]} - f32x2{mean[t], mean[t]} * f32x2{cs3[f][r], cs3[f][r + 1]}, gc1[t], gc2[t], gis[t]); }
      *(LAS u32x4*)(lds + L_HID + (t * 8 + w) * KB + l16) = hv;
    }
  }
  STAMP(10);
  BARS();
  STAMP(11);
  const int tb = w & 1, ft = w >> 1;
  u32x4 hf[2][8];
#pragma unroll
  for (int ks = 0; ks < 8; ++ks)
#pragma unroll
    for (int t = 0; t < 2; ++t) hf[t][ks] = lds128(lds + L_HID + ((2 * tb + t) * 8 + ks) * KB + l16);
  const unsigned pbase = l16 + WS_P + ((i * 49 + j) * 8 + w) * KB;
  const int Fo = 4 * j + ft;
  u32x2 hres[2], xres[2];
  unsigned ro[2];
#pragma unroll
  for (int t = 0; t < 2; ++t) { ro[t] = (((4 * i + 2 * tb + t) * 14 + (Fo >> 1)) * 64 + (2 * (Fo & 1) + (g >> 1)) * 16 + l15) * 16 + (g & 1) * 8;
    aload64_sc1(hres[t], rws, ro[t], WS_H); aload64_sc1(xres[t], rws, ro[t], WS_X1); }
  f32x4 acc[2] = {};
  {
    constexpr int PD = 4;
    constexpr unsigned slots[7] = {L_RB, L_RC, L_RA, L_RD, L_RB, L_RC, L_RA};
    const LAS char* wp = lds + (4 * g + q4) * 128 + ((ft ^ ((2 * g + (q4 >> 1)) & 3)) * 32) + pp * 8;
    auto w4frag = [&](unsigned off) { const u32x2 lo = tr_read(wp + off), hi = tr_read(wp + off + 2048); return u32x4{lo[0], lo[1], hi[0], hi[1]}; };
    u32x4 buf[PD + 1];
#pragma unroll
    for (int m = 0; m < PD; ++m) buf[m] = w4frag(slots[0] + m * 4096);
    SB();
#pragma unroll
    for (int m = 0; m < 56; ++m) {
      const int s = m >> 3, ks = m & 7;
      if (ks == 8 - PD && s < 6) { if (s >= 3) WAIT_VM(2); BARS(); }
      if (m + PD < 56) buf[(m + PD) % (PD + 1)] = w4frag(slots[(m + PD) >> 3] + ((m + PD) & 7) * 4096);
      SB();
      acc[0] = mfma16(buf[m % (PD + 1)], hf[0][ks], acc[0]); acc[1] = mfma16(buf[m % (PD + 1)], hf[1][ks], acc[1]);
      if (ks == 7 && s < 6) {
        bst128<16>(u32x4{pk2(acc[0][0], acc[0][1]), pk2(acc[0][2], acc[0][3]), pk2(acc[1][0], acc[1][1]), pk2(acc[1][2], acc[1][3])}, rws, pbase + pcol(s) * 56 * KB);
        acc[0] = f32x4{0.f, 0.f, 0.f, 0.f}; acc[1] = acc[0];
      }
      SB();
    }
  }
  STAMP(12);
  PROBE(5);
  {
    const __amdgpu_buffer_rsrc_t rout = mk_rsrc(p.out, S * D * 2);
    u32x4 pv[6];
    const unsigned pin = WS_P + ((i * 7 + j) * 7 * 8 + w) * KB;
    WAIT_VM(0); RETIRE(hres[0]); RETIRE(hres[1]); RETIRE(xres[0]); RETIRE(xres[1]);
    BARS();
    for (unsigned spin = 0; spin < SPIN_MAX; ++spin) {
      const unsigned m = max(max(max(hres[0][0], hres[0][1]), max(xres[0][0], xres[0][1])), max(max(hres[1][0], hres[1][1]), max(xres[1][0], xres[1][1])));
      if (!__any(m == SENT)) break;
#pragma unroll
      for (int t = 0; t < 2; ++t) { aload64_sc1(hres[t], rws, ro[t], WS_H); aload64_sc1(xres[t], rws, ro[t], WS_X1); }
      WAIT_VM(0); RETIRE(hres[0]); RETIRE(hres[1]); RETIRE(xres[0]); RETIRE(xres[1]);
    }
    wait_flags(p, lane < 7 && lane != j, WS_PF + ((i * 7 + j) * 8 + lane) * 128, epoch);
#pragma unroll
    for (int s = 0; s < 6; ++s) { int js = j + 1 + s; js = js >= 7 ? js - 7 : js; aload128_sc1(pv[s], rws, l16, pin + js * 8 * KB); }
    WAIT_VM(0);
#pragma unroll
    for (int s = 0; s < 6; ++s) RETIRE(pv[s]);
    const u32x4 selAB = selA | selB;
#pragma unroll
    for (int s = 0; s < 6; ++s) { acc[0] = mfma16(selA, pv[s], acc[0]); acc[1] = mfma16(selB, pv[s], acc[1]); }
#pragma unroll
    for (int t = 0; t < 2; ++t) {
      acc[t] = mfma16(selAB, u32x4{hres[t][0], hres[t][1], xres[t][0], xres[t][1]}, acc[t]);
      __builtin_amdgcn_raw_buffer_store_b64(u32x2{pk2(acc[t][0], acc[t][1]), pk2(acc[t][2], acc[t][3])}, rout, ((16 * (4 * i + 2 * tb + t) + l15) * D + 16 * Fo + 4 * g) * 2, 0, 0);
    }
  }
  PROBE(6);
  STAMP(13);
  ST_FLUSH(1, p.ws, b, w); ST_FLUSHQ(p.ws, b, w);
}

extern "C" void kernel_launch(void* const* d_in, const int* in_sizes, int n_in, void* d_out, int out_size, void* d_ws, size_t ws_size, hipStream_t stream) {
  Params p;
  p.x = (const u16*)d_in[0]; p.wqkv = (const u16*)d_in[1]; p.wo = (const u16*)d_in[2];
  p.w1 = (const u16*)d_in[4]; p.w2 = (const u16*)d_in[5]; p.w3 = (const u16*)d_in[6]; p.w4 = (const u16*)d_in[7];
  p.out = (u16*)d_out; p.ws = (char*)d_ws;
  (void)hipFuncSetAttribute((const void*)block_fused, hipFuncAttributeMaxDynamicSharedMemorySize, K2_LDS);
  block_fused<<<256, 640, K2_LDS, stream>>>(p);
}
```

```cpp
#include <hip/hip_runtime.h>
#include <hip/hip_bf16.h>
#include <stdint.h>

typedef unsigned short u16;
typedef __attribute__((ext_vector_type(8))) __bf16 bf16x8;
typedef __attribute__((ext_vector_type(2))) __bf16 bf16x2;
typedef __attribute__((ext_vector_type(4))) short s16x4;
typedef __attribute__((ext_vector_type(4))) float f32x4;
typedef __attribute__((ext_vector_type(16))) float f32x16;
typedef __attribute__((ext_vector_type(4))) unsigned u32x4;
typedef __attribute__((ext_vector_type(2))) unsigned u32x2;
#define LAS __attribute__((address_space(3)))
#define GAS __attribute__((address_space(1)))
#define DI __device__ __forceinline__

constexpr int S = 2048, D = 448, NH = 16, DH = 28, F = 1792;
constexpr unsigned SENT = 0xFFFFFFFFu;

constexpr unsigned KB = 1024;
constexpr unsigned WS_Q   = 0;
constexpr unsigned WS_K   = WS_Q + 2048 * KB;
constexpr unsigned WS_V   = WS_K + 2048 * KB;
constexpr unsigned WS_WO  = WS_V + 2048 * KB;
constexpr unsigned WS_W1  = WS_WO + 448 * KB;
constexpr unsigned WS_W2  = WS_W1 + 392 * KB;
constexpr unsigned WS_O   = WS_W2 + 392 * KB;
constexpr unsigned WS_H   = WS_O + 2048 * KB;
constexpr unsigned WS_X1  = WS_H + 1792 * KB;
constexpr unsigned WS_Z   = WS_X1 + 1792 * KB;
constexpr unsigned WS_P   = WS_Z + 1820 * KB;
constexpr unsigned WS_CS  = WS_P + 32 * 49 * 8 * KB;
constexpr unsigned WS_PF  = WS_CS + 18 * KB;
constexpr unsigned WS_FLG = WS_PF + 224 * KB;
constexpr unsigned WS_EP  = WS_FLG + 32 * KB;
constexpr unsigned WS_END = WS_EP + KB;

struct Params {
  const u16* x; const u16* wqkv; const u16* wo; const u16* w1; const u16* w2; const u16* w3; const u16* w4;
  u16* out; char* ws;
};

DI __amdgpu_buffer_rsrc_t mk_rsrc(const void* p, unsigned bytes) { return __builtin_amdgcn_make_buffer_rsrc((void*)p, (short)0, (int)bytes, 0x00020000); }
DI void dma16(__amdgpu_buffer_rsrc_t rs, unsigned voff, unsigned soff, unsigned ldsaddr) {
  asm volatile("s_mov_b32 m0, %0\n\ts_nop 0\n\tbuffer_load_dwordx4 %1, %2, %3 offen lds" :: "s"(ldsaddr), "v"(voff), "s"(rs), "s"(soff) : "memory");
}
DI void dma16_sc1(__amdgpu_buffer_rsrc_t rs, unsigned voff, unsigned soff, unsigned ldsaddr) {
  asm volatile("s_mov_b32 m0, %0\n\ts_nop 0\n\tbuffer_load_dwordx4 %1, %2, %3 offen sc1 lds" :: "s"(ldsaddr), "v"(voff), "s"(rs), "s"(soff) : "memory");
}
DI void dma4(__amdgpu_buffer_rsrc_t rs, unsigned voff, unsigned soff, unsigned ldsaddr) {
  asm volatile("s_mov_b32 m0, %0\n\ts_nop 0\n\tbuffer_load_dwordx4 %1, %2, %3 offen sc1 lds\n\tbuffer_load_dwordx4 %1, %2, %3 offen offset:1024 sc1 lds\n\t"
               "buffer_load_dwordx4 %1, %2, %3 offen offset:2048 sc1 lds\n\tbuffer_load_dwordx4 %1, %2, %3 offen offset:3072 sc1 lds" :: "s"(ldsaddr), "v"(voff), "s"(rs), "s"(soff) : "memory");
}
DI void dma3(__amdgpu_buffer_rsrc_t rs, unsigned voff, unsigned soff, unsigned ldsaddr) {
  asm volatile("s_mov_b32 m0, %0\n\ts_nop 0\n\tbuffer_load_dwordx4 %1, %2, %3 offen sc1 lds\n\tbuffer_load_dwordx4 %1, %2, %3 offen offset:1024 sc1 lds\n\t"
               "buffer_load_dwordx4 %1, %2, %3 offen offset:2048 sc1 lds" :: "s"(ldsaddr), "v"(voff), "s"(rs), "s"(soff) : "memory");
}
DI void dma4v(__amdgpu_buffer_rsrc_t rs, const unsigned (&v)[4], unsigned soff, unsigned ldsaddr) {
  asm volatile("s_mov_b32 m0, %0\n\ts_nop 0\n\tbuffer_load_dwordx4 %1, %5, %6 offen lds\n\tbuffer_load_dwordx4 %2, %5, %6 offen offset:1024 lds\n\t"
               "buffer_load_dwordx4 %3, %5, %6 offen offset:2048 lds\n\tbuffer_load_dwordx4 %4, %5, %6 offen offset:3072 lds"
               :: "s"(ldsaddr), "v"(v[0]), "v"(v[1]), "v"(v[2]), "v"(v[3]), "s"(rs), "s"(soff) : "memory");
}
#define WAIT_VM(n) asm volatile("s_waitcnt vmcnt(" #n ")" ::: "memory")
#define BAR() asm volatile("s_waitcnt lgkmcnt(0)\n\ts_barrier" ::: "memory")
#define BARS() do { __builtin_amdgcn_sched_barrier(0); BAR(); __builtin_amdgcn_sched_barrier(0); } while (0)
DI unsigned rfl(unsigned v) { return __builtin_amdgcn_readfirstlane(v); }
DI float bflo(unsigned u) { return __uint_as_float(u << 16); }
DI float bfhi(unsigned u) { return __uint_as_float(u & 0xffff0000u); }
DI unsigned pk2(float a, float b) { bf16x2 v; v[0] = (__bf16)a; v[1] = (__bf16)b; return __builtin_bit_cast(unsigned, v); }
DI float dot2(unsigned a, unsigned b, float c) { return __builtin_amdgcn_fdot2_f32_bf16(__builtin_bit_cast(bf16x2, a), __builtin_bit_cast(bf16x2, b), c, false); }
DI f32x16 mfma32(u32x4 a, u32x4 b, f32x16 c) { return __builtin_amdgcn_mfma_f32_32x32x16_bf16(__builtin_bit_cast(bf16x8, a), __builtin_bit_cast(bf16x8, b), c, 0, 0, 0); }
DI f32x4 mfma16(u32x4 a, u32x4 b, f32x4 c) { return __builtin_amdgcn_mfma_f32_16x16x32_bf16(__builtin_bit_cast(bf16x8, a), __builtin_bit_cast(bf16x8, b), c, 0, 0, 0); }
DI u32x2 tr_read(const LAS char* p) { return __builtin_bit_cast(u32x2, __builtin_amdgcn_ds_read_tr16_b64_v4i16((LAS s16x4*)p)); }
DI u32x4 lds128(const LAS char* p) { return *(const LAS u32x4*)p; }
template <int AUX> DI void bst128(u32x4 v, __amdgpu_buffer_rsrc_t rs, unsigned off) { __builtin_amdgcn_raw_buffer_store_b128(v, rs, off, 0, AUX); }
#define XJ j
constexpr unsigned ONES = 0x3F803F80u;
#define AUX_SIDE 16
#define AUX_QKV 16
#define ST_DECL
#define ST_PARAM
#define ST_DECLQ
#define ST_ARGQ
#define ST_FLUSHQ(ws, b, w)
#define CSTAMP(n)
#define STAMP(n)
#define ST_FLUSH(kern, ws, b, w)

DI void aload128(u32x4& d, __amdgpu_buffer_rsrc_t rs, unsigned voff, unsigned soff) { asm volatile("buffer_load_dwordx4 %0, %1, %2, %3 offen" : "=v"(d) : "v"(voff), "s"(rs), "s"(soff) : "memory"); }
DI void aload64(u32x2& d, __amdgpu_buffer_rsrc_t rs, unsigned voff, unsigned soff) { asm volatile("buffer_load_dwordx2 %0, %1, %2, %3 offen" : "=v"(d) : "v"(voff), "s"(rs), "s"(soff) : "memory"); }
DI void aload128_sc1(u32x4& d, __amdgpu_buffer_rsrc_t rs, unsigned voff, unsigned soff) { asm volatile("buffer_load_dwordx4 %0, %1, %2, %3 offen sc1" : "=v"(d) : "v"(voff), "s"(rs), "s"(soff) : "memory"); }
DI void aload64_sc1(u32x2& d, __amdgpu_buffer_rsrc_t rs, unsigned voff, unsigned soff) { asm volatile("buffer_load_dwordx2 %0, %1, %2, %3 offen sc1" : "=v"(d) : "v"(voff), "s"(rs), "s"(soff) : "memory"); }
#define RETIRE(x) asm volatile("; RETIRE %0" : "+v"(x))
constexpr unsigned CODE_BYTES = 50 * 1024;
DI unsigned touch_code(unsigned long long base, unsigned bytes, int b, int lane, bool on) {
  const __amdgpu_buffer_rsrc_t rc = mk_rsrc((const void*)base, bytes);
  const unsigned per = (bytes / 128 + 31) / 32, line = (b >> 3) * per + lane;
  unsigned d;
  asm volatile("buffer_load_dword %0, %1, %2, 0 offen" : "=v"(d) : "v"(on && lane < (int)per ? line * 128 : 0xF0000000u), "s"(rc) : "memory");
  return d;
}
#define PFS 128
template <unsigned BYTES> DI void touch_input(unsigned& d, const void* base, int b, int lane) {
  constexpr unsigned PART = (BYTES / 256 + PFS - 1) / PFS * PFS;
  const __amdgpu_buffer_rsrc_t r = mk_rsrc(base, BYTES);
#pragma unroll
  for (unsigned o = 0; o < PART; o += 64 * PFS) asm volatile("buffer_load_dword %0, %1, %2, %3 offen" : "+v"(d) : "v"(o + lane * PFS < PART ? lane * PFS : 0xF0000000u), "s"(r), "s"(b * PART + o) : "memory");
}
DI void touch_proj(unsigned& d, const Params& p, int b, int lane) {
  const int x = b & 7, s = b >> 3, e = x & 1, tile0 = (x >> 1) * 8;
  const __amdgpu_buffer_rsrc_t rw = mk_rsrc(p.wqkv, D * 3 * D * 2), rx = mk_rsrc(p.x, S * D * 2);
#pragma unroll
  for (int q = 0; q < 2; ++q) { const int n = q * 64 + lane, row = 128 + 10 * s + n / 12, r = n % 12;
    asm volatile("buffer_load_dword %0, %1, %2, 0 offen" : "+v"(d) : "v"(n < 120 && (e == 0 || (r & 3) < 3) ? row * 2688 + (r >> 2) * 896 + (4 * e + (r & 3)) * 128 : 0xF0000000u), "s"(rw) : "memory"); }
#pragma unroll
  for (int q = 0; q < 2; ++q) { const int n = q * 64 + lane;
    asm volatile("buffer_load_dword %0, %1, %2, 0 offen" : "+v"(d) : "v"(n < 80 ? (64 * tile0 + 16 * s + n / 5) * 896 + (2 + n % 5) * 128 : 0xF0000000u), "s"(rx) : "memory"); }
}
DI unsigned long long code_base() { unsigned long long pc; asm volatile("s_getpc_b64 %0" : "=s"(pc)); return pc & ~0xFFull; }

#define HX0 8
#define HX1 8
#define HX2 12
#define HX3 10
#define HWAIT(x) do { WAIT_VM(0); if ((x) > 0) __builtin_amdgcn_s_sleep(x); } while (0)
#define ROTQ 0
#define XMAP 2
DI void map_block(int b, int& i, int& j) { const int x = b & 7, s = b >> 3; constexpr int JPX = 8 / XMAP; i = (x / XMAP) * 4 * XMAP + s / JPX; j = (x % XMAP) * JPX + s % JPX; }

constexpr unsigned K1_XS = 0;
constexpr unsigned K1_W0 = 7 * 8192;
constexpr unsigned K1_SLOT = 24576;
constexpr unsigned K1_ST = K1_W0 + 4 * K1_SLOT;
constexpr unsigned K1_CS = K1_ST + 512;
constexpr unsigned K1_EP = K1_CS + 2 * 2 * 3 * 32 * 4;

DI void retile_load(const Params& p, int mat, int ks, int cb, int lane, u32x4 (&v)[4]) {
  const u16* W = mat == 0 ? p.wo : mat == 1 ? p.w1 : mat == 2 ? p.w2 : p.w3;
  const unsigned ldw = mat == 3 ? F : D, nrows = D, krow0 = (mat == 0 ? 28 : 32) * ks;
#pragma unroll
  for (int q = 0; q < 4; ++q) {
    unsigned row = krow0 + (lane >> 3) + 8 * q; row = row < nrows ? row : nrows - 1;
    v[q] = *(const u32x4*)(W + (size_t)row * ldw + cb * 64 + (lane & 7) * 8);
  }
}
template <bool SUMS>
DI void retile_store(const Params& p, LAS char* scr, int mat, int ks, int cb, int lane, const u32x4 (&v)[4], f32x4 (&cs)[4]) {
#pragma unroll
  for (int q = 0; q < 4; ++q) *(LAS u32x4*)(scr + q * 1024 + lane * 16) = v[q];
  const int g = lane >> 4, q4 = (lane & 15) >> 2, pp = lane & 3;
  const int r0 = 8 * g + q4, r1 = 8 * g + 4 + q4;
  const __amdgpu_buffer_rsrc_t rws = mk_rsrc(p.ws, WS_END);
  const u32x4 ones = {ONES, ONES, ONES, ONES};
#pragma unroll
  for (int ft = 0; ft < 4; ++ft) {
    u32x2 lo = tr_read(scr + r0 * 128 + ft * 32 + pp * 8), hi = tr_read(scr + r1 * 128 + ft * 32 + pp * 8);
    if (mat == 0 && g == 3) { hi[0] = 0u; hi[1] = 0u; }
    const u32x4 f = {lo[0], lo[1], hi[0], hi[1]};
    const int Fi = 4 * cb + ft;
    unsigned dst;
    if (mat == 0) dst = WS_WO + (((Fi / 7) * 16 + ks) * 7 + Fi % 7) * KB;
    else dst = (mat == 1 ? WS_W1 : WS_W2) + (((Fi / 7) * 14 + ks) * 7 + Fi % 7) * KB;
    if (mat != 3) bst128<AUX_SIDE>(f, rws, lane * 16 + dst);
    if (SUMS) cs[ft] = mfma16(f, ones, cs[ft]);
  }
}

DI float gsum(float v) {
  const auto a = __builtin_amdgcn_permlane16_swap(__float_as_uint(v), __float_as_uint(v), false, false); const float s = __uint_as_float(a[0]) + __uint_as_float(a[1]);
  const auto c = __builtin_amdgcn_permlane32_swap(__float_as_uint(s), __float_as_uint(s), false, false); return __uint_as_float(c[0]) + __uint_as_float(c[1]);
}
DI int qk_slot(int n0, int& half, bool& last) {
  const int head = n0 / 28, hs = 28 * head, bnd = (hs / 64 + 1) * 64;
  const bool split = bnd < hs + 28, second = split && n0 >= bnd;
  const int seg0 = second ? bnd : hs, seg1 = (split && !second) ? bnd : hs + 28, off = n0 - seg0;
  half = (off >> 2) & 1;
  last = n0 + 4 == seg1 && half == 0;
  return 4 * head + (second ? (bnd - hs + 7) >> 3 : 0) + (off >> 3);
}
DI void fill_early(__amdgpu_buffer_rsrc_t rws, int i, int j, int lw, int lane) {
  const u32x4 sv = {SENT, SENT, SENT, SENT};
  const unsigned l16 = lane * 16, tt0 = 4 * i + 2 * (j >> 2), F0 = 7 * (j & 3);
#pragma unroll
  for (int t = 0; t < 2; ++t) { const unsigned o = WS_O + ((4 * i + 2 * lw + t) * 16 + 2 * j) * KB + l16; bst128<16>(sv, rws, o); bst128<16>(sv, rws, o + KB); }
  bst128<16>(sv, rws, lane < 28 ? WS_Z + ((tt0 + lw + 1) * 14 + (F0 >> 1)) * KB + (F0 & 1) * 512 + (lane >> 1) * 256 + (lw * 14 + (lane & 1)) * 16 : 0xF0000000u);
}
DI void fill_late(__amdgpu_buffer_rsrc_t rws, int i, int j, int lw, int lane) {
  const u32x4 sv = {SENT, SENT, SENT, SENT};
  const unsigned l16 = lane * 16, tt0 = 4 * i + 2 * (j >> 2), F0 = 7 * (j & 3);
  const unsigned o = ((tt0 + lw) * 14 + (F0 >> 1)) * KB + (F0 & 1) * 512 + l16;
#pragma unroll
  for (int u = 0; u < 3; ++u) { const unsigned base = u == 0 ? WS_H : u == 1 ? WS_X1 : WS_Z + 14 * KB;
    bst128<16>(sv, rws, o + base); bst128<16>(sv, rws, o + base + KB); bst128<16>(sv, rws, o + base + 2 * KB); bst128<16>(sv, rws, lane < 32 ? o + base + 3 * KB : 0xF0000000u); }
}

DI void phase_qkv(const Params& p, LAS char* lds, unsigned lds0, unsigned long long cbase, int b, int i, int j, int w, int lane ST_PARAM) {
  STAMP(0);
  unsigned epv = 0, cwarm = 0, twarm = 0;
  auto errands = [&] {
    asm volatile("buffer_load_dword %0, %1, %2, 0 offen sc1" : "=v"(epv) : "v"(WS_EP), "s"(mk_rsrc(p.ws, WS_END)) : "memory");
    cwarm = touch_code(cbase, CODE_BYTES, b, lane, true);
  };
  auto errands_done = [&] { WAIT_VM(0); RETIRE(epv); RETIRE(cwarm); RETIRE(twarm); if (lane == 0) *(LAS unsigned*)(lds + K1_EP) = epv + 1u; };

  if (j == 7) {
    const __amdgpu_buffer_rsrc_t rws = mk_rsrc(p.ws, WS_END);
    if (w >= 8) {
      const int lw = w - 8;
      if (lw == 1) { errands(); errands_done(); }
      else { unsigned d = 0; touch_input<D * 3 * D * 2>(d, p.wqkv, b, lane); touch_input<S * D * 2>(d, p.x, b, lane); touch_input<D * F * 2>(d, p.w4, b, lane); WAIT_VM(0); RETIRE(d); }
      fill_early(rws, i, j, lw, lane);
#pragma unroll
      for (int n = 0; n < 4; ++n) { const int pr = (lw * 4 + n) * 4 + (lane >> 4), gq = (lane >> 2) & 3, row = lane & 3; const unsigned o = row == 0 ? ONES : 0u;
        bst128<16>(u32x4{o, o, o, o}, rws, WS_V + ((((pr >> 1) * 64 + 2 * i + (pr & 1)) * 2 + 1) * 64 + gq * 16 + 12 + row) * 16); }
      if (i == 0 || i == 31) {
#pragma unroll
        for (int n = 0; n < 7; ++n) bst128<16>(u32x4{0u, 0u, 0u, 0u}, rws, WS_Z + ((i == 0 ? 0 : 129) * 14 + lw * 7 + n) * KB + lane * 16); }
    } else {
    const int colid = w < 2 ? w * 32 + i : (w == 2 && i < 6) ? 64 + i : -1;
    const int nid = w >= 3 ? i * 5 + (w - 3) : 160 + (i - 6);
    u32x4 v[7][4];
    auto other = [&](int m, int& mat, int& ks, int& cb) { if (m < 112) { mat = 0; ks = m / 7; cb = m % 7; } else { mat = 2; ks = (m - 112) / 7; cb = (m - 112) % 7; } };
    const int ccol = colid >> 1, chalf = colid & 1, cmat = ccol < 7 ? 1 : 3, ccb = ccol < 7 ? ccol : ccol - 7;
    f32x4 cs[4] = {};
    if (colid >= 0) {
#pragma unroll
      for (int q = 0; q < 7; ++q) retile_load(p, cmat, 7 * chalf + q, ccb, lane, v[q]);
#pragma unroll
      for (int q = 0; q < 7; ++q) retile_store<true>(p, lds + (2 * w + (q & 1)) * 4096, cmat, 7 * chalf + q, ccb, lane, v[q], cs);
      if ((lane & 15) == 0) {
#pragma unroll
        for (int ft = 0; ft < 4; ++ft) bst128<16>(__builtin_bit_cast(u32x4, cs[ft]), rws, WS_CS + (chalf * 2240 + (cmat == 1 ? 0 : 448) + 64 * ccb + 16 * ft + 4 * (lane >> 4)) * 4); }
    } else {
#pragma unroll
      for (int q = 0; q < 2; ++q) { const int m = nid + 186 * q; if (m < 210) { int mat, ks, cb; other(m, mat, ks, cb); retile_load(p, mat, ks, cb, lane, v[q]); } }
#pragma unroll
      for (int q = 0; q < 2; ++q) { const int m = nid + 186 * q; if (m < 210) { int mat, ks, cb; other(m, mat, ks, cb); retile_store<false>(p, lds + (2 * w + (q & 1)) * 4096, mat, ks, cb, lane, v[q], cs); } }
    }
    }
    STAMP(7);
    WAIT_VM(0); BARS();
    return;
  }
  if (w >= 4 && w < 8) {
    unsigned pfd = 0;
    if (w == 6) {
 touch_input<D * 3 * D * 2>(pfd, p.wqkv, b, lane); touch_input<S * D * 2>(pfd, p.x, b, lane);
 touch_input<D * F * 2>(pfd, p.w4, b, lane); }
    if (w == 7) errands();
    const int tok = 16 * (w - 4) + (lane & 15), part = lane >> 4;
    const LAS char* xb[2];
#pragma unroll
    for (int q = 0; q < 2; ++q) xb[q] = lds + K1_XS + tok * 128 + ((2 * part + q) ^ ((tok >> 1) & 7)) * 16;
    float s1a = 0.f, s1b = 0.f, s2a = 0.f, s2b = 0.f;
    BARS();
    if (w == 5) touch_proj(pfd, p, b, lane);
#pragma unroll
    for (int c = 0; c < 7; ++c) {
      u32x4 v[2];
#pragma unroll
      for (int q = 0; q < 2; ++q) v[q] = lds128(xb[q] + c * 8192);
      __builtin_amdgcn_sched_barrier(0);
#pragma unroll
      for (int q = 0; q < 2; ++q)
#pragma unroll
        for (int u = 0; u < 4; ++u) { float lo = bflo(v[q][u]), hi = bfhi(v[q][u]); asm volatile("" : "+v"(lo), "+v"(hi));
          s1a += lo; s1b += hi; s2a = fmaf(lo, lo, s2a); s2b = fmaf(hi, hi, s2b); }
      asm volatile("" : "+v"(s1a), "+v"(s1b), "+v"(s2a), "+v"(s2b));
      if (c == 4 && w == 5) { WAIT_VM(0); RETIRE(pfd); }
      if (c == 2 && w >= 6) {
        WAIT_VM(0); RETIRE(pfd);
        if (w == 7) errands_done();
        fill_early(mk_rsrc(p.ws, WS_END), i, j, w - 6, lane);
      }
      if (c == 6) {
        const float a1 = gsum(s1a + s1b), a2 = gsum(s2a + s2b);
        const float mean = a1 * (1.f / D), var = fmaxf(a2 * (1.f / D) - mean * mean, 0.f);
        if (lane < 16) { *(LAS float*)(lds + K1_ST + tok * 4) = mean; *(LAS float*)(lds + K1_ST + 256 + tok * 4) = __builtin_amdgcn_rsqf(var + 1e-5f); }
      }
      BARS();
    }
    BARS();
    STAMP(7);
    WAIT_VM(0); BARS();
    return;
  }
  if (w >= 8) {
    const int lw = w - 8;
    const __amdgpu_buffer_rsrc_t rx = mk_rsrc(p.x, S * D * 2), rw = mk_rsrc(p.wqkv, D * 3 * D * 2);
    const unsigned xv0 = (lane >> 3) * 896 + ((lane & 7) ^ (lane >> 4)) * 16, xv1 = (lane >> 3) * 896 + ((lane & 7) ^ (lane >> 4) ^ 4) * 16;
    const unsigned wv = (lane >> 3) * 2688 + (((lane & 7) ^ (((lane >> 4) & 1) * 4)) * 16);
    const int rotq = ROTQ;
    auto issue_chunk = [&](int c) {
      int cc = c + rotq; cc = cc >= 7 ? cc - 7 : cc;
#pragma unroll
      for (int q = 0; q < 4; ++q) { const unsigned n = lw * 4 + q; dma16(rx, (q & 1) ? xv1 : xv0, (64 * i + 8 * n) * 896 + cc * 128, lds0 + K1_XS + c * 8192 + n * 1024); }
#pragma unroll
      for (int q = 0; q < 12; ++q) { const unsigned n = lw * 12 + q, type = n >> 3, rg = n & 7;
        dma16(rw, wv, (64 * cc + 8 * rg) * 2688 + type * 896 + j * 128, lds0 + K1_W0 + (c & 3) * K1_SLOT + type * 8192 + rg * 1024); }
    };
    issue_chunk(0); issue_chunk(1);
    STAMP(1);
    WAIT_VM(16); STAMP(2); BARS();
    issue_chunk(2); issue_chunk(3);
    WAIT_VM(32); STAMP(3); BARS();
    issue_chunk(4);
    WAIT_VM(32); STAMP(8); BARS();
    issue_chunk(5);
    WAIT_VM(32); STAMP(9); BARS();
    issue_chunk(6);
    WAIT_VM(32); STAMP(10); BARS();
    WAIT_VM(16); STAMP(11); BARS();
    WAIT_VM(0); STAMP(5); BARS();
    BAR();
    BARS();
    STAMP(7);
    WAIT_VM(0); BARS();
    return;
  }
  const int tb = w & 1, e = w >> 1, r = lane & 31, h = lane >> 5;
  f32x16 aq = {}, ak = {}, av = {};
  f32x4 cq = {}, ck = {}, cv = {};
  const unsigned cs1 = ((lane & 15) == ((lane >> 4) & 1)) ? ONES : 0u;
  const u32x4 csel = {cs1, cs1, cs1, cs1};
  const int q4 = (lane & 15) >> 2, pp = lane & 3, blk = (lane >> 4) & 1;
  const LAS char* xb[4];
#pragma unroll
  for (int kl = 0; kl < 4; ++kl) xb[kl] = lds + K1_XS + (32 * tb + r) * 128 + ((2 * (kl ^ tb) + h) ^ ((r >> 1) & 7)) * 16;
  const unsigned wb = K1_W0 + (8 * h + q4) * 128 + (((8 * e + 4 * blk + pp) ^ (8 * ((q4 >> 1) & 1))) * 8);
  const LAS char* we = lds + wb + tb * 2048;
  const LAS char* wo = lds + wb + (1 - tb) * 2048;
  u32x4 xf[3], f[3][3];
  auto load_step = [&](int n) {
    xf[n % 3] = lds128(xb[n & 3] + (n >> 2) * 8192);
    const LAS char* wp = ((n & 1) ? wo : we) + ((n >> 2) & 3) * K1_SLOT + (n & 2) * 2048;
#pragma unroll
    for (int t = 0; t < 3; ++t) { const u32x2 lo = tr_read(wp + t * 8192), hi = tr_read(wp + t * 8192 + 512); f[n % 3][t] = u32x4{lo[0], lo[1], hi[0], hi[1]}; }
  };
  const __amdgpu_buffer_rsrc_t rws = mk_rsrc(p.ws, WS_END);
  constexpr unsigned K1_IMG = K1_W0 + 3 * K1_SLOT;
  int g0half; bool g0last; const int G0 = qk_slot(64 * j, g0half, g0last);
  unsigned qdst[4], zdst[4], voff;
  {
#pragma unroll
    for (int g4 = 0; g4 < 4; ++g4) { int half; bool last; const int G = qk_slot(64 * j + 32 * e + 8 * g4 + 4 * h, half, last);
      qdst[g4] = K1_IMG + (G - G0) * KB + ((2 * tb + (r >> 4)) * 16 + (r & 15)) * 16 + half * 8;
      zdst[g4] = last ? qdst[g4] + 8 : K1_IMG + 2 * 10 * KB + lane * 8;
      asm volatile("" : "+v"(qdst[g4]), "+v"(zdst[g4])); }
    const unsigned n = 64 * j + 32 * e + r, head = n / 28, d = n - 28 * head;
    voff = WS_V + (((head * 64 + 2 * i + tb) * 2 + (d >> 4)) * 64 + h * 16 + (d & 15)) * 16; asm volatile("" : "+v"(voff)); }
  STAMP(1);
  BARS();
  STAMP(2);
  load_step(0);
#pragma unroll
  for (int n = 0; n < 28; ++n) {
    if ((n & 3) == 3 && n < 27) { if (n == 7) STAMP(8); if (n == 11) STAMP(10); BARS(); if (n == 7) { STAMP(9); CSTAMP(13); } if (n == 11) { STAMP(11); CSTAMP(14); } if (n == 15) STAMP(12); }
    if (n < 27) load_step(n + 1);
    aq = mfma32(f[n % 3][0], xf[n % 3], aq);
    ak = mfma32(f[n % 3][1], xf[n % 3], ak);
    av = mfma32(xf[n % 3], f[n % 3][2], av);
    if (!(n & 1)) {
      cq = mfma16(f[n % 3][0], csel, cq); ck = mfma16(f[n % 3][1], csel, ck); cv = mfma16(f[n % 3][2], csel, cv);
    }
    __builtin_amdgcn_sched_group_barrier(0x8, 1, 0); __builtin_amdgcn_sched_group_barrier(0x100, 3, 0);
    __builtin_amdgcn_sched_group_barrier(0x8, 1, 0); __builtin_amdgcn_sched_group_barrier(0x100, 2, 0);
    __builtin_amdgcn_sched_group_barrier(0x8, 1, 0); __builtin_amdgcn_sched_group_barrier(0x100, 2, 0);
    __builtin_amdgcn_sched_group_barrier(0x8, 3, 0);
    __builtin_amdgcn_sched_barrier(0);
  }
  STAMP(4);
  LAS float* cs = (LAS float*)(lds + K1_CS) + e * 192;
  if ((lane & 15) < 2) { LAS float* o = cs + tb * 96 + 16 * (lane & 1) + 4 * (lane >> 4);
    *(LAS f32x4*)o = cq; *(LAS f32x4*)(o + 32) = ck; *(LAS f32x4*)(o + 64) = cv; }
  BAR();
  STAMP(5);
  const LAS float* st = (const LAS float*)(lds + K1_ST);
  f32x4 cqk[2][4][2], m4[4], r4[4];
#pragma unroll
  for (int t = 0; t < 2; ++t)
#pragma unroll
    for (int g4 = 0; g4 < 4; ++g4) { cqk[t][g4][0] = *(const LAS f32x4*)(cs + t * 32 + 8 * g4 + 4 * h); cqk[t][g4][1] = *(const LAS f32x4*)(cs + 96 + t * 32 + 8 * g4 + 4 * h); }
  const float mean = st[32 * tb + r], rstd = st[64 + 32 * tb + r];
  const float cs2 = cs[64 + r] + cs[96 + 64 + r];
#pragma unroll
  for (int g4 = 0; g4 < 4; ++g4) { m4[g4] = *(const LAS f32x4*)(st + 32 * tb + 8 * g4 + 4 * h); r4[g4] = *(const LAS f32x4*)(st + 64 + 32 * tb + 8 * g4 + 4 * h); }
  __builtin_amdgcn_sched_barrier(0);
#pragma unroll
  for (int g4 = 0; g4 < 4; ++g4)
#pragma unroll
    for (int t = 0; t < 2; ++t) {
      const f32x16& a = t ? ak : aq;
      float v[4];
#pragma unroll
      for (int q = 0; q < 4; ++q) v[q] = rstd * (a[4 * g4 + q] - mean * (cqk[t][g4][0][q] + cqk[t][g4][1][q]));
      *(LAS u32x2*)(lds + qdst[g4] + t * 10 * KB) = u32x2{pk2(v[0], v[1]), pk2(v[2], v[3])};
      *(LAS u32x2*)(lds + zdst[g4] + (zdst[g4] < K1_IMG + 2 * 10 * KB ? t * 10 * KB : 0)) = u32x2{0u, 0u};
    }
  {
    float v[16];
#pragma unroll
    for (int g4 = 0; g4 < 4; ++g4)
#pragma unroll
      for (int q = 0; q < 4; ++q) v[4 * g4 + q] = r4[g4][q] * (av[4 * g4 + q] - m4[g4][q] * cs2);
    const u32x4 oa = {pk2(v[0], v[1]), pk2(v[2], v[3]), pk2(v[8], v[9]), pk2(v[10], v[11])};
    const u32x4 ob = {pk2(v[4], v[5]), pk2(v[6], v[7]), pk2(v[12], v[13]), pk2(v[14], v[15])};
    bst128<AUX_QKV>(oa, rws, voff);
    bst128<AUX_QKV>(ob, rws, voff + 512);
  }
  BARS();
  { int lh; bool ll; const int NS = qk_slot(64 * j + 60, lh, ll) - G0 + 1;
#pragma unroll
    for (int q = 0; q < 3; ++q) { const int ls = w + 4 * q;
      if (ls < NS) { const int G = G0 + ls; const unsigned o = WS_Q + (((G >> 2) * 128 + 4 * i + (lane >> 4)) * 64 + (G & 3) * 16 + (lane & 15)) * 16;
        bst128<AUX_QKV>(lds128(lds + K1_IMG + ls * KB + lane * 16), rws, o);
        bst128<AUX_QKV>(lds128(lds + K1_IMG + (10 + ls) * KB + lane * 16), rws, o + (WS_K - WS_Q)); } }
  }
  STAMP(6);
  WAIT_VM(0);
  STAMP(7);
  WAIT_VM(0); BARS();
}

constexpr unsigned L_K = 0, L_WOA = 40 * KB, L_V = 120 * KB, L_WOB = 0;
constexpr unsigned L_O = 128 * KB;
constexpr unsigned L_W = 0;
constexpr unsigned L_H = 98 * KB, L_ST4 = 126 * KB;
constexpr unsigned L_Z = 128 * KB;
constexpr unsigned L_X1 = 96 * KB, L_ST6 = 152 * KB;
constexpr unsigned L_HID = 96 * KB;
constexpr unsigned L_RA = 0, L_RB = 32 * KB, L_RC = 64 * KB, L_RD = 128 * KB;
constexpr unsigned K2_LDS = 163840;
constexpr unsigned SPIN_MAX = 1u << 16;

DI u32x4 bload(__amdgpu_buffer_rsrc_t rs, unsigned voff, unsigned soff) { return __builtin_amdgcn_raw_buffer_load_b128(rs, voff, soff, 0); }
DI u32x4 bload_sc1(__amdgpu_buffer_rsrc_t rs, unsigned voff, unsigned soff) { return __builtin_amdgcn_raw_buffer_load_b128(rs, voff, soff, 16); }
DI void bstore_sc1(u32x4 v, __amdgpu_buffer_rsrc_t rs, unsigned voff, unsigned soff) { bst128<16>(v, rs, voff + soff); }
DI unsigned umax4(u32x4 v) { return max(max(v[0], v[1]), max(v[2], v[3])); }
#define FENCE() asm volatile("" ::: "memory")
typedef __attribute__((ext_vector_type(2))) float f32x2_;
DI float gelu_tanh(float x) {
  const float e = __builtin_amdgcn_exp2f(x * fmaf(x * x, -0.10294324f, -2.3022082f));
  return x * __builtin_amdgcn_rcpf(1.f + e);
}
DI u32x4 pair_tiles(u32x2 a, u32x2 b) {
  const auto s0 = __builtin_amdgcn_permlane16_swap(a[0], b[0], false, false), s1 = __builtin_amdgcn_permlane16_swap(a[1], b[1], false, false);
  return u32x4{s0[0], s1[0], s0[1], s1[1]};
}
template <int NT, int NS, class F>
DI void fetch_payload(u32x4 (&v)[NT][2], __amdgpu_buffer_rsrc_t rs, unsigned voff0, unsigned voff1, unsigned soff, unsigned tstride, LAS char* lds, unsigned lds0, unsigned dst, int lane, F fill, void* dbg = nullptr) {
  for (unsigned spin = 0;; ++spin) {
#pragma unroll
    for (int t = 0; t < NT; ++t) { dma16_sc1(rs, voff0, soff + t * tstride, lds0 + dst + t * NS * KB); dma16_sc1(rs, voff1, soff + t * tstride, lds0 + dst + (t * NS + 1) * KB); }
    if (spin == 0) { fill(); __builtin_amdgcn_sched_barrier(0); }
    WAIT_VM(0);
    unsigned m = 0;
#pragma unroll
    for (int t = 0; t < NT; ++t)
#pragma unroll
      for (int q = 0; q < 2; ++q) { v[t][q] = lds128(lds + dst + (t * NS + q) * KB + lane * 16); m = max(m, max(max(v[t][q][0], v[t][q][1]), max(v[t][q][2], v[t][q][3]))); }
    if (!__any(m == SENT) || spin > SPIN_MAX) break;
  }
}
template <int NT> DI void row_stats(const u32x4 (&v)[NT][2], u32x4 ones, LAS float* stp, int w, int l15, int g) {
#pragma unroll
  for (int t = 0; t < NT; ++t) {
    f32x4 d1 = {}, d2 = {};
#pragma unroll
    for (int q = 0; q < 2; ++q) { d1 = __builtin_amdgcn_mfma_f32_16x16x32_bf16(__builtin_bit_cast(bf16x8, ones), __builtin_bit_cast(bf16x8, v[t][q]), d1, 0, 0, 0);
                                  d2 = __builtin_amdgcn_mfma_f32_16x16x32_bf16(__builtin_bit_cast(bf16x8, v[t][q]), __builtin_bit_cast(bf16x8, v[t][q]), d2, 0, 0, 0); }
    const float lo = (l15 & 1) ? d2[1] : d2[0], hi = (l15 & 1) ? d2[3] : d2[2], s2 = (l15 & 2) ? hi : lo;
    if (g == (l15 >> 2)) *(LAS f32x2_*)(stp + ((t * 16 + l15) * 8 + w) * 2) = f32x2_{d1[0], s2};
  }
}

#define SB() __builtin_amdgcn_sched_barrier(0)
typedef __attribute__((ext_vector_type(2))) float f32x2;
DI float max3(float a, float b, float c) { float r; asm("v_max3_f32 %0, %1, %2, %3" : "=v"(r) : "v"(a), "v"(b), "v"(c)); return r; }
DI float gmax(float v) {
  const auto a = __builtin_amdgcn_permlane16_swap(__float_as_uint(v), __float_as_uint(v), false, false); const float s = max3(__uint_as_float(a[0]), __uint_as_float(a[1]), __uint_as_float(a[1]));
  const auto c = __builtin_amdgcn_permlane32_swap(__float_as_uint(s), __float_as_uint(s), false, false); return max3(__uint_as_float(c[0]), __uint_as_float(c[1]), __uint_as_float(c[1]));
}
DI unsigned gelu2(f32x2 x) {
  const f32x2 t = x * (x * x * f32x2{-0.10294324f, -0.10294324f} + f32x2{-2.3022082f, -2.3022082f});
  const f32x2 d = f32x2{__builtin_amdgcn_exp2f(t[0]), __builtin_amdgcn_exp2f(t[1])} + f32x2{1.f, 1.f};
  const f32x2 r = x * f32x2{__builtin_amdgcn_rcpf(d[0]), __builtin_amdgcn_rcpf(d[1])};
  return pk2(r[0], r[1]);
}
DI unsigned gelu2s(f32x2 a, float c1, float c2, float is) {
  const f32x2 t = a * (a * a * f32x2{c2, c2} + f32x2{c1, c1});
  const f32x2 d = f32x2{__builtin_amdgcn_exp2f(t[0]), __builtin_amdgcn_exp2f(t[1])} * f32x2{is, is} + f32x2{is, is};
  const f32x2 r = a * f32x2{__builtin_amdgcn_rcpf(d[0]), __builtin_amdgcn_rcpf(d[1])};
  return pk2(r[0], r[1]);
}
template <int N, int PD, class L, class U, class M> DI void lds_pipe(L load, U use, M mid) {
  u32x4 buf[PD + 1];
#pragma unroll
  for (int n = 0; n < PD; ++n) buf[n] = load(n);
  SB();
#pragma unroll
  for (int n = 0; n < N; ++n) {
    if (n + PD < N) buf[(n + PD) % (PD + 1)] = load(n + PD);
    SB();
    use(n, buf[n % (PD + 1)]);
    mid(n);
    SB();
  }
}

DI void wait_flags(const Params& p, bool on, unsigned off, unsigned epoch) {
  const GAS unsigned* f = (const GAS unsigned*)(p.ws + (on ? off : WS_EP));
  for (unsigned spin = 0; spin < SPIN_MAX; ++spin) {
    const unsigned v = __hip_atomic_load(f, __ATOMIC_RELAXED, __HIP_MEMORY_SCOPE_AGENT);
    if (__all(!on || v == epoch)) break;
    __builtin_amdgcn_s_sleep(2);
  }
}

__global__ void __launch_bounds__(640, 3) block_fused(Params p) {
  extern __shared__ __attribute__((aligned(16))) char smem[];
  LAS char* lds = (LAS char*)smem;
  const unsigned lds0 = (unsigned)(size_t)lds;
  const int b = blockIdx.x, tid = threadIdx.x, lane = tid & 63;
  const int w = rfl(tid >> 6);
#define PROBE(n)
  PROBE(0);
  const unsigned long long cbase = code_base();
  int i, j; map_block(b, i, j);
  const __amdgpu_buffer_rsrc_t rws = mk_rsrc(p.ws, WS_END);
  ST_DECLQ;
  phase_qkv(p, lds, lds0, cbase, b, i, j, w, lane ST_ARGQ);
  PROBE(1);
  const unsigned epoch = rfl(*(const LAS unsigned*)(lds + K1_EP));
  ST_DECL; STAMP(0);
  const int l15 = lane & 15, g = lane >> 4;
  const unsigned l16 = lane * 16;
  constexpr unsigned OOB = 0xF0000000u;
  const int t32 = 2 * i + (j >> 2), c4 = j & 3, tt0 = 2 * t32;
  const int sid = XMAP == 1 ? (((i & 3) << 1) | (j >> 2)) : XMAP == 2 ? (i & 7) : ((i & 15) >> 1);
  const int rot14 = (sid * 7) >> 2, rot16 = 2 * sid, rotc = XMAP == 1 ? ((i & 3) * 3) >> 1 : (i & 7) % 6;
  auto pcol = [&](int s) { int u = s + rotc; u = u >= 6 ? u - 6 : u; int c = j + 1 + u; return c >= 7 ? c - 7 : c; };
  const u32x4 sentv = {SENT, SENT, SENT, SENT};

  if (w >= 8) {
    const int lw = w - 8;
    if (lw == 0 && lane == 0) __hip_atomic_store((GAS unsigned*)(p.ws + WS_FLG + (i * 8 + j) * 128), epoch, __ATOMIC_RELAXED, __HIP_MEMORY_SCOPE_AGENT);
    { const int t = i - 4 + (lane >> 3);
      if (lw == 0) wait_flags(p, lane < 48 && t >= 0 && t < 32, WS_FLG + (t * 8 + (lane & 7)) * 128, epoch);
      else wait_flags(p, lane < 32, WS_FLG + (lane * 8 + 7) * 128, epoch); }
    STAMP(6);
    BARS();
#pragma unroll
    for (int q = 0; q < 5; ++q) { const int n = 2 * q + lw, hh = n / 5, kt = 4 * i - 16 + 4 * (n % 5);
      dma4(rws, l16, WS_K + ((2 * j + hh) * 128 + (kt < 0 ? 0 : kt)) * KB, lds0 + L_K + n * 4 * KB); }
#pragma unroll
    for (int q = 0; q < 5; ++q) { const int n = 2 * q + lw, hh = n / 5, kb = 2 * i - 8 + 2 * (n % 5);
      dma4(rws, l16, WS_V + ((2 * j + hh) * 64 + (kb < 0 ? 0 : kb)) * 2 * KB, lds0 + L_V + n * 4 * KB); }
    STAMP(1); WAIT_VM(20); STAMP(2); BARS();
    auto wo_slot = [&](int q) { const int n = 2 * q + lw; const unsigned src = WS_WO + (c4 * 16 + ((n + rot16) & 15)) * 7 * KB, dst = lds0 + (n <= 10 ? L_WOA + n * 7 * KB : L_WOB + (n - 11) * 7 * KB);
      dma4(rws, l16, src, dst); dma3(rws, l16, src + 4 * KB, dst + 4 * KB); };
    wo_slot(0); wo_slot(1);
    STAMP(3); WAIT_VM(14); STAMP(4); BARS(); STAMP(5);
    fill_late(rws, i, j, lw, lane);
    FENCE();
#pragma unroll
    for (int q = 2; q < 8; ++q) wo_slot(q);
    WAIT_VM(0); STAMP(7); BARS(); STAMP(8);
    auto w_stage = [&](unsigned wsrc, int q0, int q1) {
#pragma unroll
      for (int q = q0; q < q1; ++q) { const int n = 2 * q + lw; int ks = n + rot14; ks = ks >= 14 ? ks - 14 : ks; const unsigned src = wsrc + (c4 * 14 + ks) * 7 * KB, dst = lds0 + L_W + n * 7 * KB;
        dma4(rws, l16, src, dst); dma3(rws, l16, src + 4 * KB, dst + 4 * KB); } };
    BARS(); STAMP(9);
    w_stage(WS_W1, 0, 2);
    BARS();
    w_stage(WS_W1, 2, 7);
    STAMP(10); WAIT_VM(0); STAMP(11); BARS(); STAMP(12);
    BARS(); STAMP(13);
    w_stage(WS_W2, 0, 2);
    BARS();
    w_stage(WS_W2, 2, 7);
    STAMP(14); WAIT_VM(0); STAMP(15); BARS(); STAMP(16);
    BARS(); STAMP(17);
    if (j == 7) { BARS(); BARS(); ST_FLUSH(1, p.ws, b, w); ST_FLUSHQ(p.ws, b, w); return; }
    const __amdgpu_buffer_rsrc_t rw3 = mk_rsrc(p.w3, D * F * 2), rw4 = mk_rsrc(p.w4, F * D * 2);
    unsigned v3[2][4], v4[4];
#pragma unroll
    for (int k = 0; k < 4; ++k) {
#pragma unroll
      for (int e = 0; e < 2; ++e) { const int rr = lane >> 5, f = (2 * (k & 1) + rr) | (e << 2); v3[e][k] = rr * 3584 + (((lane & 31) ^ (f << 1)) * 16) + k * 6144; }
      const int rr = lane >> 3; v4[k] = rr * 896 + (((lane & 7) ^ (((rr >> 1) & 3) << 1)) * 16) + k * 6144;
    }
    auto w3_round = [&](int r, unsigned slot) { int ks = 2 * r + lw + rot14; ks = ks >= 14 ? ks - 14 : ks;
#pragma unroll
      for (int u = 0; u < 4; ++u) dma4v(rw3, v3[u & 1], (ks * 32 + 8 * u) * 3584 + j * 512, lds0 + slot + (lw * 16 + u * 4) * KB); };
    auto w4_step = [&](int s, unsigned slot) { const int c = s < 6 ? pcol(s) : j;
#pragma unroll
      for (int u = 0; u < 4; ++u) dma4v(rw4, v4, (256 * j + 128 * lw + 32 * u) * 896 + c * 128, lds0 + slot + (lw * 16 + u * 4) * KB); };
    auto arrive = [&](int s) { if (lw == 0 && lane == 0) __hip_atomic_store((GAS unsigned*)(p.ws + WS_PF + ((i * 7 + pcol(s)) * 8 + j) * 128), epoch, __ATOMIC_RELAXED, __HIP_MEMORY_SCOPE_AGENT); };
    w3_round(0, L_RA);
    BARS();
    w3_round(1, L_RB); w3_round(2, L_RC);
    BARS();
    STAMP(18); WAIT_VM(0); STAMP(19); BARS();
    BARS(); STAMP(20);
    BARS();
    w3_round(3, L_RA); w3_round(4, L_RB);
    WAIT_VM(16); BARS(); w3_round(5, L_RC);
    WAIT_VM(16); BARS(); w3_round(6, L_RA);
    WAIT_VM(16); BARS(); w4_step(0, L_RB);
    WAIT_VM(16); BARS(); w4_step(1, L_RC);
    BARS(); w4_step(2, L_RA); w4_step(3, L_RD);
    STAMP(21); WAIT_VM(16); STAMP(22); BARS();
    BARS(); w4_step(4, L_RB);
    BARS(); w4_step(5, L_RC);
    WAIT_VM(32); BARS(); w4_step(6, L_RA);
    WAIT_VM(32); BARS(); arrive(0);
    WAIT_VM(16); BARS(); arrive(1);
    WAIT_VM(0); BARS(); arrive(2);
    BARS(); arrive(3); arrive(4); arrive(5);
    if (b == 0 && lw == 0) {
#pragma unroll
      for (int q = 0; q < 4; ++q) wait_flags(p, true, WS_FLG + (q * 64 + lane) * 128, epoch);
      if (lane == 0) __hip_atomic_store((GAS unsigned*)(p.ws + WS_EP), epoch, __ATOMIC_RELAXED, __HIP_MEMORY_SCOPE_AGENT);
    }
    STAMP(23); ST_FLUSH(1, p.ws, b, w); ST_FLUSHQ(p.ws, b, w);
    return;
  }

  const int hl = w >> 2, a = w & 3, head = 2 * j + hl, tt = 4 * i + a;
  const bool act = w < 7;
  const int wf = act ? w : 6, Fg = 7 * c4 + wf;
  u32x4 qf; u32x2 xr[2]; u32x4 cs1u[2], cs3u[2][2];
  const u32x4 ones = {ONES, ONES, ONES, ONES};
  unsigned act_off = act ? (((tt0 + (g & 1)) * 14 + (Fg >> 1)) * 64 + (2 * (Fg & 1) + (g >> 1)) * 16 + l15) * 16 : OOB;
  const int ka = (2 * w + rot14) % 14, kb14 = (2 * w + 1 + rot14) % 14;
  unsigned pv0 = l16 + ka * KB, pv1 = l16 + kb14 * KB, ov0 = l16 + ((2 * w + rot16) & 15) * KB, ov1 = l16 + ((2 * w + 1 + rot16) & 15) * KB;
  unsigned zv[2];
#pragma unroll
  for (int q = 0; q < 2; ++q) { const int ks = q ? kb14 : ka, f0 = 32 * ks + 8 * g, sh = f0 < 112 ? -1 : f0 < 224 ? 1 : f0 < 336 ? -2 : 2, tz = 16 * (tt0 + 1) + l15 + sh;
    zv[q] = (((tz >> 4) * 14 + ks) * 64 + g * 16 + (tz & 15)) * 16; }
  u32x4 selA, selB;
  { const unsigned v = g == (l15 >> 2) ? ((l15 & 1) ? 0x3F800000u : 0x00003F80u) : 0u, d0 = (l15 & 2) ? 0u : v, d1 = (l15 & 2) ? v : 0u;
    selA = u32x4{d0, d1, 0u, 0u}; selB = u32x4{0u, 0u, d0, d1}; }
  unsigned ooff = ((tt * 16 + head) * 64 + ((g >> 1) + 2 * (g & 1)) * 16 + l15) * 16 + WS_O;
  asm volatile("" : "+v"(act_off), "+v"(pv0), "+v"(pv1), "+v"(ov0), "+v"(ov1), "+v"(zv[0]), "+v"(zv[1]), "+v"(selA), "+v"(selB), "+v"(ooff));
  { const __amdgpu_buffer_rsrc_t rx = mk_rsrc(p.x, S * D * 2);
#pragma unroll
    for (int t = 0; t < 2; ++t) aload64(xr[t], rx, ((16 * (tt0 + t) + l15) * D + 16 * Fg + 4 * g) * 2, 0); }
  BARS();
  aload128_sc1(qf, rws, l16, WS_Q + (head * 128 + tt) * KB);
#pragma unroll
  for (int hf = 0; hf < 2; ++hf) { aload128_sc1(cs1u[hf], rws, (16 * Fg + 4 * g) * 4, WS_CS + hf * 2240 * 4);
#pragma unroll
    for (int f = 0; f < 2; ++f) aload128_sc1(cs3u[hf][f], rws, (448 + 256 * (j < 7 ? j : 6) + 32 * w + 16 * f + 4 * g) * 4, WS_CS + hf * 2240 * 4); }
  WAIT_VM(0); RETIRE(qf); RETIRE(xr[0]); RETIRE(xr[1]); RETIRE(cs1u[0]); RETIRE(cs1u[1]); RETIRE(cs3u[0][0]); RETIRE(cs3u[0][1]); RETIRE(cs3u[1][0]); RETIRE(cs3u[1][1]);
  const f32x4 cs1 = __builtin_bit_cast(f32x4, cs1u[0]) + __builtin_bit_cast(f32x4, cs1u[1]);
  f32x4 cs3[2];
#pragma unroll
  for (int f = 0; f < 2; ++f) cs3[f] = __builtin_bit_cast(f32x4, cs3u[0][f]) + __builtin_bit_cast(f32x4, cs3u[1][f]);
  STAMP(1);
  BARS();
  PROBE(2);

  u32x4 opiece;
  {
    const int par = a & 1, kl0 = hl * 20 + (a & ~1), vl0 = hl * 20 + (a >> 1) * 2;
    f32x4 sc[18];
    const LAS char* kp = lds + L_K + kl0 * KB + l16;
    const f32x4 zero4 = {0.f, 0.f, 0.f, 0.f};
    lds_pipe<18, 8>([&](int n) { return lds128(kp + n * KB); }, [&](int n, u32x4 kf) { sc[n] = mfma16(kf, qf, zero4); }, [](int) {});
    const int dq = l15 - 4 * g;
    const float NEG = -__builtin_inff();
    if (par == 0) {
#pragma unroll
      for (int r = 0; r < 4; ++r) { const bool ge = dq >= r; sc[0][r] = ge ? NEG : sc[0][r]; sc[16][r] = ge ? sc[16][r] : NEG; sc[17][r] = NEG; }
    } else {
#pragma unroll
      for (int r = 0; r < 4; ++r) { const bool ge = dq >= r; sc[1][r] = ge ? NEG : sc[1][r]; sc[17][r] = ge ? sc[17][r] : NEG; sc[0][r] = NEG; }
    }
    const int nneg = 16 - (tt & ~1);
    if (nneg > 0) {
#pragma unroll
      for (int n = 0; n < 16; ++n) if (n < nneg) sc[n] = f32x4{NEG, NEG, NEG, NEG};
    }
    float m = max3(sc[0][0], sc[0][1], sc[0][2]), m2 = max3(sc[0][3], sc[1][0], sc[1][1]);
    m = max3(m, sc[1][2], sc[1][3]);
#pragma unroll
    for (int n = 2; n < 18; ++n) { m = max3(m, sc[n][0], sc[n][1]); m2 = max3(m2, sc[n][2], sc[n][3]); }
    m = gmax(max3(m, m2, m2));
    const float cexp = 0.18898223650461363f * 1.4426950408889634f, mc = -m * cexp;
    STAMP(14);
    BARS();
    STAMP(15);
    f32x4 o0 = zero4, o1 = zero4;
    const LAS char* vp = lds + L_V + vl0 * KB + l16;
    u32x4 v0 = lds128(vp), v1 = lds128(vp + KB);
#pragma unroll
    for (int mm = 0; mm < 9; ++mm) {
      u32x4 n0, n1;
      if (mm < 8) { n0 = lds128(vp + (2 * mm + 2) * KB); n1 = lds128(vp + (2 * mm + 3) * KB); }
      u32x4 pf;
#pragma unroll
      for (int q = 0; q < 4; ++q) { const f32x4& sv = sc[2 * mm + (q >> 1)];
        pf[q] = pk2(__builtin_amdgcn_exp2f(fmaf(sv[2 * (q & 1)], cexp, mc)), __builtin_amdgcn_exp2f(fmaf(sv[2 * (q & 1) + 1], cexp, mc))); }
      o0 = mfma16(v0, pf, o0);
      o1 = mfma16(v1, pf, o1);
      if (mm < 8) { v0 = n0; v1 = n1; }
      SB();
    }
    const float rl = __builtin_amdgcn_rcpf(gsum(g == 3 ? o1[0] : 0.f));
    opiece = pair_tiles(u32x2{pk2(o0[0] * rl, o0[1] * rl), pk2(o0[2] * rl, o0[3] * rl)}, u32x2{pk2(o1[0] * rl, o1[1] * rl), pk2(o1[2] * rl, o1[3] * rl)});
  }
  STAMP(16);
  BARS();
  bst128<16>(opiece, rws, ooff);
  u32x4 wr[16];
#pragma unroll
  for (int n = 0; n < 16; ++n) wr[n] = lds128(lds + (n <= 10 ? L_WOA + n * 7 * KB : L_WOB + (n - 11) * 7 * KB) + wf * KB + l16);
  STAMP(2);
  HWAIT(HX0);

  { u32x4 v[2][2];
    fetch_payload<2, 16>(v, rws, ov0, ov1, WS_O + tt0 * 16 * KB, 16 * KB, lds, lds0, L_O + 2 * w * KB, lane, [] { BARS();   }, p.ws + (40u << 20)); }
  STAMP(3);
  BARS();
  PROBE(3);
  u32x2 hp[2];
  {
    f32x4 acc[2];
#pragma unroll
    for (int t = 0; t < 2; ++t) acc[t] = mfma16(selA, u32x4{xr[t][0], xr[t][1], 0u, 0u}, f32x4{0.f, 0.f, 0.f, 0.f});
    const LAS char* op = lds + L_O + l16;
    lds_pipe<32, 8>([&](int n) { return lds128(op + ((n & 1) * 16 + (n >> 1)) * KB); }, [&](int n, u32x4 f) { acc[n & 1] = mfma16(wr[n >> 1], f, acc[n & 1]); }, [](int) {});
#pragma unroll
    for (int t = 0; t < 2; ++t) hp[t] = u32x2{pk2(acc[t][0], acc[t][1]), pk2(acc[t][2], acc[t][3])};
    bst128<16>(pair_tiles(hp[0], hp[1]), rws, act_off + WS_H);
  }
  STAMP(4);
  HWAIT(HX1);

  LAS float* st4 = (LAS float*)(lds + L_ST4);
  auto stage_weights = [&] { BARS();
#pragma unroll
    for (int n = 0; n < 14; ++n) wr[n] = lds128(lds + L_W + (n * 7 + wf) * KB + l16);
    BARS(); };
  { u32x4 v[2][2] = {};
    if (act) fetch_payload<2, 14>(v, rws, pv0, pv1, WS_H + tt0 * 14 * KB, 14 * KB, lds, lds0, L_H + 2 * w * KB, lane, stage_weights, p.ws + (40u << 20) + 4); else stage_weights();
    row_stats<2>(v, ones, st4, w, l15, g); }
  STAMP(5);
  BARS();
  f32x2 yv[2][2];
  {
    const LAS char* hpz = lds + L_H + l16;
    f32x4 pt[2];
#pragma unroll
    for (int t = 0; t < 2; ++t) pt[t] = *(const LAS f32x4*)(st4 + ((t * 16 + l15) * 8 + 2 * g) * 2);
    f32x4 acc[2] = {};
    lds_pipe<28, 8>([&](int n) { return lds128(hpz + ((n & 1) * 14 + (n >> 1)) * KB); }, [&](int n, u32x4 f) { acc[n & 1] = mfma16(wr[n >> 1], f, acc[n & 1]); }, [](int) {});
    u32x2 zp[2];
#pragma unroll
    for (int t = 0; t < 2; ++t) {
      const float s1 = gsum(pt[t][0] + pt[t][2]), s2 = gsum(pt[t][1] + pt[t][3]);
      const float mean = s1 * (1.f / D), var = fmaxf(s2 * (1.f / D) - mean * mean, 0.f);
      const float r1 = __builtin_amdgcn_rsqf(var + 1e-5f);
      const float r12 = r1 * __builtin_amdgcn_rsqf(var * r1 * r1 + 1e-5f);
      const f32x2 m2 = {mean, mean};
#pragma unroll
      for (int q = 0; q < 2; ++q) {
        yv[t][q] = (f32x2{bflo(hp[t][q]), bfhi(hp[t][q])} - m2) * f32x2{r1, r1};
        zp[t][q] = gelu2((f32x2{acc[t][2 * q], acc[t][2 * q + 1]} - m2 * f32x2{cs1[2 * q], cs1[2 * q + 1]}) * f32x2{r12, r12});
      }
    }
    bst128<16>(pair_tiles(zp[0], zp[1]), rws, act_off + WS_Z + 14 * KB);
  }
  STAMP(6);
  HWAIT(HX2);

  { u32x4 v[2][2];
    if (act) fetch_payload<2, 14>(v, rws, zv[0], zv[1], WS_Z, 14 * KB, lds, lds0, L_Z + 2 * w * KB, lane, stage_weights, p.ws + (40u << 20) + 8); else stage_weights(); }
  STAMP(7);
  BARS();
  {
    f32x4 acc[2] = {};
    const LAS char* zb = lds + L_Z + l16;
    lds_pipe<28, 8>([&](int n) { return lds128(zb + ((n & 1) * 14 + (n >> 1)) * KB); }, [&](int n, u32x4 f) { acc[n & 1] = mfma16(wr[n >> 1], f, acc[n & 1]); }, [](int) {});
    BARS();
    u32x2 xp[2];
#pragma unroll
    for (int t = 0; t < 2; ++t) xp[t] = u32x2{pk2(yv[t][0][0] + acc[t][0], yv[t][0][1] + acc[t][1]), pk2(yv[t][1][0] + acc[t][2], yv[t][1][1] + acc[t][3])};
    bst128<16>(pair_tiles(xp[0], xp[1]), rws, act_off + WS_X1);
  }
  STAMP(8);
  if (j == 7) { ST_FLUSH(1, p.ws, b, w); ST_FLUSHQ(p.ws, b, w); return; }
  HWAIT(HX3);

  LAS float* st6 = (LAS float*)(lds + L_ST6);
  const int q4 = l15 >> 2, pp = lane & 3;
  const LAS char* a3[2];
#pragma unroll
  for (int f = 0; f < 2; ++f) a3[f] = lds + (8 * g + q4) * 512 + (((2 * w + f) ^ (q4 | ((g & 1) << 2))) * 32) + pp * 8;
  auto w3frag = [&](unsigned off, int f) { const u32x2 lo = tr_read(a3[f] + off), hi = tr_read(a3[f] + off + 2048); return u32x4{lo[0], lo[1], hi[0], hi[1]}; };
  u32x4 areg[4][2];
  { u32x4 v[4][2] = {};
    auto w3_regs = [&] { BARS();
#pragma unroll
      for (int n = 0; n < 4; ++n)
#pragma unroll
        for (int f = 0; f < 2; ++f) areg[n][f] = w3frag(n * 16 * KB, f);
      BARS(); };
    if (act) fetch_payload<4, 14>(v, rws, pv0, pv1, WS_X1 + 4 * i * 14 * KB, 14 * KB, lds, lds0, L_X1 + 2 * w * KB, lane, w3_regs, p.ws + (40u << 20) + 12); else w3_regs();
    row_stats<4>(v, ones, st6, w, l15, g); }
  STAMP(9);
  BARS();
  PROBE(4);
  {
    const LAS char* xp = lds + L_X1 + l16;
    f32x4 pt[4];
#pragma unroll
    for (int t = 0; t < 4; ++t) pt[t] = *(const LAS f32x4*)(st6 + ((t * 16 + l15) * 8 + 2 * g) * 2);
    f32x4 acc[2][4] = {};
    u32x4 bfr[2][4], afr[2][2];
    auto load_k = [&](int n) { const unsigned slot = ((n >> 1) % 3 == 2 ? L_RC : (n >> 1) % 3 == 0 ? L_RA : L_RB) + (n & 1) * 16 * KB;
#pragma unroll
      for (int f = 0; f < 2; ++f) afr[n & 1][f] = n < 4 ? areg[n][f] : w3frag(slot, f);
#pragma unroll
      for (int t = 0; t < 4; ++t) bfr[n & 1][t] = lds128(xp + (t * 14 + n) * KB); };
    load_k(0);
    SB();
#pragma unroll
    for (int n = 0; n < 14; ++n) {
      if ((n & 1) && n >= 5) BARS();
      if (n < 13) load_k(n + 1);
#pragma unroll
      for (int t = 0; t < 4; ++t) { acc[0][t] = mfma16(afr[n & 1][0], bfr[n & 1][t], acc[0][t]); acc[1][t] = mfma16(afr[n & 1][1], bfr[n & 1][t], acc[1][t]); }
#pragma unroll
      for (int q = 0; q < 4; ++q) { __builtin_amdgcn_sched_group_barrier(0x8, 1, 0); __builtin_amdgcn_sched_group_barrier(0x100, 2, 0); }
      __builtin_amdgcn_sched_group_barrier(0x8, 4, 0);
      SB();
    }
    STAMP(20);
    float mean[4], gis[4], gc1[4], gc2[4];
#pragma unroll
    for (int t = 0; t < 4; ++t) {
      const float s1 = gsum(pt[t][0] + pt[t][2]), s2 = gsum(pt[t][1] + pt[t][3]);
      mean[t] = s1 * (1.f / D);
      const float ve = fmaxf(s2 * (1.f / D) - mean[t] * mean[t], 0.f) + 1e-5f, rs = __builtin_amdgcn_rsqf(ve);
      gis[t] = ve * rs; gc1[t] = -2.3022082f * rs; gc2[t] = -0.10294324f * rs * rs * rs;
    }
#pragma unroll
    for (int t = 0; t < 4; ++t) {
      u32x4 hv;
#pragma unroll
      for (int q = 0; q < 4; ++q) { const int f = q >> 1, r = 2 * (q & 1);
        hv[q] = gelu2s(f32x2{acc[f][t][r], acc[f][t][r + 1]} - f32x2{mean[t], mean[t]} * f32x2{cs3[f][r], cs3[f][r + 1]}, gc1[t], gc2[t], gis[t]); }
      *(LAS u32x4*)(lds + L_HID + (t * 8 + w) * KB + l16) = hv;
    }
  }
  STAMP(10);
  BARS();
  STAMP(11);
  const int tb = w & 1, ft = w >> 1;
  u32x4 hf[2][8];
#pragma unroll
  for (int ks = 0; ks < 8; ++ks)
#pragma unroll
    for (int t = 0; t < 2; ++t) hf[t][ks] = lds128(lds + L_HID + ((2 * tb + t) * 8 + ks) * KB + l16);
  const unsigned pbase = l16 + WS_P + ((i * 49 + j) * 8 + w) * KB;
  const int Fo = 4 * j + ft;
  u32x2 hres[2], xres[2];
  unsigned ro[2];
#pragma unroll
  for (int t = 0; t < 2; ++t) { ro[t] = (((4 * i + 2 * tb + t) * 14 + (Fo >> 1)) * 64 + (2 * (Fo & 1) + (g >> 1)) * 16 + l15) * 16 + (g & 1) * 8;
    aload64_sc1(hres[t], rws, ro[t], WS_H); aload64_sc1(xres[t], rws, ro[t], WS_X1); }
  f32x4 acc[2] = {};
  {
    constexpr int PD = 4;
    constexpr unsigned slots[7] = {L_RB, L_RC, L_RA, L_RD, L_RB, L_RC, L_RA};
    const LAS char* wp = lds + (4 * g + q4) * 128 + ((ft ^ ((2 * g + (q4 >> 1)) & 3)) * 32) + pp * 8;
    auto w4frag = [&](unsigned off) { const u32x2 lo = tr_read(wp + off), hi = tr_read(wp + off + 2048); return u32x4{lo[0], lo[1], hi[0], hi[1]}; };
    u32x4 buf[PD + 1];
#pragma unroll
    for (int m = 0; m < PD; ++m) buf[m] = w4frag(slots[0] + m * 4096);
    SB();
#pragma unroll
    for (int m = 0; m < 56; ++m) {
      const int s = m >> 3, ks = m & 7;
      if (ks == 8 - PD && s < 6) { if (s >= 3) WAIT_VM(2); BARS(); }
      if (m + PD < 56) buf[(m + PD) % (PD + 1)] = w4frag(slots[(m + PD) >> 3] + ((m + PD) & 7) * 4096);
      SB();
      acc[0] = mfma16(buf[m % (PD + 1)], hf[0][ks], acc[0]); acc[1] = mfma16(buf[m % (PD + 1)], hf[1][ks], acc[1]);
      if (ks == 7 && s < 6) {
        bst128<16>(u32x4{pk2(acc[0][0], acc[0][1]), pk2(acc[0][2], acc[0][3]), pk2(acc[1][0], acc[1][1]), pk2(acc[1][2], acc[1][3])}, rws, pbase + pcol(s) * 56 * KB);
        acc[0] = f32x4{0.f, 0.f, 0.f, 0.f}; acc[1] = acc[0];
      }
      SB();
    }
  }
  STAMP(12);
  PROBE(5);
  {
    const __amdgpu_buffer_rsrc_t rout = mk_rsrc(p.out, S * D * 2);
    u32x4 pv[6];
    const unsigned pin = WS_P + ((i * 7 + j) * 7 * 8 + w) * KB;
    WAIT_VM(0); RETIRE(hres[0]); RETIRE(hres[1]); RETIRE(xres[0]); RETIRE(xres[1]);
    BARS();
    for (unsigned spin = 0; spin < SPIN_MAX; ++spin) {
      const unsigned m = max(max(max(hres[0][0], hres[0][1]), max(xres[0][0], xres[0][1])), max(max(hres[1][0], hres[1][1]), max(xres[1][0], xres[1][1])));
      if (!__any(m == SENT)) break;
#pragma unroll
      for (int t = 0; t < 2; ++t) { aload64_sc1(hres[t], rws, ro[t], WS_H); aload64_sc1(xres[t], rws, ro[t], WS_X1); }
      WAIT_VM(0); RETIRE(hres[0]); RETIRE(hres[1]); RETIRE(xres[0]); RETIRE(xres[1]);
    }
    wait_flags(p, lane < 7 && lane != j, WS_PF + ((i * 7 + j) * 8 + lane) * 128, epoch);
#pragma unroll
    for (int s = 0; s < 6; ++s) { int js = j + 1 + s; js = js >= 7 ? js - 7 : js; aload128_sc1(pv[s], rws, l16, pin + js * 8 * KB); }
    WAIT_VM(0);
#pragma unroll
    for (int s = 0; s < 6; ++s) RETIRE(pv[s]);
    const u32x4 selAB = selA | selB;
#pragma unroll
    for (int s = 0; s < 6; ++s) { acc[0] = mfma16(selA, pv[s], acc[0]); acc[1] = mfma16(selB, pv[s], acc[1]); }
#pragma unroll
    for (int t = 0; t < 2; ++t) {
      acc[t] = mfma16(selAB, u32x4{hres[t][0], hres[t][1], xres[t][0], xres[t][1]}, acc[t]);
      __builtin_amdgcn_raw_buffer_store_b64(u32x2{pk2(acc[t][0], acc[t][1]), pk2(acc[t][2], acc[t][3])}, rout, ((16 * (4 * i + 2 * tb + t) + l15) * D + 16 * Fo + 4 * g) * 2, 0, 0);
    }
  }
  PROBE(6);
  STAMP(13);
  ST_FLUSH(1, p.ws, b, w); ST_FLUSHQ(p.ws, b, w);
}

extern "C" void kernel_launch(void* const* d_in, const int* in_sizes, int n_in, void* d_out, int out_size, void* d_ws, size_t ws_size, hipStream_t stream) {
  Params p;
  p.x = (const u16*)d_in[0]; p.wqkv = (const u16*)d_in[1]; p.wo = (const u16*)d_in[2];
  p.w1 = (const u16*)d_in[4]; p.w2 = (const u16*)d_in[5]; p.w3 = (const u16*)d_in[6]; p.w4 = (const u16*)d_in[7];
  p.out = (u16*)d_out; p.ws = (char*)d_ws;
  (void)hipFuncSetAttribute((const void*)block_fused, hipFuncAttributeMaxDynamicSharedMemorySize, K2_LDS);
  block_fused<<<256, 640, K2_LDS, stream>>>(p);
}
```
